```python
import torch

def _softmax(x, axis=-1):
    return torch.softmax(x, dim=axis)
def _gelu(x):
    return torch.nn.functional.gelu(x, approximate="tanh")
def _silu(x):
    return torch.nn.functional.silu(x)
def _sigmoid(x):
    return torch.sigmoid(x)
def _layernorm(x, axis=-1, eps=1e-5):
    m = x.mean(dim=axis, keepdim=True)
    v = x.var(dim=axis, keepdim=True, unbiased=False)
    return (x - m) / torch.sqrt(v + eps)
def _scatter_add(tgt, idx, src, axis=0):
    return tgt.clone().index_add_(axis, idx, src)
def _segment_sum(src, seg, n):
    out = torch.zeros((n,) + src.shape[1:], dtype=src.dtype)
    return out.index_add_(0, seg, src)
def _pad(x, pw):
    flat = tuple(v for pair in reversed(pw) for v in pair)
    return torch.nn.functional.pad(x, flat)

kernel_name = 'concat_heads__gqa_block_cached__gqa_block_cached__near_ridge'

def setup_inputs():

    _w = (lambda a: (lambda t: t.to(torch.bfloat16) if t.is_floating_point() else t)(torch.as_tensor(a)))
    def _A_setup():
        B, Sq, D = 8, 96, 512
        Hq, Hkv, Sk, Dh = 32, 32, 512, 128
        assert Hq % Hkv == 0, (Hq, Hkv)
        return {
            "x": _w((lambda *s: torch.randn(*s, dtype=torch.bfloat16) * 0.8)(B, Sq, D)),
            "k_cache": _w((lambda *s: torch.randn(*s, dtype=torch.bfloat16) * 0.8)(B, Hkv, Sk, Dh)),
            "v_cache": _w((lambda *s: torch.randn(*s, dtype=torch.bfloat16) * 0.05)(B, Hkv, Sk, Dh)),
            "wq": _w((lambda *s: torch.randn(*s, dtype=torch.bfloat16) * 0.8)(D, Hq * Dh) * (D ** -0.5)),
            "wo": _w((lambda *s: torch.randn(*s, dtype=torch.bfloat16) * 0.05)(Hq * Dh, D) * ((Hq * Dh) ** -0.5)),
        }
    _A_in = _A_setup()

    def _B_setup():
        B, Sq, D = 8, 96, 512
        Hq, Hkv, Sk, Dh = 32, 32, 512, 128
        assert Hq % Hkv == 0, (Hq, Hkv)
        return {
            "x": _w((lambda *s: torch.randn(*s, dtype=torch.bfloat16) * 0.8)(B, Sq, D)),
            "k_cache": _w((lambda *s: torch.randn(*s, dtype=torch.bfloat16) * 0.8)(B, Hkv, Sk, Dh)),
            "v_cache": _w((lambda *s: torch.randn(*s, dtype=torch.bfloat16) * 0.05)(B, Hkv, Sk, Dh)),
            "wq": _w((lambda *s: torch.randn(*s, dtype=torch.bfloat16) * 0.8)(D, Hq * Dh) * (D ** -0.5)),
            "wo": _w((lambda *s: torch.randn(*s, dtype=torch.bfloat16) * 0.05)(Hq * Dh, D) * ((Hq * Dh) ** -0.5)),
        }
    _B_in = _B_setup()

    return {
        "A_x": _A_in["x"],
        "A_k_cache": _A_in["k_cache"],
        "A_v_cache": _A_in["v_cache"],
        "A_wq": _A_in["wq"],
        "A_wo": _A_in["wo"],
        "B_x": _B_in["x"],
        "B_k_cache": _B_in["k_cache"],
        "B_v_cache": _B_in["v_cache"],
        "B_wq": _B_in["wq"],
        "B_wo": _B_in["wo"],
    }

def reference(A_x, A_k_cache, A_v_cache, A_wq, A_wo, B_x, B_k_cache, B_v_cache, B_wq, B_wo):
    def _A_ref(x, k_cache, v_cache, wq, wo):
        B, Sq, D = x.shape
        Hq, Dh = 32, 128
        Hkv = k_cache.shape[1]
        rep = Hq // Hkv
        q = (lambda x, *d: x.permute(*d))((x @ wq).reshape(B, Sq, Hq, Dh), 0, 2, 1, 3)
        k = (lambda x, n, axis: torch.repeat_interleave(x, n, dim=axis))(k_cache, rep, axis=1)
        v = (lambda x, n, axis: torch.repeat_interleave(x, n, dim=axis))(v_cache, rep, axis=1)
        scores = torch.einsum("bhqd,bhkd->bhqk", q, k) * (Dh ** -0.5)
        p = _softmax(scores, axis=-1)
        o = torch.einsum("bhqk,bhkd->bhqd", p, v)
        return (lambda x, *d: x.permute(*d))(o, 0, 2, 1, 3).reshape(B, Sq, Hq * Dh) @ wo

    def _B_ref(x, k_cache, v_cache, wq, wo):
        B, Sq, D = x.shape
        Hq, Dh = 32, 128
        Hkv = k_cache.shape[1]
        rep = Hq // Hkv
        q = (lambda x, *d: x.permute(*d))((x @ wq).reshape(B, Sq, Hq, Dh), 0, 2, 1, 3)
        k = (lambda x, n, axis: torch.repeat_interleave(x, n, dim=axis))(k_cache, rep, axis=1)
        v = (lambda x, n, axis: torch.repeat_interleave(x, n, dim=axis))(v_cache, rep, axis=1)
        scores = torch.einsum("bhqd,bhkd->bhqk", q, k) * (Dh ** -0.5)
        p = _softmax(scores, axis=-1)
        o = torch.einsum("bhqk,bhkd->bhqd", p, v)
        return (lambda x, *d: x.permute(*d))(o, 0, 2, 1, 3).reshape(B, Sq, Hq * Dh) @ wo

    a = _A_ref(A_x, A_k_cache, A_v_cache, A_wq, A_wo)
    b = _B_ref(B_x, B_k_cache, B_v_cache, B_wq, B_wo)
    return torch.concatenate([a, b], axis=-1)
```

```cpp
#include <hip/hip_runtime.h>
#include <hip/hip_bf16.h>
#include <stdint.h>

using bf16 = __hip_bfloat16;
using bf16x8 = __attribute__((ext_vector_type(8))) short;
using s16x4 = __attribute__((ext_vector_type(4))) short;
using f32x16 = __attribute__((ext_vector_type(16))) float;
using f32x4 = __attribute__((ext_vector_type(4))) float;
using u32x4 = __attribute__((ext_vector_type(4))) unsigned;
using u32x2 = __attribute__((ext_vector_type(2))) unsigned;
typedef __attribute__((address_space(3))) s16x4 lds_s16x4;
typedef __attribute__((address_space(3))) bf16x8 lds_bf16x8;

#define DI __device__ __forceinline__
#define MFMA(a, b, c) __builtin_amdgcn_mfma_f32_32x32x16_bf16((a), (b), (c), 0, 0, 0)

constexpr int NB = 8, SQ = 96, DM = 512, NH = 32, SK = 512, DH = 128, HD = NH * DH;
constexpr int SLOT = 16384;
constexpr int NSLOT = 5;

using f32x2 = __attribute__((ext_vector_type(2))) float;
using bf16x2n = __attribute__((ext_vector_type(2))) __bf16;
DI unsigned cvtpk(float lo, float hi) {
  bf16x2n r = __builtin_convertvector((f32x2){lo, hi}, bf16x2n);
  return __builtin_bit_cast(unsigned, r);
}
DI void wait_vm(int) {}
#define WAIT_VM(n) asm volatile("s_waitcnt vmcnt(" #n ")" ::: "memory")
#define BARRIER()                                        \
  do {                                                   \
    asm volatile("s_waitcnt lgkmcnt(0)" ::: "memory");   \
    __builtin_amdgcn_s_barrier();                        \
    asm volatile("" ::: "memory");                       \
  } while (0)

template <int PITCH>
DI void load_slot(const char* gbase, char* lds_slot, int lo0, int lo1, int lo2, int lo3) {
#pragma unroll
  for (int g = 0; g < 4; ++g) {
    __builtin_amdgcn_global_load_lds((const unsigned*)(gbase + lo0 + g * 16 * PITCH), (unsigned*)(lds_slot + (g * 4 + 0) * 1024), 16, 0, 0);
    __builtin_amdgcn_global_load_lds((const unsigned*)(gbase + lo1 + g * 16 * PITCH), (unsigned*)(lds_slot + (g * 4 + 1) * 1024), 16, 0, 0);
    __builtin_amdgcn_global_load_lds((const unsigned*)(gbase + lo2 + g * 16 * PITCH), (unsigned*)(lds_slot + (g * 4 + 2) * 1024), 16, 0, 0);
    __builtin_amdgcn_global_load_lds((const unsigned*)(gbase + lo3 + g * 16 * PITCH), (unsigned*)(lds_slot + (g * 4 + 3) * 1024), 16, 0, 0);
  }
}
DI int lane_src_off(int lane, int i, int pitch) { return (4 * i + (lane >> 4)) * pitch + 16 * ((lane & 15) ^ (((lane >> 4) << 2) | i)); }

DI s16x4 tr_read(const char* lds_base, int off) {
  return __builtin_amdgcn_ds_read_tr16_b64_v4i16((lds_s16x4*)(lds_base + off));
}
DI bf16x8 pack8(s16x4 a, s16x4 b) { return (bf16x8){a[0], a[1], a[2], a[3], b[0], b[1], b[2], b[3]}; }

DI void proj_chunk(f32x16* acc, const char* slot, const bf16x8* xf, int lane) {
  const int h = lane >> 5, blk = (lane >> 4) & 1, q4 = (lane & 15) >> 2, p = lane & 3;
  const int base = 256 * (32 * h + q4) + (q4 << 6) + ((2 * blk + (p >> 1)) << 4) + 8 * (p & 1);
#pragma unroll
  for (int s = 0; s < 4; ++s) {
#pragma unroll
    for (int nb = 0; nb < 4; ++nb) {
      const int x0 = (nb << 6) | (((2 * s) & 3) << 4), x1 = (nb << 6) | (((2 * s + 1) & 3) << 4);
      s16x4 a0 = tr_read(slot, (base ^ x0) + 256 * (8 * s));
      s16x4 a1 = tr_read(slot, (base ^ x1) + 256 * (8 * s + 4));
      acc[nb] = MFMA(pack8(a0, a1), xf[s], acc[nb]);
    }
  }
}

__global__ __launch_bounds__(256, 2) void attn_kernel(const bf16* __restrict__ xA, const bf16* __restrict__ kA, const bf16* __restrict__ vA,
                                                      const bf16* __restrict__ wqA, const bf16* __restrict__ xB, const bf16* __restrict__ kB,
                                                      const bf16* __restrict__ vB, const bf16* __restrict__ wqB, bf16* __restrict__ o_ws) {
  extern __shared__ __attribute__((aligned(16))) char lds[];
  const int bid = blockIdx.x, xcd = bid & 7, jj = bid >> 3, ph = xcd * 8 + (jj >> 3), b = jj & 7, p = ph >> 5, h = ph & 31;
  const int wid = __builtin_amdgcn_readfirstlane(threadIdx.x >> 6), lane = threadIdx.x & 63;
  const bf16* x = p ? xB : xA;
  const bf16* wq = p ? wqB : wqA;
  const size_t kvoff = (size_t)(b * NH + h) * SK * DH;

  if (wid == 3) {
    const char* kg = (const char*)((p ? kB : kA) + kvoff);
    const char* vg = (const char*)((p ? vB : vA) + kvoff);
    const char* wg = (const char*)(wq + h * DH);
    const int k0 = lane_src_off(lane, 0, 256), k1 = lane_src_off(lane, 1, 256), k2 = lane_src_off(lane, 2, 256), k3 = lane_src_off(lane, 3, 256);
    const int w0 = lane_src_off(lane, 0, 8192), w1 = lane_src_off(lane, 1, 8192), w2 = lane_src_off(lane, 2, 8192), w3 = lane_src_off(lane, 3, 8192);
#define LW(c, sl) load_slot<8192>(wg + (size_t)(c) * 64 * 8192, lds + (sl) * SLOT, w0, w1, w2, w3)
#define LK(t, sl) load_slot<256>(kg + (t) * 16384, lds + (sl) * SLOT, k0, k1, k2, k3)
#define LV(t, sl) load_slot<256>(vg + (t) * 16384, lds + (sl) * SLOT, k0, k1, k2, k3)
#define PB() __builtin_amdgcn_s_barrier()
    LW(0, 0); LW(1, 1); LW(2, 2); LK(0, 3); LV(0, 4);
    WAIT_VM(63); PB();
    WAIT_VM(48); PB();
    LW(3, 0); WAIT_VM(48); PB();
    LW(4, 1); WAIT_VM(16); PB();
    LW(5, 2); WAIT_VM(16); PB();
    LW(6, 0); WAIT_VM(16); PB();
    LW(7, 1); WAIT_VM(16); PB();
    LK(1, 2); WAIT_VM(16); PB();
    LV(1, 0); PB();
    LK(2, 1); WAIT_VM(16); PB();
    LV(2, 3); LK(3, 4); WAIT_VM(16); PB();
    LV(3, 2); LK(4, 0); WAIT_VM(16); PB();
    LV(4, 1); LK(5, 3); WAIT_VM(16); PB();
    LV(5, 4); LK(6, 2); WAIT_VM(16); PB();
    LV(6, 0); LK(7, 1); WAIT_VM(16); PB();
    LV(7, 3); WAIT_VM(0); PB();
#undef LW
#undef LK
#undef LV
#undef PB
    return;
  }

  const int r32 = lane & 31, hi = lane >> 5;
  f32x16 acc[4];
#pragma unroll
  for (int i = 0; i < 4; ++i) acc[i] = f32x16{};
  const bf16* xrow = x + (size_t)(b * SQ + wid * 32 + r32) * DM + 32 * hi;
  bf16x8 xf[4], xn[4];
#pragma unroll
  for (int s = 0; s < 4; ++s) xf[s] = *(const bf16x8*)(xrow + 8 * s);
  constexpr int wslot[8] = {0, 1, 2, 0, 1, 2, 0, 1};
#pragma unroll
  for (int c = 0; c < 8; ++c) {
    if (c < 7) {
#pragma unroll
      for (int s = 0; s < 4; ++s) xn[s] = *(const bf16x8*)(xrow + 64 * (c + 1) + 8 * s);
    }
    BARRIER();
    proj_chunk(acc, lds + wslot[c] * SLOT, xf, lane);
#pragma unroll
    for (int s = 0; s < 4; ++s) xf[s] = xn[s];
  }
  bf16x8 qf[8];
#pragma unroll
  for (int s = 0; s < 8; ++s) {
    const f32x16& a = acc[s >> 1];
    const int o8 = (s & 1) * 8;
    unsigned a0 = cvtpk(a[o8 + 0], a[o8 + 1]), a1 = cvtpk(a[o8 + 2], a[o8 + 3]);
    unsigned b0 = cvtpk(a[o8 + 4], a[o8 + 5]), b1 = cvtpk(a[o8 + 6], a[o8 + 7]);
    auto r0 = __builtin_amdgcn_permlane32_swap(a0, b0, false, false);
    auto r1 = __builtin_amdgcn_permlane32_swap(a1, b1, false, false);
    u32x4 w = {r0[0], r1[0], r0[1], r1[1]};
    qf[s] = __builtin_bit_cast(bf16x8, w);
  }
  constexpr float C = 0.088388347648318440f * 1.4426950408889634f;
  float m_run = -1e30f, l_run = 0.f;
#pragma unroll
  for (int i = 0; i < 4; ++i) acc[i] = f32x16{};
  constexpr int kslot[8] = {3, 2, 1, 4, 0, 3, 2, 1};
  constexpr int vslot[8] = {4, 0, 3, 2, 1, 4, 0, 3};
  const int fr = ((r32 & 3) << 2) | ((r32 >> 2) & 3);
  const int kbase = 256 * r32 + ((hi ^ fr) << 4);
  const int blk = (lane >> 4) & 1, q4 = (lane & 15) >> 2, p4 = lane & 3;
  const int vbase = 256 * (4 * hi + q4) + (q4 << 6) + (((2 * blk + (p4 >> 1)) ^ hi) << 4) + 8 * (p4 & 1);
#pragma unroll
  for (int t = 0; t < 8; ++t) {
    BARRIER();
    const char* ks = lds + kslot[t] * SLOT;
    const char* vs = lds + vslot[t] * SLOT;
    f32x16 s0 = f32x16{}, s1 = f32x16{};
#pragma unroll
    for (int s = 0; s < 8; ++s) {
      bf16x8 ka = *(const lds_bf16x8*)(ks + (kbase ^ (s << 5)));
      bf16x8 kb = *(const lds_bf16x8*)(ks + (kbase ^ (s << 5)) + 8192);
      s0 = MFMA(ka, qf[s], s0);
      s1 = MFMA(kb, qf[s], s1);
    }
    float pm = s0[0];
#pragma unroll
    for (int r = 1; r < 16; ++r) pm = fmaxf(pm, s0[r]);
#pragma unroll
    for (int r = 0; r < 16; ++r) pm = fmaxf(pm, s1[r]);
    {
      auto rr = __builtin_amdgcn_permlane32_swap(__float_as_uint(pm), __float_as_uint(pm), false, false);
      pm = fmaxf(__uint_as_float(rr[0]), __uint_as_float(rr[1]));
    }
    const float mn = fmaxf(m_run, pm);
    const float alpha = __builtin_amdgcn_exp2f((m_run - mn) * C);
    m_run = mn;
    const float mnC = -mn * C;
    float ps = 0.f;
#pragma unroll
    for (int r = 0; r < 16; ++r) { s0[r] = __builtin_amdgcn_exp2f(fmaf(s0[r], C, mnC)); ps += s0[r]; }
#pragma unroll
    for (int r = 0; r < 16; ++r) { s1[r] = __builtin_amdgcn_exp2f(fmaf(s1[r], C, mnC)); ps += s1[r]; }
    l_run = l_run * alpha + ps;
#pragma unroll
    for (int i = 0; i < 4; ++i)
#pragma unroll
      for (int r = 0; r < 16; ++r) acc[i][r] *= alpha;
    bf16x8 pf[4];
#pragma unroll
    for (int s = 0; s < 4; ++s) {
      const f32x16& a = (s >> 1) ? s1 : s0;
      const int o8 = (s & 1) * 8;
      u32x4 w = {cvtpk(a[o8 + 0], a[o8 + 1]), cvtpk(a[o8 + 2], a[o8 + 3]), cvtpk(a[o8 + 4], a[o8 + 5]), cvtpk(a[o8 + 6], a[o8 + 7])};
      pf[s] = __builtin_bit_cast(bf16x8, w);
    }
#pragma unroll
    for (int s = 0; s < 4; ++s) {
#pragma unroll
      for (int db = 0; db < 4; ++db) {
        s16x4 a0 = tr_read(vs, (vbase ^ (db << 6)) + 256 * (16 * s));
        s16x4 a1 = tr_read(vs, (vbase ^ (db << 6) ^ (2 << 4)) + 256 * (16 * s + 8));
        acc[db] = MFMA(pack8(a0, a1), pf[s], acc[db]);
      }
    }
  }
  {
    auto rr = __builtin_amdgcn_permlane32_swap(__float_as_uint(l_run), __float_as_uint(l_run), false, false);
    l_run = __uint_as_float(rr[0]) + __uint_as_float(rr[1]);
  }
  const float inv = 1.f / l_run;
  bf16* orow = o_ws + (size_t)((p * NB + b) * SQ + wid * 32 + r32) * HD + h * DH + 4 * hi;
#pragma unroll
  for (int db = 0; db < 4; ++db)
#pragma unroll
    for (int g = 0; g < 4; ++g) {
      u32x2 w = {cvtpk(acc[db][4 * g + 0] * inv, acc[db][4 * g + 1] * inv), cvtpk(acc[db][4 * g + 2] * inv, acc[db][4 * g + 3] * inv)};
      *(u32x2*)(orow + 32 * db + 8 * g) = w;
    }
}

__global__ __launch_bounds__(256, 2) void oproj_kernel(const bf16* __restrict__ o_ws, const bf16* __restrict__ woA, const bf16* __restrict__ woB,
                                                       float* __restrict__ slabs) {
  extern __shared__ __attribute__((aligned(16))) char lds[];
  const int bid = blockIdx.x, xcd = bid & 7, jj = bid >> 3;
  const int pb = xcd * 2 + (jj >> 4), nbk = (jj >> 2) & 3, ks = jj & 3, p = pb >> 3;
  const int wid = __builtin_amdgcn_readfirstlane(threadIdx.x >> 6), lane = threadIdx.x & 63;
  const bf16* wo = p ? woB : woA;
  if (wid == 3) {
    const char* wg = (const char*)(wo + (size_t)ks * 1024 * DM + nbk * 128);
    const int w0 = lane_src_off(lane, 0, 1024), w1 = lane_src_off(lane, 1, 1024), w2 = lane_src_off(lane, 2, 1024), w3 = lane_src_off(lane, 3, 1024);
#define LW(c) load_slot<1024>(wg + (size_t)(c) * 64 * 1024, lds + ((c) % 4) * SLOT, w0, w1, w2, w3)
    LW(0); LW(1); LW(2);
#pragma unroll
    for (int c = 0; c < 16; ++c) {
      if (c < 14) WAIT_VM(32); else if (c == 14) WAIT_VM(16); else WAIT_VM(0);
      __builtin_amdgcn_s_barrier();
      if (c + 3 < 16) LW(c + 3);
    }
#undef LW
    return;
  }
  const int r32 = lane & 31, hi = lane >> 5;
  f32x16 acc[4];
#pragma unroll
  for (int i = 0; i < 4; ++i) acc[i] = f32x16{};
  const bf16* xrow = o_ws + (size_t)(pb * SQ + wid * 32 + r32) * HD + ks * 1024 + 32 * hi;
  bf16x8 xf[4], xn[4];
#pragma unroll
  for (int s = 0; s < 4; ++s) xf[s] = *(const bf16x8*)(xrow + 8 * s);
#pragma unroll
  for (int c = 0; c < 16; ++c) {
    if (c < 15) {
#pragma unroll
      for (int s = 0; s < 4; ++s) xn[s] = *(const bf16x8*)(xrow + 64 * (c + 1) + 8 * s);
    }
    BARRIER();
    proj_chunk(acc, lds + (c % 4) * SLOT, xf, lane);
#pragma unroll
    for (int s = 0; s < 4; ++s) xf[s] = xn[s];
  }
  float* srow = slabs + ((size_t)ks * (2 * NB * SQ) + pb * SQ + wid * 32 + r32) * DM + nbk * 128 + 4 * hi;
#pragma unroll
  for (int nb = 0; nb < 4; ++nb)
#pragma unroll
    for (int g = 0; g < 4; ++g) {
      f32x4 w = {acc[nb][4 * g + 0], acc[nb][4 * g + 1], acc[nb][4 * g + 2], acc[nb][4 * g + 3]};
      *(f32x4*)(srow + 32 * nb + 8 * g) = w;
    }
}

__global__ __launch_bounds__(256) void reduce_kernel(const float* __restrict__ slabs, bf16* __restrict__ out) {
  const int i = blockIdx.x * 256 + threadIdx.x;
  const int row = i >> 7, c4 = (i & 127) * 4;
  constexpr size_t S = (size_t)2 * NB * SQ * DM;
  const float* s = slabs + (size_t)row * DM + c4;
  f32x4 a = *(const f32x4*)s, b = *(const f32x4*)(s + S), c = *(const f32x4*)(s + 2 * S), d = *(const f32x4*)(s + 3 * S);
  a = (a + b) + (c + d);
  const int p = row / (NB * SQ), r = row - p * (NB * SQ);
  u32x2 w = {cvtpk(a[0], a[1]), cvtpk(a[2], a[3])};
  *(u32x2*)(out + (size_t)r * (2 * DM) + p * DM + c4) = w;
}

extern "C" void kernel_launch(void* const* d_in, const int* in_sizes, int n_in, void* d_out, int out_size, void* d_ws, size_t ws_size,
                              hipStream_t stream) {
  const bf16* xA = (const bf16*)d_in[0];
  const bf16* kA = (const bf16*)d_in[1];
  const bf16* vA = (const bf16*)d_in[2];
  const bf16* wqA = (const bf16*)d_in[3];
  const bf16* woA = (const bf16*)d_in[4];
  const bf16* xB = (const bf16*)d_in[5];
  const bf16* kB = (const bf16*)d_in[6];
  const bf16* vB = (const bf16*)d_in[7];
  const bf16* wqB = (const bf16*)d_in[8];
  const bf16* woB = (const bf16*)d_in[9];
  bf16* o_ws = (bf16*)d_ws;
  float* slabs = (float*)((char*)d_ws + (size_t)16 * 1024 * 1024);
  (void)hipFuncSetAttribute((const void*)attn_kernel, hipFuncAttributeMaxDynamicSharedMemorySize, NSLOT * SLOT);
  (void)hipFuncSetAttribute((const void*)oproj_kernel, hipFuncAttributeMaxDynamicSharedMemorySize, 4 * SLOT);
  hipLaunchKernelGGL(attn_kernel, dim3(512), dim3(256), NSLOT * SLOT, stream, xA, kA, vA, wqA, xB, kB, vB, wqB, o_ws);
  hipLaunchKernelGGL(oproj_kernel, dim3(256), dim3(256), 4 * SLOT, stream, (const bf16*)o_ws, woA, woB, slabs);
  hipLaunchKernelGGL(reduce_kernel, dim3(768), dim3(256), 0, stream, (const float*)slabs, (bf16*)d_out);
}
```

```cpp
#include <hip/hip_runtime.h>
#include <hip/hip_bf16.h>
#include <stdint.h>

using bf16 = __hip_bfloat16;
using bf16x8 = __attribute__((ext_vector_type(8))) short;
using s16x4 = __attribute__((ext_vector_type(4))) short;
using f32x16 = __attribute__((ext_vector_type(16))) float;
using f32x4 = __attribute__((ext_vector_type(4))) float;
using u32x4 = __attribute__((ext_vector_type(4))) unsigned;
using u32x2 = __attribute__((ext_vector_type(2))) unsigned;
typedef __attribute__((address_space(3))) s16x4 lds_s16x4;
typedef __attribute__((address_space(3))) bf16x8 lds_bf16x8;

#define DI __device__ __forceinline__
#define MFMA(a, b, c) __builtin_amdgcn_mfma_f32_32x32x16_bf16((a), (b), (c), 0, 0, 0)

constexpr int NB = 8, SQ = 96, DM = 512, NH = 32, SK = 512, DH = 128, HD = NH * DH;
constexpr int SLOT = 16384;
constexpr int NSLOT = 5;

using f32x2 = __attribute__((ext_vector_type(2))) float;
using bf16x2n = __attribute__((ext_vector_type(2))) __bf16;
DI unsigned cvtpk(float lo, float hi) {
  bf16x2n r = __builtin_convertvector((f32x2){lo, hi}, bf16x2n);
  return __builtin_bit_cast(unsigned, r);
}
DI void wait_vm(int) {}
#define WAIT_VM(n) asm volatile("s_waitcnt vmcnt(" #n ")" ::: "memory")
#define SCHED_FENCE() __builtin_amdgcn_sched_barrier(0)
#define BARRIER()                                        \
  do {                                                   \
    SCHED_FENCE();                                       \
    asm volatile("s_waitcnt lgkmcnt(0)" ::: "memory");   \
    __builtin_amdgcn_s_barrier();                        \
    asm volatile("" ::: "memory");                       \
    SCHED_FENCE();                                       \
  } while (0)

template <int PITCH, int AUX>
DI void load_slot(const char* gbase, char* lds_slot, int lo0, int lo1, int lo2, int lo3) {
#pragma unroll
  for (int g = 0; g < 4; ++g) {
    __builtin_amdgcn_global_load_lds((const unsigned*)(gbase + lo0 + g * 16 * PITCH), (unsigned*)(lds_slot + (g * 4 + 0) * 1024), 16, 0, AUX);
    __builtin_amdgcn_global_load_lds((const unsigned*)(gbase + lo1 + g * 16 * PITCH), (unsigned*)(lds_slot + (g * 4 + 1) * 1024), 16, 0, AUX);
    __builtin_amdgcn_global_load_lds((const unsigned*)(gbase + lo2 + g * 16 * PITCH), (unsigned*)(lds_slot + (g * 4 + 2) * 1024), 16, 0, AUX);
    __builtin_amdgcn_global_load_lds((const unsigned*)(gbase + lo3 + g * 16 * PITCH), (unsigned*)(lds_slot + (g * 4 + 3) * 1024), 16, 0, AUX);
  }
}
DI int lane_src_off(int lane, int i, int pitch) { return (4 * i + (lane >> 4)) * pitch + 16 * ((lane & 15) ^ (((lane >> 4) << 2) | i)); }

DI s16x4 tr_read(const char* lds_base, int off) {
  return __builtin_amdgcn_ds_read_tr16_b64_v4i16((lds_s16x4*)(lds_base + off));
}
DI bf16x8 pack8(s16x4 a, s16x4 b) { return (bf16x8){a[0], a[1], a[2], a[3], b[0], b[1], b[2], b[3]}; }

DI void proj_chunk(f32x16* acc, const char* slot, const bf16x8* xf, int lane) {
  const int h = lane >> 5, blk = (lane >> 4) & 1, q4 = (lane & 15) >> 2, p = lane & 3;
  const int base = 256 * (32 * h + q4) + (q4 << 6) + ((2 * blk + (p >> 1)) << 4) + 8 * (p & 1);
  s16x4 a0[4][4], a1[4][4];
  auto rd = [&](int s) {
#pragma unroll
    for (int nb = 0; nb < 4; ++nb) {
      const int x0 = (nb << 6) | (((2 * s) & 3) << 4), x1 = (nb << 6) | (((2 * s + 1) & 3) << 4);
      a0[s][nb] = tr_read(slot, (base ^ x0) + 256 * (8 * s));
      a1[s][nb] = tr_read(slot, (base ^ x1) + 256 * (8 * s + 4));
    }
  };
  rd(0);
#pragma unroll
  for (int s = 0; s < 4; ++s) {
    if (s < 3) rd(s + 1);
    SCHED_FENCE();
#pragma unroll
    for (int nb = 0; nb < 4; ++nb) acc[nb] = MFMA(pack8(a0[s][nb], a1[s][nb]), xf[s], acc[nb]);
    SCHED_FENCE();
  }
}

__global__ __launch_bounds__(256, 2) void attn_kernel(const bf16* __restrict__ xA, const bf16* __restrict__ kA, const bf16* __restrict__ vA,
                                                      const bf16* __restrict__ wqA, const bf16* __restrict__ xB, const bf16* __restrict__ kB,
                                                      const bf16* __restrict__ vB, const bf16* __restrict__ wqB, bf16* __restrict__ o_ws) {
  extern __shared__ __attribute__((aligned(16))) char lds[];
  const int bid = blockIdx.x, xcd = bid & 7, jj = bid >> 3, ph = xcd * 8 + (jj >> 3), b = jj & 7, p = ph >> 5, h = ph & 31;
  const int wid = __builtin_amdgcn_readfirstlane(threadIdx.x >> 6), lane = threadIdx.x & 63;
  const bf16* x = p ? xB : xA;
  const bf16* wq = p ? wqB : wqA;
  const size_t kvoff = (size_t)(b * NH + h) * SK * DH;

  if (wid == 3) {
    const char* kg = (const char*)((p ? kB : kA) + kvoff);
    const char* vg = (const char*)((p ? vB : vA) + kvoff);
    const char* wg = (const char*)(wq + h * DH);
    const int k0 = lane_src_off(lane, 0, 256), k1 = lane_src_off(lane, 1, 256), k2 = lane_src_off(lane, 2, 256), k3 = lane_src_off(lane, 3, 256);
    const int w0 = lane_src_off(lane, 0, 8192), w1 = lane_src_off(lane, 1, 8192), w2 = lane_src_off(lane, 2, 8192), w3 = lane_src_off(lane, 3, 8192);
#define LW(c, sl) load_slot<8192, 0>(wg + (size_t)(c) * 64 * 8192, lds + (sl) * SLOT, w0, w1, w2, w3)
#define LK(t, sl) load_slot<256, 2>(kg + (t) * 16384, lds + (sl) * SLOT, k0, k1, k2, k3)
#define LV(t, sl) load_slot<256, 2>(vg + (t) * 16384, lds + (sl) * SLOT, k0, k1, k2, k3)
#define PB() __builtin_amdgcn_s_barrier()
    LW(0, 0); LW(1, 1); LW(2, 2); LK(0, 3); LV(0, 4);
    WAIT_VM(63); PB();
    WAIT_VM(48); PB();
    LW(3, 0); WAIT_VM(48); PB();
    LW(4, 1); WAIT_VM(16); PB();
    LW(5, 2); WAIT_VM(16); PB();
    LW(6, 0); WAIT_VM(16); PB();
    LW(7, 1); WAIT_VM(16); PB();
    LK(1, 2); WAIT_VM(16); PB();
    LV(1, 0); PB();
    LK(2, 1); PB();
    LV(2, 3); WAIT_VM(48); PB();
    LK(3, 4); WAIT_VM(48); PB();
    LV(3, 2); WAIT_VM(48); PB();
    LK(4, 0); WAIT_VM(48); PB();
    LV(4, 1); WAIT_VM(48); PB();
    LK(5, 3); WAIT_VM(48); PB();
    LV(5, 4); WAIT_VM(48); PB();
    LK(6, 2); WAIT_VM(48); PB();
    LV(6, 0); WAIT_VM(48); PB();
    LK(7, 1); WAIT_VM(48); PB();
    LV(7, 3); WAIT_VM(48); PB();
    WAIT_VM(32); PB();
    WAIT_VM(16); PB();
    WAIT_VM(0); PB();
#undef LW
#undef LK
#undef LV
#undef PB
    return;
  }

  const int r32 = lane & 31, hi = lane >> 5;
  f32x16 acc[4];
#pragma unroll
  for (int i = 0; i < 4; ++i) acc[i] = f32x16{};
  const bf16* xrow = x + (size_t)(b * SQ + wid * 32 + r32) * DM + 32 * hi;
  bf16x8 xf[4], xn[4];
#pragma unroll
  for (int s = 0; s < 4; ++s) xf[s] = *(const bf16x8*)(xrow + 8 * s);
  constexpr int wslot[8] = {0, 1, 2, 0, 1, 2, 0, 1};
#pragma unroll
  for (int c = 0; c < 8; ++c) {
    if (c < 7) {
#pragma unroll
      for (int s = 0; s < 4; ++s) xn[s] = *(const bf16x8*)(xrow + 64 * (c + 1) + 8 * s);
    }
    BARRIER();
    proj_chunk(acc, lds + wslot[c] * SLOT, xf, lane);
#pragma unroll
    for (int s = 0; s < 4; ++s) xf[s] = xn[s];
  }
  bf16x8 qf[8];
#pragma unroll
  for (int s = 0; s < 8; ++s) {
    const f32x16& a = acc[s >> 1];
    const int o8 = (s & 1) * 8;
    unsigned a0 = cvtpk(a[o8 + 0], a[o8 + 1]), a1 = cvtpk(a[o8 + 2], a[o8 + 3]);
    unsigned b0 = cvtpk(a[o8 + 4], a[o8 + 5]), b1 = cvtpk(a[o8 + 6], a[o8 + 7]);
    auto r0 = __builtin_amdgcn_permlane32_swap(a0, b0, false, false);
    auto r1 = __builtin_amdgcn_permlane32_swap(a1, b1, false, false);
    u32x4 w = {r0[0], r1[0], r0[1], r1[1]};
    qf[s] = __builtin_bit_cast(bf16x8, w);
  }
  constexpr float C = 0.088388347648318440f * 1.4426950408889634f;
  float m_run = -1e30f, l_run = 0.f;
#pragma unroll
  for (int i = 0; i < 4; ++i) acc[i] = f32x16{};
  constexpr int kslot[8] = {3, 2, 1, 4, 0, 3, 2, 1};
  constexpr int vslot[8] = {4, 0, 3, 2, 1, 4, 0, 3};
  const int fr = ((r32 & 3) << 2) | ((r32 >> 2) & 3);
  const int kbase = 256 * r32 + ((hi ^ fr) << 4);
  const int blk = (lane >> 4) & 1, q4 = (lane & 15) >> 2, p4 = lane & 3;
  const int vbase = 256 * (4 * hi + q4) + (q4 << 6) + (((2 * blk + (p4 >> 1)) ^ hi) << 4) + 8 * (p4 & 1);
#pragma unroll
  for (int t = 0; t < 8; ++t) {
    BARRIER();
    const char* ks = lds + kslot[t] * SLOT;
    const char* vs = lds + vslot[t] * SLOT;
    f32x16 s0 = f32x16{}, s1 = f32x16{};
    {
      bf16x8 ka[8], kb[8];
      auto rd = [&](int g) {
#pragma unroll
        for (int s = 2 * g; s < 2 * g + 2; ++s) {
          ka[s] = *(const lds_bf16x8*)(ks + (kbase ^ (s << 5)));
          kb[s] = *(const lds_bf16x8*)(ks + (kbase ^ (s << 5)) + 8192);
        }
      };
      rd(0);
#pragma unroll
      for (int g = 0; g < 4; ++g) {
        if (g < 3) rd(g + 1);
        SCHED_FENCE();
#pragma unroll
        for (int s = 2 * g; s < 2 * g + 2; ++s) {
          s0 = MFMA(ka[s], qf[s], s0);
          s1 = MFMA(kb[s], qf[s], s1);
        }
        SCHED_FENCE();
      }
    }
    BARRIER();
    s16x4 a0[4][4], a1[4][4];
    auto rdv = [&](int s) {
#pragma unroll
      for (int db = 0; db < 4; ++db) {
        a0[s][db] = tr_read(vs, (vbase ^ (db << 6)) + 256 * (16 * s));
        a1[s][db] = tr_read(vs, (vbase ^ (db << 6) ^ (2 << 4)) + 256 * (16 * s + 8));
      }
    };
    rdv(0);
    SCHED_FENCE();
    float pm = s0[0];
#pragma unroll
    for (int r = 1; r < 16; ++r) pm = fmaxf(pm, s0[r]);
#pragma unroll
    for (int r = 0; r < 16; ++r) pm = fmaxf(pm, s1[r]);
    {
      auto rr = __builtin_amdgcn_permlane32_swap(__float_as_uint(pm), __float_as_uint(pm), false, false);
      pm = fmaxf(__uint_as_float(rr[0]), __uint_as_float(rr[1]));
    }
    const float mn = fmaxf(m_run, pm);
    const float alpha = __builtin_amdgcn_exp2f((m_run - mn) * C);
    m_run = mn;
    const float mnC = -mn * C;
    float ps = 0.f;
    bf16x8 pf[4];
#pragma unroll
    for (int r = 0; r < 16; ++r) { s0[r] = __builtin_amdgcn_exp2f(fmaf(s0[r], C, mnC)); ps += s0[r]; }
#pragma unroll
    for (int s = 0; s < 2; ++s) {
      const int o8 = s * 8;
      u32x4 w = {cvtpk(s0[o8 + 0], s0[o8 + 1]), cvtpk(s0[o8 + 2], s0[o8 + 3]), cvtpk(s0[o8 + 4], s0[o8 + 5]), cvtpk(s0[o8 + 6], s0[o8 + 7])};
      pf[s] = __builtin_bit_cast(bf16x8, w);
    }
#pragma unroll
    for (int i = 0; i < 4; ++i)
#pragma unroll
      for (int r = 0; r < 16; ++r) acc[i][r] *= alpha;
    rdv(1);
    SCHED_FENCE();
#pragma unroll
    for (int db = 0; db < 4; ++db) acc[db] = MFMA(pack8(a0[0][db], a1[0][db]), pf[0], acc[db]);
#pragma unroll
    for (int r = 0; r < 16; ++r) { s1[r] = __builtin_amdgcn_exp2f(fmaf(s1[r], C, mnC)); ps += s1[r]; }
    l_run = l_run * alpha + ps;
#pragma unroll
    for (int s = 0; s < 2; ++s) {
      const int o8 = s * 8;
      u32x4 w = {cvtpk(s1[o8 + 0], s1[o8 + 1]), cvtpk(s1[o8 + 2], s1[o8 + 3]), cvtpk(s1[o8 + 4], s1[o8 + 5]), cvtpk(s1[o8 + 6], s1[o8 + 7])};
      pf[2 + s] = __builtin_bit_cast(bf16x8, w);
    }
    SCHED_FENCE();
#pragma unroll
    for (int s = 1; s < 4; ++s) {
      if (s < 3) rdv(s + 1);
      SCHED_FENCE();
#pragma unroll
      for (int db = 0; db < 4; ++db) acc[db] = MFMA(pack8(a0[s][db], a1[s][db]), pf[s], acc[db]);
      SCHED_FENCE();
    }
  }
  {
    auto rr = __builtin_amdgcn_permlane32_swap(__float_as_uint(l_run), __float_as_uint(l_run), false, false);
    l_run = __uint_as_float(rr[0]) + __uint_as_float(rr[1]);
  }
  const float inv = 1.f / l_run;
  u32x4* ofr = (u32x4*)o_ws + ((size_t)(((p * NB + b) * 3 + wid) * NH + h) * 8) * 64 + lane;
#pragma unroll
  for (int db = 0; db < 4; ++db)
#pragma unroll
    for (int j = 0; j < 2; ++j) {
      const f32x16& a = acc[db];
      u32x4 w = {cvtpk(a[8 * j + 0] * inv, a[8 * j + 1] * inv), cvtpk(a[8 * j + 2] * inv, a[8 * j + 3] * inv),
                 cvtpk(a[8 * j + 4] * inv, a[8 * j + 5] * inv), cvtpk(a[8 * j + 6] * inv, a[8 * j + 7] * inv)};
      ofr[(db * 2 + j) * 64] = w;
    }
}

constexpr int NSLOT2 = 8;
__global__ __launch_bounds__(256, 1) void oproj_kernel(const bf16* __restrict__ o_ws, const bf16* __restrict__ woA, const bf16* __restrict__ woB,
                                                       float* __restrict__ slabs) {
  extern __shared__ __attribute__((aligned(16))) char lds[];
  const int bid = blockIdx.x, xcd = bid & 7, jj = bid >> 3;
  const int pb = xcd * 2 + (jj >> 4), nbk = (jj >> 2) & 3, ks = jj & 3, p = pb >> 3;
  const int wid = __builtin_amdgcn_readfirstlane(threadIdx.x >> 6), lane = threadIdx.x & 63;
  const bf16* wo = p ? woB : woA;
  if (wid == 3) {
    const char* wg = (const char*)(wo + (size_t)ks * 1024 * DM + nbk * 128);
    const int w0 = lane_src_off(lane, 0, 1024), w1 = lane_src_off(lane, 1, 1024), w2 = lane_src_off(lane, 2, 1024), w3 = lane_src_off(lane, 3, 1024);
#define LW(c) load_slot<1024, 0>(wg + (size_t)(c) * 64 * 1024, lds + ((c) % NSLOT2) * SLOT, w0, w1, w2, w3)
    LW(0); LW(1); LW(2); LW(3);
#pragma unroll
    for (int c = 0; c < 16; ++c) {
      if (c < 13) WAIT_VM(48); else if (c == 13) WAIT_VM(32); else if (c == 14) WAIT_VM(16); else WAIT_VM(0);
      __builtin_amdgcn_s_barrier();
      if (c + 4 < 16) LW(c + 4);
    }
#undef LW
    return;
  }
  const int hi = lane >> 5, blk = (lane >> 4) & 1, q4 = (lane & 15) >> 2, p4 = lane & 3;
  const int vbase = 256 * (4 * hi + q4) + (q4 << 6) + (((2 * blk + (p4 >> 1)) ^ hi) << 4) + 8 * (p4 & 1);
  f32x16 acc[4];
#pragma unroll
  for (int i = 0; i < 4; ++i) acc[i] = f32x16{};
  const bf16x8* ofr = (const bf16x8*)o_ws + ((size_t)((pb * 3 + wid) * NH + ks * 8) * 8) * 64 + lane;
  constexpr int PF = 4;
  bf16x8 xf[PF + 1][4];
#pragma unroll
  for (int c = 0; c < PF; ++c)
#pragma unroll
    for (int s = 0; s < 4; ++s) xf[c][s] = ofr[(c * 4 + s) * 64];
#pragma unroll
  for (int c = 0; c < 16; ++c) {
    if (c + PF < 16) {
#pragma unroll
      for (int s = 0; s < 4; ++s) xf[(c + PF) % (PF + 1)][s] = ofr[((c + PF) * 4 + s) * 64];
    }
    BARRIER();
    const char* ws = lds + (c % NSLOT2) * SLOT;
    s16x4 a0[4][4], a1[4][4];
    auto rd = [&](int s) {
#pragma unroll
      for (int nb = 0; nb < 4; ++nb) {
        a0[s][nb] = tr_read(ws, (vbase ^ (nb << 6)) + 256 * (16 * s));
        a1[s][nb] = tr_read(ws, (vbase ^ (nb << 6) ^ (2 << 4)) + 256 * (16 * s + 8));
      }
    };
    rd(0);
#pragma unroll
    for (int s = 0; s < 4; ++s) {
      if (s < 3) rd(s + 1);
      SCHED_FENCE();
#pragma unroll
      for (int nb = 0; nb < 4; ++nb) acc[nb] = MFMA(pack8(a0[s][nb], a1[s][nb]), xf[c % (PF + 1)][s], acc[nb]);
      SCHED_FENCE();
    }
  }
  f32x4* sl = (f32x4*)slabs + ((size_t)(((pb * 4 + nbk) * 4 + ks) * 3 + wid) * 16) * 64 + lane;
#pragma unroll
  for (int nb = 0; nb < 4; ++nb)
#pragma unroll
    for (int g = 0; g < 4; ++g) {
      f32x4 w = {acc[nb][4 * g + 0], acc[nb][4 * g + 1], acc[nb][4 * g + 2], acc[nb][4 * g + 3]};
      sl[(nb * 4 + g) * 64] = w;
    }
}

__global__ __launch_bounds__(256) void reduce_kernel(const float* __restrict__ slabs, bf16* __restrict__ out) {
  const int lane = threadIdx.x & 63, u = blockIdx.x * 4 + (threadIdx.x >> 6);
  const int j = u & 1, nb = (u >> 1) & 3, tw = u >> 3, wv = tw % 3, tile = tw / 3, pb = tile >> 2, nbk = tile & 3;
  const f32x4* sp = (const f32x4*)slabs + ((size_t)((tile * 4) * 3 + wv) * 16 + nb * 4 + 2 * j) * 64 + lane;
  constexpr size_t KS = (size_t)3 * 16 * 64;
  f32x4 a = (sp[0] + sp[KS]) + (sp[2 * KS] + sp[3 * KS]);
  f32x4 b = (sp[64] + sp[KS + 64]) + (sp[2 * KS + 64] + sp[3 * KS + 64]);
  unsigned a0 = cvtpk(a[0], a[1]), a1 = cvtpk(a[2], a[3]), b0 = cvtpk(b[0], b[1]), b1 = cvtpk(b[2], b[3]);
  auto r0 = __builtin_amdgcn_permlane32_swap(a0, b0, false, false);
  auto r1 = __builtin_amdgcn_permlane32_swap(a1, b1, false, false);
  u32x4 w = {r0[0], r1[0], r0[1], r1[1]};
  const int p = pb >> 3, b_ = pb & 7, q = wv * 32 + (lane & 31), hi = lane >> 5;
  *(u32x4*)(out + (size_t)(b_ * SQ + q) * (2 * DM) + p * DM + nbk * 128 + 32 * nb + 16 * j + 8 * hi) = w;
}

extern "C" void kernel_launch(void* const* d_in, const int* in_sizes, int n_in, void* d_out, int out_size, void* d_ws, size_t ws_size,
                              hipStream_t stream) {
  const bf16* xA = (const bf16*)d_in[0];
  const bf16* kA = (const bf16*)d_in[1];
  const bf16* vA = (const bf16*)d_in[2];
  const bf16* wqA = (const bf16*)d_in[3];
  const bf16* woA = (const bf16*)d_in[4];
  const bf16* xB = (const bf16*)d_in[5];
  const bf16* kB = (const bf16*)d_in[6];
  const bf16* vB = (const bf16*)d_in[7];
  const bf16* wqB = (const bf16*)d_in[8];
  const bf16* woB = (const bf16*)d_in[9];
  bf16* o_ws = (bf16*)d_ws;
  float* slabs = (float*)((char*)d_ws + (size_t)16 * 1024 * 1024);
  (void)hipFuncSetAttribute((const void*)attn_kernel, hipFuncAttributeMaxDynamicSharedMemorySize, NSLOT * SLOT);
  (void)hipFuncSetAttribute((const void*)oproj_kernel, hipFuncAttributeMaxDynamicSharedMemorySize, NSLOT2 * SLOT);
  hipLaunchKernelGGL(attn_kernel, dim3(512), dim3(256), NSLOT * SLOT, stream, xA, kA, vA, wqA, xB, kB, vB, wqB, o_ws);
  hipLaunchKernelGGL(oproj_kernel, dim3(256), dim3(256), NSLOT2 * SLOT, stream, (const bf16*)o_ws, woA, woB, slabs);
  hipLaunchKernelGGL(reduce_kernel, dim3(384), dim3(256), 0, stream, (const float*)slabs, (bf16*)d_out);
}
```

```cpp
#include <hip/hip_runtime.h>
#include <hip/hip_bf16.h>
#include <stdint.h>

using bf16 = __hip_bfloat16;
using bf16x8 = __attribute__((ext_vector_type(8))) short;
using s16x4 = __attribute__((ext_vector_type(4))) short;
using f32x16 = __attribute__((ext_vector_type(16))) float;
using f32x4 = __attribute__((ext_vector_type(4))) float;
using u32x4 = __attribute__((ext_vector_type(4))) unsigned;
using u32x2 = __attribute__((ext_vector_type(2))) unsigned;
typedef __attribute__((address_space(3))) s16x4 lds_s16x4;
typedef __attribute__((address_space(3))) bf16x8 lds_bf16x8;

#define DI __device__ __forceinline__
#define MFMA(a, b, c) __builtin_amdgcn_mfma_f32_32x32x16_bf16((a), (b), (c), 0, 0, 0)

constexpr int NB = 8, SQ = 96, DM = 512, NH = 32, SK = 512, DH = 128, HD = NH * DH;
constexpr int SLOT = 16384;
constexpr int NSLOT = 5;

using f32x2 = __attribute__((ext_vector_type(2))) float;
using bf16x2n = __attribute__((ext_vector_type(2))) __bf16;
DI unsigned cvtpk(float lo, float hi) {
  bf16x2n r = __builtin_convertvector((f32x2){lo, hi}, bf16x2n);
  return __builtin_bit_cast(unsigned, r);
}
DI void wait_vm(int) {}
#define WAIT_VM(n) asm volatile("s_waitcnt vmcnt(" #n ")" ::: "memory")
#define SCHED_FENCE() __builtin_amdgcn_sched_barrier(0)
#define BARRIER()                                        \
  do {                                                   \
    SCHED_FENCE();                                       \
    asm volatile("s_waitcnt lgkmcnt(0)" ::: "memory");   \
    __builtin_amdgcn_s_barrier();                        \
    asm volatile("" ::: "memory");                       \
    SCHED_FENCE();                                       \
  } while (0)

template <int PITCH, int AUX>
DI void load_slot(const char* gbase, char* lds_slot, int lo0, int lo1, int lo2, int lo3) {
#pragma unroll
  for (int g = 0; g < 4; ++g) {
    __builtin_amdgcn_global_load_lds((const unsigned*)(gbase + lo0 + g * 16 * PITCH), (unsigned*)(lds_slot + (g * 4 + 0) * 1024), 16, 0, AUX);
    __builtin_amdgcn_global_load_lds((const unsigned*)(gbase + lo1 + g * 16 * PITCH), (unsigned*)(lds_slot + (g * 4 + 1) * 1024), 16, 0, AUX);
    __builtin_amdgcn_global_load_lds((const unsigned*)(gbase + lo2 + g * 16 * PITCH), (unsigned*)(lds_slot + (g * 4 + 2) * 1024), 16, 0, AUX);
    __builtin_amdgcn_global_load_lds((const unsigned*)(gbase + lo3 + g * 16 * PITCH), (unsigned*)(lds_slot + (g * 4 + 3) * 1024), 16, 0, AUX);
  }
}
DI int lane_src_off(int lane, int i, int pitch) { return (4 * i + (lane >> 4)) * pitch + 16 * ((lane & 15) ^ (((lane >> 4) << 2) | i)); }

DI s16x4 tr_read(const char* lds_base, int off) {
  return __builtin_amdgcn_ds_read_tr16_b64_v4i16((lds_s16x4*)(lds_base + off));
}
DI bf16x8 pack8(s16x4 a, s16x4 b) { return (bf16x8){a[0], a[1], a[2], a[3], b[0], b[1], b[2], b[3]}; }

DI void proj_chunk(f32x16* acc, const char* slot, const bf16x8* xf, int lane) {
  const int h = lane >> 5, blk = (lane >> 4) & 1, q4 = (lane & 15) >> 2, p = lane & 3;
  const int base = 256 * (32 * h + q4) + (q4 << 6) + ((2 * blk + (p >> 1)) << 4) + 8 * (p & 1);
  s16x4 a0[4][4], a1[4][4];
  auto rd = [&](int s) {
#pragma unroll
    for (int nb = 0; nb < 4; ++nb) {
      const int x0 = (nb << 6) | (((2 * s) & 3) << 4), x1 = (nb << 6) | (((2 * s + 1) & 3) << 4);
      a0[s][nb] = tr_read(slot, (base ^ x0) + 256 * (8 * s));
      a1[s][nb] = tr_read(slot, (base ^ x1) + 256 * (8 * s + 4));
    }
  };
  rd(0);
#pragma unroll
  for (int s = 0; s < 4; ++s) {
    if (s < 3) rd(s + 1);
    SCHED_FENCE();
#pragma unroll
    for (int nb = 0; nb < 4; ++nb) acc[nb] = MFMA(pack8(a0[s][nb], a1[s][nb]), xf[s], acc[nb]);
    SCHED_FENCE();
  }
}

__global__ __launch_bounds__(256, 2) void attn_kernel(const bf16* __restrict__ xA, const bf16* __restrict__ kA, const bf16* __restrict__ vA,
                                                      const bf16* __restrict__ wqA, const bf16* __restrict__ xB, const bf16* __restrict__ kB,
                                                      const bf16* __restrict__ vB, const bf16* __restrict__ wqB, bf16* __restrict__ o_ws) {
  extern __shared__ __attribute__((aligned(16))) char lds[];
  const int bid = blockIdx.x, xcd = bid & 7, jj = bid >> 3, ph = xcd * 8 + (jj >> 3), b = jj & 7, p = ph >> 5, h = ph & 31;
  const int wid = __builtin_amdgcn_readfirstlane(threadIdx.x >> 6), lane = threadIdx.x & 63;
  const bf16* x = p ? xB : xA;
  const bf16* wq = p ? wqB : wqA;
  const size_t kvoff = (size_t)(b * NH + h) * SK * DH;

  if (wid == 3) {
    const char* kg = (const char*)((p ? kB : kA) + kvoff);
    const char* vg = (const char*)((p ? vB : vA) + kvoff);
    const char* wg = (const char*)(wq + h * DH);
    const int k0 = lane_src_off(lane, 0, 256), k1 = lane_src_off(lane, 1, 256), k2 = lane_src_off(lane, 2, 256), k3 = lane_src_off(lane, 3, 256);
    const int w0 = lane_src_off(lane, 0, 8192), w1 = lane_src_off(lane, 1, 8192), w2 = lane_src_off(lane, 2, 8192), w3 = lane_src_off(lane, 3, 8192);
#define LW(c, sl) load_slot<8192, 0>(wg + (size_t)(c) * 64 * 8192, lds + (sl) * SLOT, w0, w1, w2, w3)
#define LK(t, sl) load_slot<256, 2>(kg + (t) * 16384, lds + (sl) * SLOT, k0, k1, k2, k3)
#define LV(t, sl) load_slot<256, 2>(vg + (t) * 16384, lds + (sl) * SLOT, k0, k1, k2, k3)
#define PB() __builtin_amdgcn_s_barrier()
    LW(0, 0); LW(1, 1); LW(2, 2); LK(0, 3); LV(0, 4);
    WAIT_VM(63); PB();
    WAIT_VM(48); PB();
    LW(3, 0); WAIT_VM(48); PB();
    LW(4, 1); WAIT_VM(16); PB();
    LW(5, 2); WAIT_VM(16); PB();
    LW(6, 0); WAIT_VM(16); PB();
    LW(7, 1); WAIT_VM(16); PB();
    LK(1, 2); WAIT_VM(16); PB();
    LV(1, 0); PB();
    LK(2, 1); PB();
    LV(2, 3); WAIT_VM(48); PB();
    LK(3, 4); WAIT_VM(48); PB();
    LV(3, 2); WAIT_VM(48); PB();
    LK(4, 0); WAIT_VM(48); PB();
    LV(4, 1); WAIT_VM(48); PB();
    LK(5, 3); WAIT_VM(48); PB();
    LV(5, 4); WAIT_VM(48); PB();
    LK(6, 2); WAIT_VM(48); PB();
    LV(6, 0); WAIT_VM(48); PB();
    LK(7, 1); WAIT_VM(48); PB();
    LV(7, 3); WAIT_VM(48); PB();
    WAIT_VM(32); PB();
    WAIT_VM(16); PB();
    WAIT_VM(0); PB();
#undef LW
#undef LK
#undef LV
#undef PB
    return;
  }

  const int r32 = lane & 31, hi = lane >> 5;
  f32x16 acc[4];
#pragma unroll
  for (int i = 0; i < 4; ++i) acc[i] = f32x16{};
  const bf16* xrow = x + (size_t)(b * SQ + wid * 32 + r32) * DM + 32 * hi;
  bf16x8 xf[4], xn[4];
#pragma unroll
  for (int s = 0; s < 4; ++s) xf[s] = *(const bf16x8*)(xrow + 8 * s);
  constexpr int wslot[8] = {0, 1, 2, 0, 1, 2, 0, 1};
#pragma unroll
  for (int c = 0; c < 8; ++c) {
    if (c < 7) {
#pragma unroll
      for (int s = 0; s < 4; ++s) xn[s] = *(const bf16x8*)(xrow + 64 * (c + 1) + 8 * s);
    }
    BARRIER();
    proj_chunk(acc, lds + wslot[c] * SLOT, xf, lane);
#pragma unroll
    for (int s = 0; s < 4; ++s) xf[s] = xn[s];
  }
  bf16x8 qf[8];
#pragma unroll
  for (int s = 0; s < 8; ++s) {
    const f32x16& a = acc[s >> 1];
    const int o8 = (s & 1) * 8;
    unsigned a0 = cvtpk(a[o8 + 0], a[o8 + 1]), a1 = cvtpk(a[o8 + 2], a[o8 + 3]);
    unsigned b0 = cvtpk(a[o8 + 4], a[o8 + 5]), b1 = cvtpk(a[o8 + 6], a[o8 + 7]);
    auto r0 = __builtin_amdgcn_permlane32_swap(a0, b0, false, false);
    auto r1 = __builtin_amdgcn_permlane32_swap(a1, b1, false, false);
    u32x4 w = {r0[0], r1[0], r0[1], r1[1]};
    qf[s] = __builtin_bit_cast(bf16x8, w);
  }
  constexpr float C = 0.088388347648318440f * 1.4426950408889634f;
  float m_run = -1e30f, l_run = 0.f;
#pragma unroll
  for (int i = 0; i < 4; ++i) acc[i] = f32x16{};
  constexpr int kslot[8] = {3, 2, 1, 4, 0, 3, 2, 1};
  constexpr int vslot[8] = {4, 0, 3, 2, 1, 4, 0, 3};
  const int fr = ((r32 & 3) << 2) | ((r32 >> 2) & 3);
  const int kbase = 256 * r32 + ((hi ^ fr) << 4);
  const int blk = (lane >> 4) & 1, q4 = (lane & 15) >> 2, p4 = lane & 3;
  const int vbase = 256 * (4 * hi + q4) + (q4 << 6) + (((2 * blk + (p4 >> 1)) ^ hi) << 4) + 8 * (p4 & 1);
#pragma unroll
  for (int t = 0; t < 8; ++t) {
    BARRIER();
    const char* ks = lds + kslot[t] * SLOT;
    const char* vs = lds + vslot[t] * SLOT;
    f32x16 s0 = f32x16{}, s1 = f32x16{};
    {
      bf16x8 ka[8], kb[8];
      auto rd = [&](int g) {
#pragma unroll
        for (int s = 2 * g; s < 2 * g + 2; ++s) {
          ka[s] = *(const lds_bf16x8*)(ks + (kbase ^ (s << 5)));
          kb[s] = *(const lds_bf16x8*)(ks + (kbase ^ (s << 5)) + 8192);
        }
      };
      rd(0);
#pragma unroll
      for (int g = 0; g < 4; ++g) {
        if (g < 3) rd(g + 1);
        SCHED_FENCE();
#pragma unroll
        for (int s = 2 * g; s < 2 * g + 2; ++s) {
          s0 = MFMA(ka[s], qf[s], s0);
          s1 = MFMA(kb[s], qf[s], s1);
        }
        SCHED_FENCE();
      }
    }
    BARRIER();
    s16x4 a0[4][4], a1[4][4];
    auto rdv = [&](int s) {
#pragma unroll
      for (int db = 0; db < 4; ++db) {
        a0[s][db] = tr_read(vs, (vbase ^ (db << 6)) + 256 * (16 * s));
        a1[s][db] = tr_read(vs, (vbase ^ (db << 6) ^ (2 << 4)) + 256 * (16 * s + 8));
      }
    };
    rdv(0);
    SCHED_FENCE();
    float pm = s0[0];
#pragma unroll
    for (int r = 1; r < 16; ++r) pm = fmaxf(pm, s0[r]);
#pragma unroll
    for (int r = 0; r < 16; ++r) pm = fmaxf(pm, s1[r]);
    {
      auto rr = __builtin_amdgcn_permlane32_swap(__float_as_uint(pm), __float_as_uint(pm), false, false);
      pm = fmaxf(__uint_as_float(rr[0]), __uint_as_float(rr[1]));
    }
    const float mn = fmaxf(m_run, pm);
    const float alpha = __builtin_amdgcn_exp2f((m_run - mn) * C);
    m_run = mn;
    const float mnC = -mn * C;
    float ps = 0.f;
    bf16x8 pf[4];
#pragma unroll
    for (int r = 0; r < 16; ++r) { s0[r] = __builtin_amdgcn_exp2f(fmaf(s0[r], C, mnC)); ps += s0[r]; }
#pragma unroll
    for (int s = 0; s < 2; ++s) {
      const int o8 = s * 8;
      u32x4 w = {cvtpk(s0[o8 + 0], s0[o8 + 1]), cvtpk(s0[o8 + 2], s0[o8 + 3]), cvtpk(s0[o8 + 4], s0[o8 + 5]), cvtpk(s0[o8 + 6], s0[o8 + 7])};
      pf[s] = __builtin_bit_cast(bf16x8, w);
    }
#pragma unroll
    for (int i = 0; i < 4; ++i)
#pragma unroll
      for (int r = 0; r < 16; ++r) acc[i][r] *= alpha;
    rdv(1);
    SCHED_FENCE();
#pragma unroll
    for (int db = 0; db < 4; ++db) acc[db] = MFMA(pack8(a0[0][db], a1[0][db]), pf[0], acc[db]);
#pragma unroll
    for (int r = 0; r < 16; ++r) { s1[r] = __builtin_amdgcn_exp2f(fmaf(s1[r], C, mnC)); ps += s1[r]; }
    l_run = l_run * alpha + ps;
#pragma unroll
    for (int s = 0; s < 2; ++s) {
      const int o8 = s * 8;
      u32x4 w = {cvtpk(s1[o8 + 0], s1[o8 + 1]), cvtpk(s1[o8 + 2], s1[o8 + 3]), cvtpk(s1[o8 + 4], s1[o8 + 5]), cvtpk(s1[o8 + 6], s1[o8 + 7])};
      pf[2 + s] = __builtin_bit_cast(bf16x8, w);
    }
    SCHED_FENCE();
#pragma unroll
    for (int s = 1; s < 4; ++s) {
      if (s < 3) rdv(s + 1);
      SCHED_FENCE();
#pragma unroll
      for (int db = 0; db < 4; ++db) acc[db] = MFMA(pack8(a0[s][db], a1[s][db]), pf[s], acc[db]);
      SCHED_FENCE();
    }
  }
  {
    auto rr = __builtin_amdgcn_permlane32_swap(__float_as_uint(l_run), __float_as_uint(l_run), false, false);
    l_run = __uint_as_float(rr[0]) + __uint_as_float(rr[1]);
  }
  const float inv = 1.f / l_run;
  u32x4* ofr = (u32x4*)o_ws + ((size_t)(((p * NB + b) * 3 + wid) * NH + h) * 8) * 64 + lane;
#pragma unroll
  for (int db = 0; db < 4; ++db)
#pragma unroll
    for (int j = 0; j < 2; ++j) {
      const f32x16& a = acc[db];
      u32x4 w = {cvtpk(a[8 * j + 0] * inv, a[8 * j + 1] * inv), cvtpk(a[8 * j + 2] * inv, a[8 * j + 3] * inv),
                 cvtpk(a[8 * j + 4] * inv, a[8 * j + 5] * inv), cvtpk(a[8 * j + 6] * inv, a[8 * j + 7] * inv)};
      ofr[(db * 2 + j) * 64] = w;
    }
}

constexpr int NSLOT2 = 8;
__global__ __launch_bounds__(256, 1) void oproj_kernel(const bf16* __restrict__ o_ws, const bf16* __restrict__ woA, const bf16* __restrict__ woB,
                                                       float* __restrict__ slabs) {
  extern __shared__ __attribute__((aligned(16))) char lds[];
  const int bid = blockIdx.x, xcd = bid & 7, jj = bid >> 3;
  const int p = xcd >> 2, ks = xcd & 3, nbk = jj & 3, pb = p * NB + (jj >> 2);
  const int wid = __builtin_amdgcn_readfirstlane(threadIdx.x >> 6), lane = threadIdx.x & 63;
  const bf16* wo = p ? woB : woA;
  if (wid == 3) {
    const char* wg = (const char*)(wo + (size_t)ks * 1024 * DM + nbk * 128);
    const int w0 = lane_src_off(lane, 0, 1024), w1 = lane_src_off(lane, 1, 1024), w2 = lane_src_off(lane, 2, 1024), w3 = lane_src_off(lane, 3, 1024);
#define LW(c) load_slot<1024, 0>(wg + (size_t)(c) * 64 * 1024, lds + ((c) % NSLOT2) * SLOT, w0, w1, w2, w3)
    LW(0); LW(1); LW(2); LW(3);
#pragma unroll
    for (int c = 0; c < 16; ++c) {
      if (c < 13) WAIT_VM(48); else if (c == 13) WAIT_VM(32); else if (c == 14) WAIT_VM(16); else WAIT_VM(0);
      __builtin_amdgcn_s_barrier();
      if (c + 4 < 16) LW(c + 4);
    }
#undef LW
    return;
  }
  const int hi = lane >> 5, blk = (lane >> 4) & 1, q4 = (lane & 15) >> 2, p4 = lane & 3;
  const int vbase = 256 * (4 * hi + q4) + (q4 << 6) + (((2 * blk + (p4 >> 1)) ^ hi) << 4) + 8 * (p4 & 1);
  f32x16 acc[4];
#pragma unroll
  for (int i = 0; i < 4; ++i) acc[i] = f32x16{};
  const bf16x8* ofr = (const bf16x8*)o_ws + ((size_t)((pb * 3 + wid) * NH + ks * 8) * 8) * 64 + lane;
  constexpr int PF = 4;
  bf16x8 xf[PF + 1][4];
#pragma unroll
  for (int c = 0; c < PF; ++c)
#pragma unroll
    for (int s = 0; s < 4; ++s) xf[c][s] = ofr[(c * 4 + s) * 64];
#pragma unroll
  for (int c = 0; c < 16; ++c) {
    if (c + PF < 16) {
#pragma unroll
      for (int s = 0; s < 4; ++s) xf[(c + PF) % (PF + 1)][s] = ofr[((c + PF) * 4 + s) * 64];
    }
    BARRIER();
    const char* ws = lds + (c % NSLOT2) * SLOT;
    s16x4 a0[4][4], a1[4][4];
    auto rd = [&](int s) {
#pragma unroll
      for (int nb = 0; nb < 4; ++nb) {
        a0[s][nb] = tr_read(ws, (vbase ^ (nb << 6)) + 256 * (16 * s));
        a1[s][nb] = tr_read(ws, (vbase ^ (nb << 6) ^ (2 << 4)) + 256 * (16 * s + 8));
      }
    };
    rd(0);
#pragma unroll
    for (int s = 0; s < 4; ++s) {
      if (s < 3) rd(s + 1);
      SCHED_FENCE();
#pragma unroll
      for (int nb = 0; nb < 4; ++nb) acc[nb] = MFMA(pack8(a0[s][nb], a1[s][nb]), xf[c % (PF + 1)][s], acc[nb]);
      SCHED_FENCE();
    }
  }
  f32x4* sl = (f32x4*)slabs + ((size_t)(((pb * 4 + nbk) * 4 + ks) * 3 + wid) * 16) * 64 + lane;
#pragma unroll
  for (int nb = 0; nb < 4; ++nb)
#pragma unroll
    for (int g = 0; g < 4; ++g) {
      f32x4 w = {acc[nb][4 * g + 0], acc[nb][4 * g + 1], acc[nb][4 * g + 2], acc[nb][4 * g + 3]};
      sl[(nb * 4 + g) * 64] = w;
    }
}

__global__ __launch_bounds__(256) void reduce_kernel(const float* __restrict__ slabs, bf16* __restrict__ out) {
  const int lane = threadIdx.x & 63, u = blockIdx.x * 4 + (threadIdx.x >> 6);
  const int j = u & 1, nb = (u >> 1) & 3, tw = u >> 3, wv = tw % 3, tile = tw / 3, pb = tile >> 2, nbk = tile & 3;
  const f32x4* sp = (const f32x4*)slabs + ((size_t)((tile * 4) * 3 + wv) * 16 + nb * 4 + 2 * j) * 64 + lane;
  constexpr size_t KS = (size_t)3 * 16 * 64;
  f32x4 a = (sp[0] + sp[KS]) + (sp[2 * KS] + sp[3 * KS]);
  f32x4 b = (sp[64] + sp[KS + 64]) + (sp[2 * KS + 64] + sp[3 * KS + 64]);
  unsigned a0 = cvtpk(a[0], a[1]), a1 = cvtpk(a[2], a[3]), b0 = cvtpk(b[0], b[1]), b1 = cvtpk(b[2], b[3]);
  auto r0 = __builtin_amdgcn_permlane32_swap(a0, b0, false, false);
  auto r1 = __builtin_amdgcn_permlane32_swap(a1, b1, false, false);
  u32x4 w = {r0[0], r1[0], r0[1], r1[1]};
  const int p = pb >> 3, b_ = pb & 7, q = wv * 32 + (lane & 31), hi = lane >> 5;
  *(u32x4*)(out + (size_t)(b_ * SQ + q) * (2 * DM) + p * DM + nbk * 128 + 32 * nb + 16 * j + 8 * hi) = w;
}

#include <type_traits>
constexpr size_t WS_CNT = 0, WS_O = (size_t)1 << 20, WS_SLAB = (size_t)16 << 20;
constexpr int CTRL = 147456, GLDS = CTRL + 256, UBUF = 40960;
using rsrc_t = __amdgpu_buffer_rsrc_t;
typedef __attribute__((address_space(3))) int lds_int;
typedef __attribute__((address_space(3))) u32x4 lds_u32x4;
#define MF16(a, b, c) __builtin_amdgcn_mfma_f32_16x16x32_bf16((a), (b), (c), 0, 0, 0)

DI int fresh(int v) {
  asm volatile("" : "+v"(v));
  return v;
}
DI int lane_id_v() {
  int l;
  asm volatile("v_mbcnt_lo_u32_b32 %0, -1, 0\nv_mbcnt_hi_u32_b32 %0, -1, %0\n" : "=v"(l));
  return l;
}
DI int lane_id() { return __builtin_amdgcn_mbcnt_hi(~0u, __builtin_amdgcn_mbcnt_lo(~0u, 0u)); }
constexpr float SM_C = 0.088388347648318440f * 1.4426950408889634f;
typedef unsigned long long u64;
extern "C" __device__ u64 amdgcn_dispatch_id(void) __asm("llvm.amdgcn.dispatch.id");
DI void put_tag(u64* flag, u64 tag) {
  if (fresh(lane_id()) == 0) __hip_atomic_store(flag, tag, __ATOMIC_RELAXED, __HIP_MEMORY_SCOPE_AGENT);
}
DI void poll_tags(u64* flags, int mask, u64 tag) {
  const int l8 = fresh(lane_id()) & mask;
  for (int it = 0; it < (1 << 20); ++it) {
    const u64 v = __hip_atomic_load(flags + l8, __ATOMIC_RELAXED, __HIP_MEMORY_SCOPE_AGENT);
    if (__builtin_amdgcn_ballot_w64(v != tag) == 0) break;
    __builtin_amdgcn_s_sleep(1);
  }
  asm volatile("" ::: "memory");
}
DI rsrc_t make_rsrc(const void* p, unsigned bytes) { return __builtin_amdgcn_make_buffer_rsrc((void*)p, 0, bytes, 0x00020000); }
DI u32x4 raw_rsrc(const void* p, unsigned bytes) {
  const unsigned long long a = (unsigned long long)p;
  return (u32x4){(unsigned)__builtin_amdgcn_readfirstlane((unsigned)a), (unsigned)__builtin_amdgcn_readfirstlane((unsigned)(a >> 32)) & 0xffffu, bytes, 0x00020000u};
}
DI u32x4 ld16(rsrc_t r, int voff, int soff) { return __builtin_amdgcn_raw_buffer_load_b128(r, voff, soff, 16); }
DI void st16(u32x4 v, rsrc_t r, int voff, int soff) { __builtin_amdgcn_raw_buffer_store_b128(v, r, voff, soff, 16); }
#define WAITN(n) asm volatile("s_waitcnt vmcnt(%0)" ::"n"(n) : "memory")
#define CBAR() asm volatile("" ::: "memory")
DI bf16x8 lds16(int a) { return *(const lds_bf16x8*)(size_t)(unsigned)a; }
DI bf16x8 ldstr(int a0, int a1) {
  return pack8(__builtin_amdgcn_ds_read_tr16_b64_v4i16((lds_s16x4*)(size_t)(unsigned)a0), __builtin_amdgcn_ds_read_tr16_b64_v4i16((lds_s16x4*)(size_t)(unsigned)a1));
}
DI float xmax4(float v) {
  auto r = __builtin_amdgcn_permlane32_swap(__float_as_uint(v), __float_as_uint(v), false, false);
  v = fmaxf(__uint_as_float(r[0]), __uint_as_float(r[1]));
  r = __builtin_amdgcn_permlane16_swap(__float_as_uint(v), __float_as_uint(v), false, false);
  return fmaxf(__uint_as_float(r[0]), __uint_as_float(r[1]));
}
DI float xsum4(float v) {
  auto r = __builtin_amdgcn_permlane32_swap(__float_as_uint(v), __float_as_uint(v), false, false);
  v = __uint_as_float(r[0]) + __uint_as_float(r[1]);
  r = __builtin_amdgcn_permlane16_swap(__float_as_uint(v), __float_as_uint(v), false, false);
  return __uint_as_float(r[0]) + __uint_as_float(r[1]);
}

constexpr int PD = 3;
template <int N = 16, class R, class M> DI void pipe16(R&& rd, M&& mm) {
  bf16x8 f[N];
#pragma unroll
  for (int i = 0; i < PD; ++i) f[i] = rd(i);
#pragma unroll
  for (int i = 0; i < N; ++i) {
    if (i + PD < N) f[i + PD] = rd(i + PD);
    SCHED_FENCE();
    mm(i, f[i]);
    SCHED_FENCE();
  }
}

__global__ __launch_bounds__(512, 1) void fused_kernel(const bf16* __restrict__ xA, const bf16* __restrict__ kA, const bf16* __restrict__ vA,
                                                       const bf16* __restrict__ wqA, const bf16* __restrict__ woA, const bf16* __restrict__ xB,
                                                       const bf16* __restrict__ kB, const bf16* __restrict__ vB, const bf16* __restrict__ wqB,
                                                       const bf16* __restrict__ woB, char* __restrict__ ws, bf16* __restrict__ out) {
  extern __shared__ __attribute__((aligned(16))) char lds[];
  unsigned long long pc0;
  asm volatile("s_getpc_b64 %0\n.Lpf0:\n" : "=s"(pc0));
  const int bid = blockIdx.x, xcd = bid & 7, jj = bid >> 3, p = xcd >> 2, h = (xcd & 3) * 8 + (jj >> 2), bp = jj & 3;
  const int xg = h >> 2, jn = h & 3, pb = p * 4 + bp;
  const int wid = __builtin_amdgcn_readfirstlane(threadIdx.x >> 6);
  const int lbase = (int)(size_t)(__attribute__((address_space(3))) char*)lds;
#define TS()
  TS();

  const u64 tag = (amdgcn_dispatch_id() * 0x9E3779B97F4A7C15ull + (u64)__builtin_amdgcn_dispatch_ptr()) ^ 0xD1B54A32D192ED03ull;
  const int rot = (int)(((0x76543217654321ull >> (4 * ((bp * 2 + h) % 7))) << 4) & 0xffffffffu), jn2 = 2 * jn;
  const u32x4 r_x = raw_rsrc((const char*)(p ? xB : xA) + (size_t)bp * 192 * 1024, 192 * 1024);
  const int hw = wid - 4;
  const u32x4 r_w = raw_rsrc((const char*)(p ? wqB : wqA) + h * 256, DM * 8192 - h * 256);
  const int lbq = lbase + hw * 4096, lbx = lbase + hw * 6144;
#define LTW(r, k)
  {
    const int l0 = fresh(lane_id()), g0 = l0 >> 4;
    const int w0 = (16 * hw + g0) * 8192 + (((l0 & 15) ^ (g0 << 1)) << 4);
    const int x0 = (48 * hw + (l0 >> 3)) * 1024 + (((l0 & 7) ^ g0) << 4);
    int t0, t1;
    asm volatile(
      "s_cmp_lt_i32 %[hw], 0\ns_cbranch_scc1 .Lsk0\n"
      "s_bfe_u32 %[t0], %[rot], 0x40000\n"
      "s_lshl_b32 %[t1], %[t0], 7\n"
      "s_lshl_b32 %[t0], %[t0], 19\n"
      "s_add_u32 m0, %[lbq], 40960\n"
      "s_nop 0\n"
      "buffer_load_dwordx4 %[w0], %[rw], %[t0] offen nt lds\n"
      "s_add_u32 m0, %[lbq], 41984\n"
      "s_add_u32 %[t0], %[t0], 0x8000\n"
      "buffer_load_dwordx4 %[w0], %[rw], %[t0] offen nt lds\n"
      "s_add_u32 m0, %[lbq], 43008\n"
      "s_add_u32 %[t0], %[t0], 0x8000\n"
      "buffer_load_dwordx4 %[w1], %[rw], %[t0] offen nt lds\n"
      "s_add_u32 m0, %[lbq], 44032\n"
      "s_add_u32 %[t0], %[t0], 0x8000\n"
      "buffer_load_dwordx4 %[w1], %[rw], %[t0] offen nt lds\n"
      "s_add_u32 m0, %[lbx], 57344\n"
      "s_nop 0\n"
      "buffer_load_dwordx4 %[x0], %[rx], %[t1] offen lds\n"
      "s_add_u32 m0, %[lbx], 58368\n"
      "s_add_u32 %[t1], %[t1], 0x2000\n"
      "buffer_load_dwordx4 %[x1], %[rx], %[t1] offen lds\n"
      "s_add_u32 m0, %[lbx], 59392\n"
      "s_add_u32 %[t1], %[t1], 0x2000\n"
      "buffer_load_dwordx4 %[x0], %[rx], %[t1] offen lds\n"
      "s_add_u32 m0, %[lbx], 60416\n"
      "s_add_u32 %[t1], %[t1], 0x2000\n"
      "buffer_load_dwordx4 %[x1], %[rx], %[t1] offen lds\n"
      "s_add_u32 m0, %[lbx], 61440\n"
      "s_add_u32 %[t1], %[t1], 0x2000\n"
      "buffer_load_dwordx4 %[x0], %[rx], %[t1] offen lds\n"
      "s_add_u32 m0, %[lbx], 62464\n"
      "s_add_u32 %[t1], %[t1], 0x2000\n"
      "buffer_load_dwordx4 %[x1], %[rx], %[t1] offen lds\n"
      LTW(251, 0)
      "s_bfe_u32 %[t0], %[rot], 0x40004\n"
      "s_lshl_b32 %[t1], %[t0], 7\n"
      "s_lshl_b32 %[t0], %[t0], 19\n"
      "s_add_u32 m0, %[lbq], 81920\n"
      "s_nop 0\n"
      "buffer_load_dwordx4 %[w0], %[rw], %[t0] offen nt lds\n"
      "s_add_u32 m0, %[lbq], 82944\n"
      "s_add_u32 %[t0], %[t0], 0x8000\n"
      "buffer_load_dwordx4 %[w0], %[rw], %[t0] offen nt lds\n"
      "s_add_u32 m0, %[lbq], 83968\n"
      "s_add_u32 %[t0], %[t0], 0x8000\n"
      "buffer_load_dwordx4 %[w1], %[rw], %[t0] offen nt lds\n"
      "s_add_u32 m0, %[lbq], 84992\n"
      "s_add_u32 %[t0], %[t0], 0x8000\n"
      "buffer_load_dwordx4 %[w1], %[rw], %[t0] offen nt lds\n"
      "s_add_u32 m0, %[lbx], 98304\n"
      "s_nop 0\n"
      "buffer_load_dwordx4 %[x0], %[rx], %[t1] offen lds\n"
      "s_add_u32 m0, %[lbx], 99328\n"
      "s_add_u32 %[t1], %[t1], 0x2000\n"
      "buffer_load_dwordx4 %[x1], %[rx], %[t1] offen lds\n"
      "s_add_u32 m0, %[lbx], 100352\n"
      "s_add_u32 %[t1], %[t1], 0x2000\n"
      "buffer_load_dwordx4 %[x0], %[rx], %[t1] offen lds\n"
      "s_add_u32 m0, %[lbx], 101376\n"
      "s_add_u32 %[t1], %[t1], 0x2000\n"
      "buffer_load_dwordx4 %[x1], %[rx], %[t1] offen lds\n"
      "s_add_u32 m0, %[lbx], 102400\n"
      "s_add_u32 %[t1], %[t1], 0x2000\n"
      "buffer_load_dwordx4 %[x0], %[rx], %[t1] offen lds\n"
      "s_add_u32 m0, %[lbx], 103424\n"
      "s_add_u32 %[t1], %[t1], 0x2000\n"
      "buffer_load_dwordx4 %[x1], %[rx], %[t1] offen lds\n"
      LTW(251, 1)
      ".Lsk0:\n"
      : [t0] "=&s"(t0), [t1] "=&s"(t1)
      : [w0] "v"(w0), [w1] "v"(w0 ^ 128), [x0] "v"(x0), [x1] "v"(x0 ^ 64), [rw] "s"(r_w), [rx] "s"(r_x), [lbq] "s"(lbq), [lbx] "s"(lbx), [rot] "s"(rot), [hw] "s"(hw)
      : "memory", "vcc", "scc", "v251");
    SCHED_FENCE();
  }
  if (wid >= 4) {
    const int l = fresh(lane_id_v()), g = l >> 4, pos = l & 15;
    const int w0 = (16 * hw + g) * 8192 + ((pos ^ (g << 1)) << 4);
    const int x0 = (48 * hw + (l >> 3)) * 1024 + (((l & 7) ^ g) << 4);
    int t0, t1, c0, c1, c2, c3;
    const size_t kvoff = (size_t)(2 * bp * NH + h) * SK * DH * 2;
    const u32x4 r_k = raw_rsrc((const char*)(p ? kB : kA) + kvoff, (NH + 1) * SK * 256), r_v = raw_rsrc((const char*)(p ? vB : vA) + kvoff, (NH + 1) * SK * 256);
    const u32x4 r_o = raw_rsrc((const char*)(p ? woB : woA) + (size_t)512 * xg * 1024 + jn * 256, 512 * 1024 - jn * 256);
    const int k0 = hw * 4096 + g * 256 + ((pos ^ g) << 4);
    const int v0 = hw * 4096 + g * 256 + ((pos ^ (g << 1)) << 4);
    const int o0 = (16 * hw + g) * 1024 + ((pos ^ (g << 1)) << 4);
    const int pa = lbase + hw * 4096 + l * 16, px = lbase + hw * 6144 + l * 16, tv = hw * 4096 + l * 64;
    const int lc = lbase + CTRL, lf = lbase + CTRL + hw;
    const u32x4 r_lt = r_o;
#define LTS(o)
#define LTE
#define LOADER_OPS                                                                                                                          \
      : [t0] "=&s"(t0), [t1] "=&s"(t1), [c0] "=&s"(c0), [c1] "=&s"(c1), [c2] "=&s"(c2), [c3] "=&s"(c3)                                                                       \
      : [k0] "v"(k0), [k1] "v"(k0 ^ 64), [k2] "v"(k0 ^ 128), [k3] "v"(k0 ^ 192), [v0] "v"(v0), [v1] "v"(v0 ^ 128), [w0] "v"(w0),              \
        [w1] "v"(w0 ^ 128), [x0] "v"(x0), [x1] "v"(x0 ^ 64), [o0] "v"(o0), [o1] "v"(o0 ^ 128), [pa] "v"(pa), [px] "v"(px), [tv] "v"(tv), [rw] "s"(r_w),     \
        [rx] "s"(r_x), [rk] "s"(r_k), [rv] "s"(r_v), [ro] "s"(r_o), [rt] "s"(r_lt), [lbq] "s"(lbq), [lbx] "s"(lbx), [lc] "s"(lc),             \
        [lf] "s"(lf), [rot] "s"(rot), [jn2] "s"(jn2) \
      : "memory", "vcc", "scc", "v16", "v17", "v18", "v19", "v20", "v21", "v22", "v23", "v24", "v25", "v26", "v27", "v28", "v29", "v30", "v31", "v32", "v33", "v34", "v35", "v36", "v37", "v38", "v39", "v40", "v41", "v42", "v43", "v44", "v45", "v46", "v47", "v48", "v49", "v50", "v51", "v52", "v53", "v54", "v55", "v56", "v57", "v58", "v59", "v60", "v61", "v62", "v63", "v64", "v65", "v66", "v67", "v68", "v69", "v70", "v71", "v72", "v73", "v74", "v75", "v76", "v77", "v78", "v79", "v80", "v81", "v82", "v83", "v84", "v85", "v86", "v87", "v88", "v89", "v90", "v91", "v92", "v93", "v94", "v95", "v96", "v97", "v98", "v99", "v100", "v101", "v102", "v103", "v104", "v105", "v106", "v107", "v108", "v109", "v110", "v111", "v112", "v113", "v114", "v115", "v116", "v117", "v118", "v119", "v120", "v121", "v122", "v123", "v124", "v125", "v126", "v127", "v128", "v129", "v130", "v131", "v132", "v133", "v134", "v135", "v136", "v137", "v138", "v139", "v140", "v141", "v142", "v143", "v144", "v145", "v146", "v147", "v148", "v149", "v150", "v151", "v152", "v153", "v154", "v155", "v156", "v157", "v158", "v159", "v160", "v161", "v162", "v163", "v164", "v165", "v166", "v167", "v168", "v169", "v170", "v171", "v172", "v173", "v174", "v175", "v176", "v177", "v178", "v179", "v180", "v181", "v182", "v183", "v184", "v185", "v186", "v187", "v188", "v189", "v190", "v191", "v192", "v193", "v194", "v195", "v196", "v197", "v198", "v199", "v200", "v201", "v202", "v203", "v204", "v205", "v206", "v207", "v208", "v209", "v210", "v211", "v212", "v213", "v214", "v215", "v216", "v217", "v218", "v219", "v220", "v221", "v222", "v223", "v224", "v225", "v226", "v227", "v228", "v229", "v230", "v231", "v232", "v233", "v234", "v235", "v236", "v237", "v238", "v239", "v240", "v241", "v242", "v243", "v244", "v245", "v246", "v247", "v248", "v249", "v250", "v251", "v252", "v253", "v254", "v255", "s60", "s61", "s62", "s63", "s64", "s65", "s66", "s67", "s68", "s69", "s70", "s71", "s72", "s73", "s74", "s75", "s76", "s77", "s78", "s79"
    asm volatile(
      "s_mov_b32 %[c0], -1\n"
      "s_mov_b32 %[c1], -1\n"
      "s_mov_b32 %[c2], -1\n"
      "s_mov_b32 %[c3], -1\n"
      "s_barrier\n"
      "s_waitcnt vmcnt(10)\n"
      LTW(250, 0)
      "v_mov_b32 v16, %[lf]\n"
      "v_mov_b32 v17, 1\n"
      "ds_write_b8 v16, v17 offset:64\n"
      LTW(252, 0)
      "s_bfe_u32 %[t0], %[rot], 0x40008\n"
      "s_lshl_b32 %[t1], %[t0], 7\n"
      "s_lshl_b32 %[t0], %[t0], 19\n"
      "s_add_u32 m0, %[lbq], 0\n"
      "s_nop 0\n"
      "buffer_load_dwordx4 %[w0], %[rw], %[t0] offen nt lds\n"
      "s_add_u32 m0, %[lbq], 1024\n"
      "s_add_u32 %[t0], %[t0], 0x8000\n"
      "buffer_load_dwordx4 %[w0], %[rw], %[t0] offen nt lds\n"
      "s_add_u32 m0, %[lbq], 2048\n"
      "s_add_u32 %[t0], %[t0], 0x8000\n"
      "buffer_load_dwordx4 %[w1], %[rw], %[t0] offen nt lds\n"
      "s_add_u32 m0, %[lbq], 3072\n"
      "s_add_u32 %[t0], %[t0], 0x8000\n"
      "buffer_load_dwordx4 %[w1], %[rw], %[t0] offen nt lds\n"
      "s_add_u32 m0, %[lbx], 16384\n"
      "s_nop 0\n"
      "buffer_load_dwordx4 %[x0], %[rx], %[t1] offen lds\n"
      "s_add_u32 m0, %[lbx], 17408\n"
      "s_add_u32 %[t1], %[t1], 0x2000\n"
      "buffer_load_dwordx4 %[x1], %[rx], %[t1] offen lds\n"
      "s_add_u32 m0, %[lbx], 18432\n"
      "s_add_u32 %[t1], %[t1], 0x2000\n"
      "buffer_load_dwordx4 %[x0], %[rx], %[t1] offen lds\n"
      "s_add_u32 m0, %[lbx], 19456\n"
      "s_add_u32 %[t1], %[t1], 0x2000\n"
      "buffer_load_dwordx4 %[x1], %[rx], %[t1] offen lds\n"
      "s_add_u32 m0, %[lbx], 20480\n"
      "s_add_u32 %[t1], %[t1], 0x2000\n"
      "buffer_load_dwordx4 %[x0], %[rx], %[t1] offen lds\n"
      "s_add_u32 m0, %[lbx], 21504\n"
      "s_add_u32 %[t1], %[t1], 0x2000\n"
      "buffer_load_dwordx4 %[x1], %[rx], %[t1] offen lds\n"
      LTW(251, 2)
      "s_waitcnt vmcnt(10)\n"
      LTW(250, 1)
      "v_mov_b32 v16, %[lf]\n"
      "v_mov_b32 v17, 1\n"
      "ds_write_b8 v16, v17 offset:68\n"
      LTW(252, 1)
      "s_bfe_u32 %[t0], %[rot], 0x4000c\n"
      "s_lshl_b32 %[t1], %[t0], 7\n"
      "s_lshl_b32 %[t0], %[t0], 19\n"
      "buffer_load_dwordx4 v[24:27], %[w0], %[rw], %[t0] offen nt\n"
      "s_add_u32 %[t0], %[t0], 0x8000\n"
      "buffer_load_dwordx4 v[28:31], %[w0], %[rw], %[t0] offen nt\n"
      "s_add_u32 %[t0], %[t0], 0x8000\n"
      "buffer_load_dwordx4 v[32:35], %[w1], %[rw], %[t0] offen nt\n"
      "s_add_u32 %[t0], %[t0], 0x8000\n"
      "buffer_load_dwordx4 v[36:39], %[w1], %[rw], %[t0] offen nt\n"
      "buffer_load_dwordx4 v[40:43], %[x0], %[rx], %[t1] offen\n"
      "s_add_u32 %[t1], %[t1], 0x2000\n"
      "buffer_load_dwordx4 v[44:47], %[x1], %[rx], %[t1] offen\n"
      "s_add_u32 %[t1], %[t1], 0x2000\n"
      "buffer_load_dwordx4 v[48:51], %[x0], %[rx], %[t1] offen\n"
      "s_add_u32 %[t1], %[t1], 0x2000\n"
      "buffer_load_dwordx4 v[52:55], %[x1], %[rx], %[t1] offen\n"
      "s_add_u32 %[t1], %[t1], 0x2000\n"
      "buffer_load_dwordx4 v[56:59], %[x0], %[rx], %[t1] offen\n"
      "s_add_u32 %[t1], %[t1], 0x2000\n"
      "buffer_load_dwordx4 v[60:63], %[x1], %[rx], %[t1] offen\n"
      LTW(251, 3)
      "s_bfe_u32 %[t0], %[rot], 0x40010\n"
      "s_lshl_b32 %[t1], %[t0], 7\n"
      "s_lshl_b32 %[t0], %[t0], 19\n"
      "buffer_load_dwordx4 v[64:67], %[w0], %[rw], %[t0] offen nt\n"
      "s_add_u32 %[t0], %[t0], 0x8000\n"
      "buffer_load_dwordx4 v[68:71], %[w0], %[rw], %[t0] offen nt\n"
      "s_add_u32 %[t0], %[t0], 0x8000\n"
      "buffer_load_dwordx4 v[72:75], %[w1], %[rw], %[t0] offen nt\n"
      "s_add_u32 %[t0], %[t0], 0x8000\n"
      "buffer_load_dwordx4 v[76:79], %[w1], %[rw], %[t0] offen nt\n"
      "buffer_load_dwordx4 v[80:83], %[x0], %[rx], %[t1] offen\n"
      "s_add_u32 %[t1], %[t1], 0x2000\n"
      "buffer_load_dwordx4 v[84:87], %[x1], %[rx], %[t1] offen\n"
      "s_add_u32 %[t1], %[t1], 0x2000\n"
      "buffer_load_dwordx4 v[88:91], %[x0], %[rx], %[t1] offen\n"
      "s_add_u32 %[t1], %[t1], 0x2000\n"
      "buffer_load_dwordx4 v[92:95], %[x1], %[rx], %[t1] offen\n"
      "s_add_u32 %[t1], %[t1], 0x2000\n"
      "buffer_load_dwordx4 v[96:99], %[x0], %[rx], %[t1] offen\n"
      "s_add_u32 %[t1], %[t1], 0x2000\n"
      "buffer_load_dwordx4 v[100:103], %[x1], %[rx], %[t1] offen\n"
      LTW(251, 4)
      "s_waitcnt vmcnt(20)\n"
      LTW(250, 2)
      "v_mov_b32 v16, %[lf]\n"
      "v_mov_b32 v17, 1\n"
      "ds_write_b8 v16, v17 offset:72\n"
      LTW(252, 2)
      "s_bfe_u32 %[t0], %[rot], 0x40014\n"
      "s_lshl_b32 %[t1], %[t0], 7\n"
      "s_lshl_b32 %[t0], %[t0], 19\n"
      "buffer_load_dwordx4 v[104:107], %[w0], %[rw], %[t0] offen nt\n"
      "s_add_u32 %[t0], %[t0], 0x8000\n"
      "buffer_load_dwordx4 v[108:111], %[w0], %[rw], %[t0] offen nt\n"
      "s_add_u32 %[t0], %[t0], 0x8000\n"
      "buffer_load_dwordx4 v[112:115], %[w1], %[rw], %[t0] offen nt\n"
      "s_add_u32 %[t0], %[t0], 0x8000\n"
      "buffer_load_dwordx4 v[116:119], %[w1], %[rw], %[t0] offen nt\n"
      "buffer_load_dwordx4 v[120:123], %[x0], %[rx], %[t1] offen\n"
      "s_add_u32 %[t1], %[t1], 0x2000\n"
      "buffer_load_dwordx4 v[124:127], %[x1], %[rx], %[t1] offen\n"
      "s_add_u32 %[t1], %[t1], 0x2000\n"
      "buffer_load_dwordx4 v[128:131], %[x0], %[rx], %[t1] offen\n"
      "s_add_u32 %[t1], %[t1], 0x2000\n"
      "buffer_load_dwordx4 v[132:135], %[x1], %[rx], %[t1] offen\n"
      "s_add_u32 %[t1], %[t1], 0x2000\n"
      "buffer_load_dwordx4 v[136:139], %[x0], %[rx], %[t1] offen\n"
      "s_add_u32 %[t1], %[t1], 0x2000\n"
      "buffer_load_dwordx4 v[140:143], %[x1], %[rx], %[t1] offen\n"
      LTW(251, 5)
      "s_bfe_u32 %[t0], %[rot], 0x40018\n"
      "s_lshl_b32 %[t1], %[t0], 7\n"
      "s_lshl_b32 %[t0], %[t0], 19\n"
      "buffer_load_dwordx4 v[144:147], %[w0], %[rw], %[t0] offen nt\n"
      "s_add_u32 %[t0], %[t0], 0x8000\n"
      "buffer_load_dwordx4 v[148:151], %[w0], %[rw], %[t0] offen nt\n"
      "s_add_u32 %[t0], %[t0], 0x8000\n"
      "buffer_load_dwordx4 v[152:155], %[w1], %[rw], %[t0] offen nt\n"
      "s_add_u32 %[t0], %[t0], 0x8000\n"
      "buffer_load_dwordx4 v[156:159], %[w1], %[rw], %[t0] offen nt\n"
      "buffer_load_dwordx4 v[160:163], %[x0], %[rx], %[t1] offen\n"
      "s_add_u32 %[t1], %[t1], 0x2000\n"
      "buffer_load_dwordx4 v[164:167], %[x1], %[rx], %[t1] offen\n"
      "s_add_u32 %[t1], %[t1], 0x2000\n"
      "buffer_load_dwordx4 v[168:171], %[x0], %[rx], %[t1] offen\n"
      "s_add_u32 %[t1], %[t1], 0x2000\n"
      "buffer_load_dwordx4 v[172:175], %[x1], %[rx], %[t1] offen\n"
      "s_add_u32 %[t1], %[t1], 0x2000\n"
      "buffer_load_dwordx4 v[176:179], %[x0], %[rx], %[t1] offen\n"
      "s_add_u32 %[t1], %[t1], 0x2000\n"
      "buffer_load_dwordx4 v[180:183], %[x1], %[rx], %[t1] offen\n"
      LTW(251, 6)
      "s_waitcnt vmcnt(30)\n"
      LTW(250, 3)
      "s_cmp_ge_i32 %[c0], 0\n"
      "s_cbranch_scc1 .LgA_0_d\n"
      "v_mov_b32 v16, %[lc]\n"
      "s_mov_b32 %[t1], 0x100000\n"
      ".LgA_0_l:\n"
      "ds_read_b128 v[20:23], v16 offset:16\n"
      "s_waitcnt lgkmcnt(0)\n"
      "v_min_i32 v20, v20, v21\n"
      "v_min_i32 v22, v22, v23\n"
      "s_nop 1\n"
      "v_readfirstlane_b32 %[c0], v20\n"
      "v_readfirstlane_b32 %[c1], v22\n"
      "s_cmp_ge_i32 %[c0], 0\n"
      "s_cbranch_scc1 .LgA_0_d\n"
      "s_sleep 1\n"
      "s_sub_u32 %[t1], %[t1], 1\n"
      "s_cbranch_scc0 .LgA_0_l\n"
      ".LgA_0_d:\n"
      "s_cmp_ge_i32 %[c1], 0\n"
      "s_cbranch_scc1 .LgA_1_d\n"
      "v_mov_b32 v16, %[lc]\n"
      "s_mov_b32 %[t1], 0x100000\n"
      ".LgA_1_l:\n"
      "ds_read_b128 v[20:23], v16 offset:16\n"
      "s_waitcnt lgkmcnt(0)\n"
      "v_min_i32 v20, v20, v21\n"
      "v_min_i32 v22, v22, v23\n"
      "s_nop 1\n"
      "v_readfirstlane_b32 %[c0], v20\n"
      "v_readfirstlane_b32 %[c1], v22\n"
      "s_cmp_ge_i32 %[c1], 0\n"
      "s_cbranch_scc1 .LgA_1_d\n"
      "s_sleep 1\n"
      "s_sub_u32 %[t1], %[t1], 1\n"
      "s_cbranch_scc0 .LgA_1_l\n"
      ".LgA_1_d:\n"
      "v_add_u32 v17, 40960, %[pa]\n"
      "v_add_u32 v18, 57344, %[px]\n"
      "ds_write_b128 v17, v[24:27] offset:0\n"
      "ds_write_b128 v17, v[28:31] offset:1024\n"
      "ds_write_b128 v17, v[32:35] offset:2048\n"
      "ds_write_b128 v17, v[36:39] offset:3072\n"
      "ds_write_b128 v18, v[40:43] offset:0\n"
      "ds_write_b128 v18, v[44:47] offset:1024\n"
      "ds_write_b128 v18, v[48:51] offset:2048\n"
      "ds_write_b128 v18, v[52:55] offset:3072\n"
      "ds_write_b128 v18, v[56:59] offset:4096\n"
      "ds_write_b128 v18, v[60:63] offset:5120\n"
      "s_waitcnt lgkmcnt(0)\n"
      "v_mov_b32 v16, %[lf]\n"
      "v_mov_b32 v17, 1\n"
      "ds_write_b8 v16, v17 offset:76\n"
      LTW(252, 3)
      "s_bfe_u32 %[t0], %[rot], 0x4001c\n"
      "s_lshl_b32 %[t1], %[t0], 7\n"
      "s_lshl_b32 %[t0], %[t0], 19\n"
      "buffer_load_dwordx4 v[24:27], %[w0], %[rw], %[t0] offen nt\n"
      "s_add_u32 %[t0], %[t0], 0x8000\n"
      "buffer_load_dwordx4 v[28:31], %[w0], %[rw], %[t0] offen nt\n"
      "s_add_u32 %[t0], %[t0], 0x8000\n"
      "buffer_load_dwordx4 v[32:35], %[w1], %[rw], %[t0] offen nt\n"
      "s_add_u32 %[t0], %[t0], 0x8000\n"
      "buffer_load_dwordx4 v[36:39], %[w1], %[rw], %[t0] offen nt\n"
      "buffer_load_dwordx4 v[40:43], %[x0], %[rx], %[t1] offen\n"
      "s_add_u32 %[t1], %[t1], 0x2000\n"
      "buffer_load_dwordx4 v[44:47], %[x1], %[rx], %[t1] offen\n"
      "s_add_u32 %[t1], %[t1], 0x2000\n"
      "buffer_load_dwordx4 v[48:51], %[x0], %[rx], %[t1] offen\n"
      "s_add_u32 %[t1], %[t1], 0x2000\n"
      "buffer_load_dwordx4 v[52:55], %[x1], %[rx], %[t1] offen\n"
      "s_add_u32 %[t1], %[t1], 0x2000\n"
      "buffer_load_dwordx4 v[56:59], %[x0], %[rx], %[t1] offen\n"
      "s_add_u32 %[t1], %[t1], 0x2000\n"
      "buffer_load_dwordx4 v[60:63], %[x1], %[rx], %[t1] offen\n"
      LTW(251, 7)
      "s_add_u32 m0, %[lbq], 131072\n"
      "s_mov_b32 %[t0], 0\n"
      "buffer_load_dwordx4 %[k0], %[rk], %[t0] offen nt lds\n"
      "buffer_load_dwordx4 %[k1], %[rk], %[t0] offen offset:1024 nt lds\n"
      "buffer_load_dwordx4 %[k2], %[rk], %[t0] offen offset:2048 nt lds\n"
      "buffer_load_dwordx4 %[k3], %[rk], %[t0] offen offset:3072 nt lds\n"
      LTW(251, 8)
      "s_waitcnt vmcnt(34)\n"
      LTW(250, 4)
      "s_cmp_ge_i32 %[c0], 1\n"
      "s_cbranch_scc1 .LgA_2_d\n"
      "v_mov_b32 v16, %[lc]\n"
      "s_mov_b32 %[t1], 0x100000\n"
      ".LgA_2_l:\n"
      "ds_read_b128 v[20:23], v16 offset:16\n"
      "s_waitcnt lgkmcnt(0)\n"
      "v_min_i32 v20, v20, v21\n"
      "v_min_i32 v22, v22, v23\n"
      "s_nop 1\n"
      "v_readfirstlane_b32 %[c0], v20\n"
      "v_readfirstlane_b32 %[c1], v22\n"
      "s_cmp_ge_i32 %[c0], 1\n"
      "s_cbranch_scc1 .LgA_2_d\n"
      "s_sleep 1\n"
      "s_sub_u32 %[t1], %[t1], 1\n"
      "s_cbranch_scc0 .LgA_2_l\n"
      ".LgA_2_d:\n"
      "s_cmp_ge_i32 %[c1], 1\n"
      "s_cbranch_scc1 .LgA_3_d\n"
      "v_mov_b32 v16, %[lc]\n"
      "s_mov_b32 %[t1], 0x100000\n"
      ".LgA_3_l:\n"
      "ds_read_b128 v[20:23], v16 offset:16\n"
      "s_waitcnt lgkmcnt(0)\n"
      "v_min_i32 v20, v20, v21\n"
      "v_min_i32 v22, v22, v23\n"
      "s_nop 1\n"
      "v_readfirstlane_b32 %[c0], v20\n"
      "v_readfirstlane_b32 %[c1], v22\n"
      "s_cmp_ge_i32 %[c1], 1\n"
      "s_cbranch_scc1 .LgA_3_d\n"
      "s_sleep 1\n"
      "s_sub_u32 %[t1], %[t1], 1\n"
      "s_cbranch_scc0 .LgA_3_l\n"
      ".LgA_3_d:\n"
      "v_add_u32 v17, 81920, %[pa]\n"
      "v_add_u32 v18, 98304, %[px]\n"
      "ds_write_b128 v17, v[64:67] offset:0\n"
      "ds_write_b128 v17, v[68:71] offset:1024\n"
      "ds_write_b128 v17, v[72:75] offset:2048\n"
      "ds_write_b128 v17, v[76:79] offset:3072\n"
      "ds_write_b128 v18, v[80:83] offset:0\n"
      "ds_write_b128 v18, v[84:87] offset:1024\n"
      "ds_write_b128 v18, v[88:91] offset:2048\n"
      "ds_write_b128 v18, v[92:95] offset:3072\n"
      "ds_write_b128 v18, v[96:99] offset:4096\n"
      "ds_write_b128 v18, v[100:103] offset:5120\n"
      "s_waitcnt lgkmcnt(0)\n"
      "v_mov_b32 v16, %[lf]\n"
      "v_mov_b32 v17, 1\n"
      "ds_write_b8 v16, v17 offset:80\n"
      LTW(252, 4)
      "s_mov_b32 %[t0], 4194304\n"
      "buffer_load_dwordx4 v[64:67], %[k0], %[rk], %[t0] offen nt\n"
      "buffer_load_dwordx4 v[68:71], %[k1], %[rk], %[t0] offen offset:1024 nt\n"
      "buffer_load_dwordx4 v[72:75], %[k2], %[rk], %[t0] offen offset:2048 nt\n"
      "buffer_load_dwordx4 v[76:79], %[k3], %[rk], %[t0] offen offset:3072 nt\n"
      LTW(251, 9)
      "s_mov_b32 %[t0], 0\n"
      "buffer_load_dwordx4 v[80:83], %[v0], %[rv], %[t0] offen nt\n"
      "buffer_load_dwordx4 v[84:87], %[v1], %[rv], %[t0] offen offset:1024 nt\n"
      "buffer_load_dwordx4 v[88:91], %[v0], %[rv], %[t0] offen offset:2048 nt\n"
      "buffer_load_dwordx4 v[92:95], %[v1], %[rv], %[t0] offen offset:3072 nt\n"
      LTW(251, 10)
      "s_waitcnt vmcnt(32)\n"
      LTW(250, 5)
      "s_cmp_ge_i32 %[c0], 2\n"
      "s_cbranch_scc1 .LgA_4_d\n"
      "v_mov_b32 v16, %[lc]\n"
      "s_mov_b32 %[t1], 0x100000\n"
      ".LgA_4_l:\n"
      "ds_read_b128 v[20:23], v16 offset:16\n"
      "s_waitcnt lgkmcnt(0)\n"
      "v_min_i32 v20, v20, v21\n"
      "v_min_i32 v22, v22, v23\n"
      "s_nop 1\n"
      "v_readfirstlane_b32 %[c0], v20\n"
      "v_readfirstlane_b32 %[c1], v22\n"
      "s_cmp_ge_i32 %[c0], 2\n"
      "s_cbranch_scc1 .LgA_4_d\n"
      "s_sleep 1\n"
      "s_sub_u32 %[t1], %[t1], 1\n"
      "s_cbranch_scc0 .LgA_4_l\n"
      ".LgA_4_d:\n"
      "s_cmp_ge_i32 %[c1], 2\n"
      "s_cbranch_scc1 .LgA_5_d\n"
      "v_mov_b32 v16, %[lc]\n"
      "s_mov_b32 %[t1], 0x100000\n"
      ".LgA_5_l:\n"
      "ds_read_b128 v[20:23], v16 offset:16\n"
      "s_waitcnt lgkmcnt(0)\n"
      "v_min_i32 v20, v20, v21\n"
      "v_min_i32 v22, v22, v23\n"
      "s_nop 1\n"
      "v_readfirstlane_b32 %[c0], v20\n"
      "v_readfirstlane_b32 %[c1], v22\n"
      "s_cmp_ge_i32 %[c1], 2\n"
      "s_cbranch_scc1 .LgA_5_d\n"
      "s_sleep 1\n"
      "s_sub_u32 %[t1], %[t1], 1\n"
      "s_cbranch_scc0 .LgA_5_l\n"
      ".LgA_5_d:\n"
      "v_add_u32 v17, 0, %[pa]\n"
      "v_add_u32 v18, 16384, %[px]\n"
      "ds_write_b128 v17, v[104:107] offset:0\n"
      "ds_write_b128 v17, v[108:111] offset:1024\n"
      "ds_write_b128 v17, v[112:115] offset:2048\n"
      "ds_write_b128 v17, v[116:119] offset:3072\n"
      "ds_write_b128 v18, v[120:123] offset:0\n"
      "ds_write_b128 v18, v[124:127] offset:1024\n"
      "ds_write_b128 v18, v[128:131] offset:2048\n"
      "ds_write_b128 v18, v[132:135] offset:3072\n"
      "ds_write_b128 v18, v[136:139] offset:4096\n"
      "ds_write_b128 v18, v[140:143] offset:5120\n"
      "s_waitcnt lgkmcnt(0)\n"
      "v_mov_b32 v16, %[lf]\n"
      "v_mov_b32 v17, 1\n"
      "ds_write_b8 v16, v17 offset:84\n"
      LTW(252, 5)
      "s_mov_b32 %[t0], 4194304\n"
      "buffer_load_dwordx4 v[96:99], %[v0], %[rv], %[t0] offen nt\n"
      "buffer_load_dwordx4 v[100:103], %[v1], %[rv], %[t0] offen offset:1024 nt\n"
      "buffer_load_dwordx4 v[104:107], %[v0], %[rv], %[t0] offen offset:2048 nt\n"
      "buffer_load_dwordx4 v[108:111], %[v1], %[rv], %[t0] offen offset:3072 nt\n"
      LTW(251, 11)
      "s_waitcnt vmcnt(26)\n"
      LTW(250, 6)
      "s_cmp_ge_i32 %[c0], 3\n"
      "s_cbranch_scc1 .LgA_6_d\n"
      "v_mov_b32 v16, %[lc]\n"
      "s_mov_b32 %[t1], 0x100000\n"
      ".LgA_6_l:\n"
      "ds_read_b128 v[20:23], v16 offset:16\n"
      "s_waitcnt lgkmcnt(0)\n"
      "v_min_i32 v20, v20, v21\n"
      "v_min_i32 v22, v22, v23\n"
      "s_nop 1\n"
      "v_readfirstlane_b32 %[c0], v20\n"
      "v_readfirstlane_b32 %[c1], v22\n"
      "s_cmp_ge_i32 %[c0], 3\n"
      "s_cbranch_scc1 .LgA_6_d\n"
      "s_sleep 1\n"
      "s_sub_u32 %[t1], %[t1], 1\n"
      "s_cbranch_scc0 .LgA_6_l\n"
      ".LgA_6_d:\n"
      "s_cmp_ge_i32 %[c1], 3\n"
      "s_cbranch_scc1 .LgA_7_d\n"
      "v_mov_b32 v16, %[lc]\n"
      "s_mov_b32 %[t1], 0x100000\n"
      ".LgA_7_l:\n"
      "ds_read_b128 v[20:23], v16 offset:16\n"
      "s_waitcnt lgkmcnt(0)\n"
      "v_min_i32 v20, v20, v21\n"
      "v_min_i32 v22, v22, v23\n"
      "s_nop 1\n"
      "v_readfirstlane_b32 %[c0], v20\n"
      "v_readfirstlane_b32 %[c1], v22\n"
      "s_cmp_ge_i32 %[c1], 3\n"
      "s_cbranch_scc1 .LgA_7_d\n"
      "s_sleep 1\n"
      "s_sub_u32 %[t1], %[t1], 1\n"
      "s_cbranch_scc0 .LgA_7_l\n"
      ".LgA_7_d:\n"
      "v_add_u32 v17, 40960, %[pa]\n"
      "v_add_u32 v18, 57344, %[px]\n"
      "ds_write_b128 v17, v[144:147] offset:0\n"
      "ds_write_b128 v17, v[148:151] offset:1024\n"
      "ds_write_b128 v17, v[152:155] offset:2048\n"
      "ds_write_b128 v17, v[156:159] offset:3072\n"
      "ds_write_b128 v18, v[160:163] offset:0\n"
      "ds_write_b128 v18, v[164:167] offset:1024\n"
      "ds_write_b128 v18, v[168:171] offset:2048\n"
      "ds_write_b128 v18, v[172:175] offset:3072\n"
      "ds_write_b128 v18, v[176:179] offset:4096\n"
      "ds_write_b128 v18, v[180:183] offset:5120\n"
      "s_waitcnt lgkmcnt(0)\n"
      "v_mov_b32 v16, %[lf]\n"
      "v_mov_b32 v17, 1\n"
      "ds_write_b8 v16, v17 offset:88\n"
      LTW(252, 6)
      "s_mov_b32 %[t0], 16384\n"
      "buffer_load_dwordx4 v[112:115], %[k0], %[rk], %[t0] offen nt\n"
      "buffer_load_dwordx4 v[116:119], %[k1], %[rk], %[t0] offen offset:1024 nt\n"
      "buffer_load_dwordx4 v[120:123], %[k2], %[rk], %[t0] offen offset:2048 nt\n"
      "buffer_load_dwordx4 v[124:127], %[k3], %[rk], %[t0] offen offset:3072 nt\n"
      LTW(251, 12)
      "s_waitcnt vmcnt(20)\n"
      LTW(250, 7)
      "s_cmp_ge_i32 %[c0], 4\n"
      "s_cbranch_scc1 .LgA_8_d\n"
      "v_mov_b32 v16, %[lc]\n"
      "s_mov_b32 %[t1], 0x100000\n"
      ".LgA_8_l:\n"
      "ds_read_b128 v[20:23], v16 offset:16\n"
      "s_waitcnt lgkmcnt(0)\n"
      "v_min_i32 v20, v20, v21\n"
      "v_min_i32 v22, v22, v23\n"
      "s_nop 1\n"
      "v_readfirstlane_b32 %[c0], v20\n"
      "v_readfirstlane_b32 %[c1], v22\n"
      "s_cmp_ge_i32 %[c0], 4\n"
      "s_cbranch_scc1 .LgA_8_d\n"
      "s_sleep 1\n"
      "s_sub_u32 %[t1], %[t1], 1\n"
      "s_cbranch_scc0 .LgA_8_l\n"
      ".LgA_8_d:\n"
      "s_cmp_ge_i32 %[c1], 4\n"
      "s_cbranch_scc1 .LgA_9_d\n"
      "v_mov_b32 v16, %[lc]\n"
      "s_mov_b32 %[t1], 0x100000\n"
      ".LgA_9_l:\n"
      "ds_read_b128 v[20:23], v16 offset:16\n"
      "s_waitcnt lgkmcnt(0)\n"
      "v_min_i32 v20, v20, v21\n"
      "v_min_i32 v22, v22, v23\n"
      "s_nop 1\n"
      "v_readfirstlane_b32 %[c0], v20\n"
      "v_readfirstlane_b32 %[c1], v22\n"
      "s_cmp_ge_i32 %[c1], 4\n"
      "s_cbranch_scc1 .LgA_9_d\n"
      "s_sleep 1\n"
      "s_sub_u32 %[t1], %[t1], 1\n"
      "s_cbranch_scc0 .LgA_9_l\n"
      ".LgA_9_d:\n"
      "v_add_u32 v17, 81920, %[pa]\n"
      "v_add_u32 v18, 98304, %[px]\n"
      "ds_write_b128 v17, v[24:27] offset:0\n"
      "ds_write_b128 v17, v[28:31] offset:1024\n"
      "ds_write_b128 v17, v[32:35] offset:2048\n"
      "ds_write_b128 v17, v[36:39] offset:3072\n"
      "ds_write_b128 v18, v[40:43] offset:0\n"
      "ds_write_b128 v18, v[44:47] offset:1024\n"
      "ds_write_b128 v18, v[48:51] offset:2048\n"
      "ds_write_b128 v18, v[52:55] offset:3072\n"
      "ds_write_b128 v18, v[56:59] offset:4096\n"
      "ds_write_b128 v18, v[60:63] offset:5120\n"
      "s_waitcnt lgkmcnt(0)\n"
      "v_mov_b32 v16, %[lf]\n"
      "v_mov_b32 v17, 1\n"
      "ds_write_b8 v16, v17 offset:92\n"
      LTW(252, 7)
      "s_mov_b32 %[t0], 4210688\n"
      "buffer_load_dwordx4 v[24:27], %[k0], %[rk], %[t0] offen nt\n"
      "buffer_load_dwordx4 v[28:31], %[k1], %[rk], %[t0] offen offset:1024 nt\n"
      "buffer_load_dwordx4 v[32:35], %[k2], %[rk], %[t0] offen offset:2048 nt\n"
      "buffer_load_dwordx4 v[36:39], %[k3], %[rk], %[t0] offen offset:3072 nt\n"
      LTW(251, 13)
      "s_waitcnt vmcnt(20)\n"
      LTW(250, 8)
      "v_mov_b32 v16, %[lf]\n"
      "v_mov_b32 v17, 1\n"
      "ds_write_b8 v16, v17 offset:96\n"
      LTW(252, 8)
      "s_waitcnt vmcnt(16)\n"
      LTW(250, 9)
      "s_cmp_ge_i32 %[c0], 5\n"
      "s_cbranch_scc1 .LgA_10_d\n"
      "v_mov_b32 v16, %[lc]\n"
      "s_mov_b32 %[t1], 0x100000\n"
      ".LgA_10_l:\n"
      "ds_read_b128 v[20:23], v16 offset:16\n"
      "s_waitcnt lgkmcnt(0)\n"
      "v_min_i32 v20, v20, v21\n"
      "v_min_i32 v22, v22, v23\n"
      "s_nop 1\n"
      "v_readfirstlane_b32 %[c0], v20\n"
      "v_readfirstlane_b32 %[c1], v22\n"
      "s_cmp_ge_i32 %[c0], 5\n"
      "s_cbranch_scc1 .LgA_10_d\n"
      "s_sleep 1\n"
      "s_sub_u32 %[t1], %[t1], 1\n"
      "s_cbranch_scc0 .LgA_10_l\n"
      ".LgA_10_d:\n"
      "s_cmp_ge_i32 %[c1], 5\n"
      "s_cbranch_scc1 .LgA_11_d\n"
      "v_mov_b32 v16, %[lc]\n"
      "s_mov_b32 %[t1], 0x100000\n"
      ".LgA_11_l:\n"
      "ds_read_b128 v[20:23], v16 offset:16\n"
      "s_waitcnt lgkmcnt(0)\n"
      "v_min_i32 v20, v20, v21\n"
      "v_min_i32 v22, v22, v23\n"
      "s_nop 1\n"
      "v_readfirstlane_b32 %[c0], v20\n"
      "v_readfirstlane_b32 %[c1], v22\n"
      "s_cmp_ge_i32 %[c1], 5\n"
      "s_cbranch_scc1 .LgA_11_d\n"
      "s_sleep 1\n"
      "s_sub_u32 %[t1], %[t1], 1\n"
      "s_cbranch_scc0 .LgA_11_l\n"
      ".LgA_11_d:\n"
      "v_add_u32 v17, 0, %[pa]\n"
      "ds_write_b128 v17, v[64:67] offset:0\n"
      "ds_write_b128 v17, v[68:71] offset:1024\n"
      "ds_write_b128 v17, v[72:75] offset:2048\n"
      "ds_write_b128 v17, v[76:79] offset:3072\n"
      "s_waitcnt lgkmcnt(0)\n"
      "v_mov_b32 v16, %[lf]\n"
      "v_mov_b32 v17, 1\n"
      "ds_write_b8 v16, v17 offset:100\n"
      LTW(252, 9)
      "s_mov_b32 %[t0], 16384\n"
      "buffer_load_dwordx4 v[40:43], %[v0], %[rv], %[t0] offen nt\n"
      "buffer_load_dwordx4 v[44:47], %[v1], %[rv], %[t0] offen offset:1024 nt\n"
      "buffer_load_dwordx4 v[48:51], %[v0], %[rv], %[t0] offen offset:2048 nt\n"
      "buffer_load_dwordx4 v[52:55], %[v1], %[rv], %[t0] offen offset:3072 nt\n"
      "s_waitcnt vmcnt(16)\n"
      LTW(250, 10)
      "v_add_u32 v17, 16384, %[pa]\n"
      "ds_write_b128 v17, v[80:83] offset:0\n"
      "ds_write_b128 v17, v[84:87] offset:1024\n"
      "ds_write_b128 v17, v[88:91] offset:2048\n"
      "ds_write_b128 v17, v[92:95] offset:3072\n"
      "s_waitcnt lgkmcnt(0)\n"
      "v_mov_b32 v16, %[lf]\n"
      "v_mov_b32 v17, 1\n"
      "ds_write_b8 v16, v17 offset:104\n"
      LTW(252, 10)
      "s_mov_b32 %[t0], 4210688\n"
      "buffer_load_dwordx4 v[56:59], %[v0], %[rv], %[t0] offen nt\n"
      "buffer_load_dwordx4 v[60:63], %[v1], %[rv], %[t0] offen offset:1024 nt\n"
      "buffer_load_dwordx4 v[64:67], %[v0], %[rv], %[t0] offen offset:2048 nt\n"
      "buffer_load_dwordx4 v[68:71], %[v1], %[rv], %[t0] offen offset:3072 nt\n"
      "s_waitcnt vmcnt(16)\n"
      LTW(250, 11)
      "s_cmp_ge_i32 %[c0], 6\n"
      "s_cbranch_scc1 .LgA_12_d\n"
      "v_mov_b32 v16, %[lc]\n"
      "s_mov_b32 %[t1], 0x100000\n"
      ".LgA_12_l:\n"
      "ds_read_b128 v[20:23], v16 offset:16\n"
      "s_waitcnt lgkmcnt(0)\n"
      "v_min_i32 v20, v20, v21\n"
      "v_min_i32 v22, v22, v23\n"
      "s_nop 1\n"
      "v_readfirstlane_b32 %[c0], v20\n"
      "v_readfirstlane_b32 %[c1], v22\n"
      "s_cmp_ge_i32 %[c0], 6\n"
      "s_cbranch_scc1 .LgA_12_d\n"
      "s_sleep 1\n"
      "s_sub_u32 %[t1], %[t1], 1\n"
      "s_cbranch_scc0 .LgA_12_l\n"
      ".LgA_12_d:\n"
      "s_cmp_ge_i32 %[c1], 6\n"
      "s_cbranch_scc1 .LgA_13_d\n"
      "v_mov_b32 v16, %[lc]\n"
      "s_mov_b32 %[t1], 0x100000\n"
      ".LgA_13_l:\n"
      "ds_read_b128 v[20:23], v16 offset:16\n"
      "s_waitcnt lgkmcnt(0)\n"
      "v_min_i32 v20, v20, v21\n"
      "v_min_i32 v22, v22, v23\n"
      "s_nop 1\n"
      "v_readfirstlane_b32 %[c0], v20\n"
      "v_readfirstlane_b32 %[c1], v22\n"
      "s_cmp_ge_i32 %[c1], 6\n"
      "s_cbranch_scc1 .LgA_13_d\n"
      "s_sleep 1\n"
      "s_sub_u32 %[t1], %[t1], 1\n"
      "s_cbranch_scc0 .LgA_13_l\n"
      ".LgA_13_d:\n"
      "v_add_u32 v17, 32768, %[pa]\n"
      "ds_write_b128 v17, v[96:99] offset:0\n"
      "ds_write_b128 v17, v[100:103] offset:1024\n"
      "ds_write_b128 v17, v[104:107] offset:2048\n"
      "ds_write_b128 v17, v[108:111] offset:3072\n"
      "s_waitcnt lgkmcnt(0)\n"
      "v_mov_b32 v16, %[lf]\n"
      "v_mov_b32 v17, 1\n"
      "ds_write_b8 v16, v17 offset:108\n"
      LTW(252, 11)
      "s_mov_b32 %[t0], 32768\n"
      "buffer_load_dwordx4 v[72:75], %[k0], %[rk], %[t0] offen nt\n"
      "buffer_load_dwordx4 v[76:79], %[k1], %[rk], %[t0] offen offset:1024 nt\n"
      "buffer_load_dwordx4 v[80:83], %[k2], %[rk], %[t0] offen offset:2048 nt\n"
      "buffer_load_dwordx4 v[84:87], %[k3], %[rk], %[t0] offen offset:3072 nt\n"
      "s_waitcnt vmcnt(16)\n"
      LTW(250, 12)
      "v_add_u32 v17, 49152, %[pa]\n"
      "ds_write_b128 v17, v[112:115] offset:0\n"
      "ds_write_b128 v17, v[116:119] offset:1024\n"
      "ds_write_b128 v17, v[120:123] offset:2048\n"
      "ds_write_b128 v17, v[124:127] offset:3072\n"
      "s_waitcnt lgkmcnt(0)\n"
      "v_mov_b32 v16, %[lf]\n"
      "v_mov_b32 v17, 1\n"
      "ds_write_b8 v16, v17 offset:112\n"
      LTW(252, 12)
      "s_mov_b32 %[t0], 4227072\n"
      "buffer_load_dwordx4 v[88:91], %[k0], %[rk], %[t0] offen nt\n"
      "buffer_load_dwordx4 v[92:95], %[k1], %[rk], %[t0] offen offset:1024 nt\n"
      "buffer_load_dwordx4 v[96:99], %[k2], %[rk], %[t0] offen offset:2048 nt\n"
      "buffer_load_dwordx4 v[100:103], %[k3], %[rk], %[t0] offen offset:3072 nt\n"
      "s_waitcnt vmcnt(16)\n"
      LTW(250, 13)
      "v_add_u32 v17, 65536, %[pa]\n"
      "ds_write_b128 v17, v[24:27] offset:0\n"
      "ds_write_b128 v17, v[28:31] offset:1024\n"
      "ds_write_b128 v17, v[32:35] offset:2048\n"
      "ds_write_b128 v17, v[36:39] offset:3072\n"
      "s_waitcnt lgkmcnt(0)\n"
      "v_mov_b32 v16, %[lf]\n"
      "v_mov_b32 v17, 1\n"
      "ds_write_b8 v16, v17 offset:116\n"
      LTW(252, 13)
      "s_mov_b32 %[t0], 32768\n"
      "buffer_load_dwordx4 v[24:27], %[v0], %[rv], %[t0] offen nt\n"
      "buffer_load_dwordx4 v[28:31], %[v1], %[rv], %[t0] offen offset:1024 nt\n"
      "buffer_load_dwordx4 v[32:35], %[v0], %[rv], %[t0] offen offset:2048 nt\n"
      "buffer_load_dwordx4 v[36:39], %[v1], %[rv], %[t0] offen offset:3072 nt\n"
      "s_waitcnt vmcnt(16)\n"
      LTW(250, 14)
      "s_cmp_ge_i32 %[c0], 7\n"
      "s_cbranch_scc1 .LgA_14_d\n"
      "v_mov_b32 v16, %[lc]\n"
      "s_mov_b32 %[t1], 0x100000\n"
      ".LgA_14_l:\n"
      "ds_read_b128 v[20:23], v16 offset:16\n"
      "s_waitcnt lgkmcnt(0)\n"
      "v_min_i32 v20, v20, v21\n"
      "v_min_i32 v22, v22, v23\n"
      "s_nop 1\n"
      "v_readfirstlane_b32 %[c0], v20\n"
      "v_readfirstlane_b32 %[c1], v22\n"
      "s_cmp_ge_i32 %[c0], 7\n"
      "s_cbranch_scc1 .LgA_14_d\n"
      "s_sleep 1\n"
      "s_sub_u32 %[t1], %[t1], 1\n"
      "s_cbranch_scc0 .LgA_14_l\n"
      ".LgA_14_d:\n"
      "s_cmp_ge_i32 %[c1], 7\n"
      "s_cbranch_scc1 .LgA_15_d\n"
      "v_mov_b32 v16, %[lc]\n"
      "s_mov_b32 %[t1], 0x100000\n"
      ".LgA_15_l:\n"
      "ds_read_b128 v[20:23], v16 offset:16\n"
      "s_waitcnt lgkmcnt(0)\n"
      "v_min_i32 v20, v20, v21\n"
      "v_min_i32 v22, v22, v23\n"
      "s_nop 1\n"
      "v_readfirstlane_b32 %[c0], v20\n"
      "v_readfirstlane_b32 %[c1], v22\n"
      "s_cmp_ge_i32 %[c1], 7\n"
      "s_cbranch_scc1 .LgA_15_d\n"
      "s_sleep 1\n"
      "s_sub_u32 %[t1], %[t1], 1\n"
      "s_cbranch_scc0 .LgA_15_l\n"
      ".LgA_15_d:\n"
      "v_add_u32 v17, 81920, %[pa]\n"
      "ds_write_b128 v17, v[40:43] offset:0\n"
      "ds_write_b128 v17, v[44:47] offset:1024\n"
      "ds_write_b128 v17, v[48:51] offset:2048\n"
      "ds_write_b128 v17, v[52:55] offset:3072\n"
      "s_waitcnt lgkmcnt(0)\n"
      "v_mov_b32 v16, %[lf]\n"
      "v_mov_b32 v17, 1\n"
      "ds_write_b8 v16, v17 offset:120\n"
      LTW(252, 14)
      "s_mov_b32 %[t0], 4227072\n"
      "buffer_load_dwordx4 v[40:43], %[v0], %[rv], %[t0] offen nt\n"
      "buffer_load_dwordx4 v[44:47], %[v1], %[rv], %[t0] offen offset:1024 nt\n"
      "buffer_load_dwordx4 v[48:51], %[v0], %[rv], %[t0] offen offset:2048 nt\n"
      "buffer_load_dwordx4 v[52:55], %[v1], %[rv], %[t0] offen offset:3072 nt\n"
      "s_waitcnt vmcnt(16)\n"
      LTW(250, 15)
      "v_add_u32 v17, 98304, %[pa]\n"
      "ds_write_b128 v17, v[56:59] offset:0\n"
      "ds_write_b128 v17, v[60:63] offset:1024\n"
      "ds_write_b128 v17, v[64:67] offset:2048\n"
      "ds_write_b128 v17, v[68:71] offset:3072\n"
      "s_waitcnt lgkmcnt(0)\n"
      "v_mov_b32 v16, %[lf]\n"
      "v_mov_b32 v17, 1\n"
      "ds_write_b8 v16, v17 offset:124\n"
      LTW(252, 15)
      "s_mov_b32 %[t0], 49152\n"
      "buffer_load_dwordx4 v[56:59], %[k0], %[rk], %[t0] offen nt\n"
      "buffer_load_dwordx4 v[60:63], %[k1], %[rk], %[t0] offen offset:1024 nt\n"
      "buffer_load_dwordx4 v[64:67], %[k2], %[rk], %[t0] offen offset:2048 nt\n"
      "buffer_load_dwordx4 v[68:71], %[k3], %[rk], %[t0] offen offset:3072 nt\n"
      "s_waitcnt vmcnt(16)\n"
      LTW(250, 16)
      "v_add_u32 v17, 114688, %[pa]\n"
      "ds_write_b128 v17, v[72:75] offset:0\n"
      "ds_write_b128 v17, v[76:79] offset:1024\n"
      "ds_write_b128 v17, v[80:83] offset:2048\n"
      "ds_write_b128 v17, v[84:87] offset:3072\n"
      "s_waitcnt lgkmcnt(0)\n"
      "v_mov_b32 v16, %[lf]\n"
      "v_mov_b32 v17, 1\n"
      "ds_write_b8 v16, v17 offset:128\n"
      LTW(252, 16)
      "s_mov_b32 %[t0], 4243456\n"
      "buffer_load_dwordx4 v[72:75], %[k0], %[rk], %[t0] offen nt\n"
      "buffer_load_dwordx4 v[76:79], %[k1], %[rk], %[t0] offen offset:1024 nt\n"
      "buffer_load_dwordx4 v[80:83], %[k2], %[rk], %[t0] offen offset:2048 nt\n"
      "buffer_load_dwordx4 v[84:87], %[k3], %[rk], %[t0] offen offset:3072 nt\n"
      "s_waitcnt vmcnt(16)\n"
      LTW(250, 17)
      "s_cmp_ge_i32 %[c0], 8\n"
      "s_cbranch_scc1 .LgA_16_d\n"
      "v_mov_b32 v16, %[lc]\n"
      "s_mov_b32 %[t1], 0x100000\n"
      ".LgA_16_l:\n"
      "ds_read_b128 v[20:23], v16 offset:16\n"
      "s_waitcnt lgkmcnt(0)\n"
      "v_min_i32 v20, v20, v21\n"
      "v_min_i32 v22, v22, v23\n"
      "s_nop 1\n"
      "v_readfirstlane_b32 %[c0], v20\n"
      "v_readfirstlane_b32 %[c1], v22\n"
      "s_cmp_ge_i32 %[c0], 8\n"
      "s_cbranch_scc1 .LgA_16_d\n"
      "s_sleep 1\n"
      "s_sub_u32 %[t1], %[t1], 1\n"
      "s_cbranch_scc0 .LgA_16_l\n"
      ".LgA_16_d:\n"
      "v_add_u32 v17, 131072, %[pa]\n"
      "ds_write_b128 v17, v[88:91] offset:0\n"
      "ds_write_b128 v17, v[92:95] offset:1024\n"
      "ds_write_b128 v17, v[96:99] offset:2048\n"
      "ds_write_b128 v17, v[100:103] offset:3072\n"
      "s_waitcnt lgkmcnt(0)\n"
      "v_mov_b32 v16, %[lf]\n"
      "v_mov_b32 v17, 1\n"
      "ds_write_b8 v16, v17 offset:132\n"
      LTW(252, 17)
      "s_mov_b32 %[t0], 49152\n"
      "buffer_load_dwordx4 v[88:91], %[v0], %[rv], %[t0] offen nt\n"
      "buffer_load_dwordx4 v[92:95], %[v1], %[rv], %[t0] offen offset:1024 nt\n"
      "buffer_load_dwordx4 v[96:99], %[v0], %[rv], %[t0] offen offset:2048 nt\n"
      "buffer_load_dwordx4 v[100:103], %[v1], %[rv], %[t0] offen offset:3072 nt\n"
      "s_waitcnt vmcnt(16)\n"
      LTW(250, 18)
      "s_cmp_ge_i32 %[c1], 9\n"
      "s_cbranch_scc1 .LgA_17_d\n"
      "v_mov_b32 v16, %[lc]\n"
      "s_mov_b32 %[t1], 0x100000\n"
      ".LgA_17_l:\n"
      "ds_read_b128 v[20:23], v16 offset:16\n"
      "s_waitcnt lgkmcnt(0)\n"
      "v_min_i32 v20, v20, v21\n"
      "v_min_i32 v22, v22, v23\n"
      "s_nop 1\n"
      "v_readfirstlane_b32 %[c0], v20\n"
      "v_readfirstlane_b32 %[c1], v22\n"
      "s_cmp_ge_i32 %[c1], 9\n"
      "s_cbranch_scc1 .LgA_17_d\n"
      "s_sleep 1\n"
      "s_sub_u32 %[t1], %[t1], 1\n"
      "s_cbranch_scc0 .LgA_17_l\n"
      ".LgA_17_d:\n"
      "v_add_u32 v17, 0, %[pa]\n"
      "ds_write_b128 v17, v[24:27] offset:0\n"
      "ds_write_b128 v17, v[28:31] offset:1024\n"
      "ds_write_b128 v17, v[32:35] offset:2048\n"
      "ds_write_b128 v17, v[36:39] offset:3072\n"
      "s_waitcnt lgkmcnt(0)\n"
      "v_mov_b32 v16, %[lf]\n"
      "v_mov_b32 v17, 1\n"
      "ds_write_b8 v16, v17 offset:136\n"
      LTW(252, 18)
      "s_mov_b32 %[t0], 4243456\n"
      "buffer_load_dwordx4 v[24:27], %[v0], %[rv], %[t0] offen nt\n"
      "buffer_load_dwordx4 v[28:31], %[v1], %[rv], %[t0] offen offset:1024 nt\n"
      "buffer_load_dwordx4 v[32:35], %[v0], %[rv], %[t0] offen offset:2048 nt\n"
      "buffer_load_dwordx4 v[36:39], %[v1], %[rv], %[t0] offen offset:3072 nt\n"
      "s_waitcnt vmcnt(16)\n"
      LTW(250, 19)
      "s_cmp_ge_i32 %[c2], 10\n"
      "s_cbranch_scc1 .LgA_18_d\n"
      "v_mov_b32 v16, %[lc]\n"
      "s_mov_b32 %[t1], 0x100000\n"
      ".LgA_18_l:\n"
      "ds_read_b128 v[20:23], v16 offset:32\n"
      "s_waitcnt lgkmcnt(0)\n"
      "v_min_i32 v20, v20, v21\n"
      "v_min_i32 v22, v22, v23\n"
      "s_nop 1\n"
      "v_readfirstlane_b32 %[c2], v20\n"
      "v_readfirstlane_b32 %[c3], v22\n"
      "s_cmp_ge_i32 %[c2], 10\n"
      "s_cbranch_scc1 .LgA_18_d\n"
      "s_sleep 1\n"
      "s_sub_u32 %[t1], %[t1], 1\n"
      "s_cbranch_scc0 .LgA_18_l\n"
      ".LgA_18_d:\n"
      "v_add_u32 v17, 16384, %[pa]\n"
      "ds_write_b128 v17, v[40:43] offset:0\n"
      "ds_write_b128 v17, v[44:47] offset:1024\n"
      "ds_write_b128 v17, v[48:51] offset:2048\n"
      "ds_write_b128 v17, v[52:55] offset:3072\n"
      "s_waitcnt lgkmcnt(0)\n"
      "v_mov_b32 v16, %[lf]\n"
      "v_mov_b32 v17, 1\n"
      "ds_write_b8 v16, v17 offset:140\n"
      LTW(252, 19)
      "s_mov_b32 %[t0], 65536\n"
      "buffer_load_dwordx4 v[40:43], %[k0], %[rk], %[t0] offen nt\n"
      "buffer_load_dwordx4 v[44:47], %[k1], %[rk], %[t0] offen offset:1024 nt\n"
      "buffer_load_dwordx4 v[48:51], %[k2], %[rk], %[t0] offen offset:2048 nt\n"
      "buffer_load_dwordx4 v[52:55], %[k3], %[rk], %[t0] offen offset:3072 nt\n"
      "s_waitcnt vmcnt(16)\n"
      LTW(250, 20)
      "s_cmp_ge_i32 %[c3], 11\n"
      "s_cbranch_scc1 .LgA_19_d\n"
      "v_mov_b32 v16, %[lc]\n"
      "s_mov_b32 %[t1], 0x100000\n"
      ".LgA_19_l:\n"
      "ds_read_b128 v[20:23], v16 offset:32\n"
      "s_waitcnt lgkmcnt(0)\n"
      "v_min_i32 v20, v20, v21\n"
      "v_min_i32 v22, v22, v23\n"
      "s_nop 1\n"
      "v_readfirstlane_b32 %[c2], v20\n"
      "v_readfirstlane_b32 %[c3], v22\n"
      "s_cmp_ge_i32 %[c3], 11\n"
      "s_cbranch_scc1 .LgA_19_d\n"
      "s_sleep 1\n"
      "s_sub_u32 %[t1], %[t1], 1\n"
      "s_cbranch_scc0 .LgA_19_l\n"
      ".LgA_19_d:\n"
      "v_add_u32 v17, 32768, %[pa]\n"
      "ds_write_b128 v17, v[56:59] offset:0\n"
      "ds_write_b128 v17, v[60:63] offset:1024\n"
      "ds_write_b128 v17, v[64:67] offset:2048\n"
      "ds_write_b128 v17, v[68:71] offset:3072\n"
      "s_waitcnt lgkmcnt(0)\n"
      "v_mov_b32 v16, %[lf]\n"
      "v_mov_b32 v17, 1\n"
      "ds_write_b8 v16, v17 offset:144\n"
      LTW(252, 20)
      "s_mov_b32 %[t0], 4259840\n"
      "buffer_load_dwordx4 v[56:59], %[k0], %[rk], %[t0] offen nt\n"
      "buffer_load_dwordx4 v[60:63], %[k1], %[rk], %[t0] offen offset:1024 nt\n"
      "buffer_load_dwordx4 v[64:67], %[k2], %[rk], %[t0] offen offset:2048 nt\n"
      "buffer_load_dwordx4 v[68:71], %[k3], %[rk], %[t0] offen offset:3072 nt\n"
      "s_waitcnt vmcnt(16)\n"
      LTW(250, 21)
      "s_cmp_ge_i32 %[c0], 12\n"
      "s_cbranch_scc1 .LgA_20_d\n"
      "v_mov_b32 v16, %[lc]\n"
      "s_mov_b32 %[t1], 0x100000\n"
      ".LgA_20_l:\n"
      "ds_read_b128 v[20:23], v16 offset:16\n"
      "s_waitcnt lgkmcnt(0)\n"
      "v_min_i32 v20, v20, v21\n"
      "v_min_i32 v22, v22, v23\n"
      "s_nop 1\n"
      "v_readfirstlane_b32 %[c0], v20\n"
      "v_readfirstlane_b32 %[c1], v22\n"
      "s_cmp_ge_i32 %[c0], 12\n"
      "s_cbranch_scc1 .LgA_20_d\n"
      "s_sleep 1\n"
      "s_sub_u32 %[t1], %[t1], 1\n"
      "s_cbranch_scc0 .LgA_20_l\n"
      ".LgA_20_d:\n"
      "v_add_u32 v17, 49152, %[pa]\n"
      "ds_write_b128 v17, v[72:75] offset:0\n"
      "ds_write_b128 v17, v[76:79] offset:1024\n"
      "ds_write_b128 v17, v[80:83] offset:2048\n"
      "ds_write_b128 v17, v[84:87] offset:3072\n"
      "s_waitcnt lgkmcnt(0)\n"
      "v_mov_b32 v16, %[lf]\n"
      "v_mov_b32 v17, 1\n"
      "ds_write_b8 v16, v17 offset:148\n"
      LTW(252, 21)
      "s_mov_b32 %[t0], 65536\n"
      "buffer_load_dwordx4 v[72:75], %[v0], %[rv], %[t0] offen nt\n"
      "buffer_load_dwordx4 v[76:79], %[v1], %[rv], %[t0] offen offset:1024 nt\n"
      "buffer_load_dwordx4 v[80:83], %[v0], %[rv], %[t0] offen offset:2048 nt\n"
      "buffer_load_dwordx4 v[84:87], %[v1], %[rv], %[t0] offen offset:3072 nt\n"
      "s_waitcnt vmcnt(16)\n"
      LTW(250, 22)
      "s_cmp_ge_i32 %[c1], 13\n"
      "s_cbranch_scc1 .LgA_21_d\n"
      "v_mov_b32 v16, %[lc]\n"
      "s_mov_b32 %[t1], 0x100000\n"
      ".LgA_21_l:\n"
      "ds_read_b128 v[20:23], v16 offset:16\n"
      "s_waitcnt lgkmcnt(0)\n"
      "v_min_i32 v20, v20, v21\n"
      "v_min_i32 v22, v22, v23\n"
      "s_nop 1\n"
      "v_readfirstlane_b32 %[c0], v20\n"
      "v_readfirstlane_b32 %[c1], v22\n"
      "s_cmp_ge_i32 %[c1], 13\n"
      "s_cbranch_scc1 .LgA_21_d\n"
      "s_sleep 1\n"
      "s_sub_u32 %[t1], %[t1], 1\n"
      "s_cbranch_scc0 .LgA_21_l\n"
      ".LgA_21_d:\n"
      "v_add_u32 v17, 65536, %[pa]\n"
      "ds_write_b128 v17, v[88:91] offset:0\n"
      "ds_write_b128 v17, v[92:95] offset:1024\n"
      "ds_write_b128 v17, v[96:99] offset:2048\n"
      "ds_write_b128 v17, v[100:103] offset:3072\n"
      "s_waitcnt lgkmcnt(0)\n"
      "v_mov_b32 v16, %[lf]\n"
      "v_mov_b32 v17, 1\n"
      "ds_write_b8 v16, v17 offset:152\n"
      LTW(252, 22)
      "s_mov_b32 %[t0], 4259840\n"
      "buffer_load_dwordx4 v[88:91], %[v0], %[rv], %[t0] offen nt\n"
      "buffer_load_dwordx4 v[92:95], %[v1], %[rv], %[t0] offen offset:1024 nt\n"
      "buffer_load_dwordx4 v[96:99], %[v0], %[rv], %[t0] offen offset:2048 nt\n"
      "buffer_load_dwordx4 v[100:103], %[v1], %[rv], %[t0] offen offset:3072 nt\n"
      "s_waitcnt vmcnt(16)\n"
      LTW(250, 23)
      "s_cmp_ge_i32 %[c2], 14\n"
      "s_cbranch_scc1 .LgA_22_d\n"
      "v_mov_b32 v16, %[lc]\n"
      "s_mov_b32 %[t1], 0x100000\n"
      ".LgA_22_l:\n"
      "ds_read_b128 v[20:23], v16 offset:32\n"
      "s_waitcnt lgkmcnt(0)\n"
      "v_min_i32 v20, v20, v21\n"
      "v_min_i32 v22, v22, v23\n"
      "s_nop 1\n"
      "v_readfirstlane_b32 %[c2], v20\n"
      "v_readfirstlane_b32 %[c3], v22\n"
      "s_cmp_ge_i32 %[c2], 14\n"
      "s_cbranch_scc1 .LgA_22_d\n"
      "s_sleep 1\n"
      "s_sub_u32 %[t1], %[t1], 1\n"
      "s_cbranch_scc0 .LgA_22_l\n"
      ".LgA_22_d:\n"
      "v_add_u32 v17, 81920, %[pa]\n"
      "ds_write_b128 v17, v[24:27] offset:0\n"
      "ds_write_b128 v17, v[28:31] offset:1024\n"
      "ds_write_b128 v17, v[32:35] offset:2048\n"
      "ds_write_b128 v17, v[36:39] offset:3072\n"
      "s_waitcnt lgkmcnt(0)\n"
      "v_mov_b32 v16, %[lf]\n"
      "v_mov_b32 v17, 1\n"
      "ds_write_b8 v16, v17 offset:156\n"
      LTW(252, 23)
      "s_mov_b32 %[t0], 81920\n"
      "buffer_load_dwordx4 v[24:27], %[k0], %[rk], %[t0] offen nt\n"
      "buffer_load_dwordx4 v[28:31], %[k1], %[rk], %[t0] offen offset:1024 nt\n"
      "buffer_load_dwordx4 v[32:35], %[k2], %[rk], %[t0] offen offset:2048 nt\n"
      "buffer_load_dwordx4 v[36:39], %[k3], %[rk], %[t0] offen offset:3072 nt\n"
      "s_waitcnt vmcnt(16)\n"
      LTW(250, 24)
      "s_cmp_ge_i32 %[c3], 15\n"
      "s_cbranch_scc1 .LgA_23_d\n"
      "v_mov_b32 v16, %[lc]\n"
      "s_mov_b32 %[t1], 0x100000\n"
      ".LgA_23_l:\n"
      "ds_read_b128 v[20:23], v16 offset:32\n"
      "s_waitcnt lgkmcnt(0)\n"
      "v_min_i32 v20, v20, v21\n"
      "v_min_i32 v22, v22, v23\n"
      "s_nop 1\n"
      "v_readfirstlane_b32 %[c2], v20\n"
      "v_readfirstlane_b32 %[c3], v22\n"
      "s_cmp_ge_i32 %[c3], 15\n"
      "s_cbranch_scc1 .LgA_23_d\n"
      "s_sleep 1\n"
      "s_sub_u32 %[t1], %[t1], 1\n"
      "s_cbranch_scc0 .LgA_23_l\n"
      ".LgA_23_d:\n"
      "v_add_u32 v17, 98304, %[pa]\n"
      "ds_write_b128 v17, v[40:43] offset:0\n"
      "ds_write_b128 v17, v[44:47] offset:1024\n"
      "ds_write_b128 v17, v[48:51] offset:2048\n"
      "ds_write_b128 v17, v[52:55] offset:3072\n"
      "s_waitcnt lgkmcnt(0)\n"
      "v_mov_b32 v16, %[lf]\n"
      "v_mov_b32 v17, 1\n"
      "ds_write_b8 v16, v17 offset:160\n"
      LTW(252, 24)
      "s_mov_b32 %[t0], 4276224\n"
      "buffer_load_dwordx4 v[40:43], %[k0], %[rk], %[t0] offen nt\n"
      "buffer_load_dwordx4 v[44:47], %[k1], %[rk], %[t0] offen offset:1024 nt\n"
      "buffer_load_dwordx4 v[48:51], %[k2], %[rk], %[t0] offen offset:2048 nt\n"
      "buffer_load_dwordx4 v[52:55], %[k3], %[rk], %[t0] offen offset:3072 nt\n"
      "s_waitcnt vmcnt(16)\n"
      LTW(250, 25)
      "s_cmp_ge_i32 %[c0], 16\n"
      "s_cbranch_scc1 .LgA_24_d\n"
      "v_mov_b32 v16, %[lc]\n"
      "s_mov_b32 %[t1], 0x100000\n"
      ".LgA_24_l:\n"
      "ds_read_b128 v[20:23], v16 offset:16\n"
      "s_waitcnt lgkmcnt(0)\n"
      "v_min_i32 v20, v20, v21\n"
      "v_min_i32 v22, v22, v23\n"
      "s_nop 1\n"
      "v_readfirstlane_b32 %[c0], v20\n"
      "v_readfirstlane_b32 %[c1], v22\n"
      "s_cmp_ge_i32 %[c0], 16\n"
      "s_cbranch_scc1 .LgA_24_d\n"
      "s_sleep 1\n"
      "s_sub_u32 %[t1], %[t1], 1\n"
      "s_cbranch_scc0 .LgA_24_l\n"
      ".LgA_24_d:\n"
      "v_add_u32 v17, 114688, %[pa]\n"
      "ds_write_b128 v17, v[56:59] offset:0\n"
      "ds_write_b128 v17, v[60:63] offset:1024\n"
      "ds_write_b128 v17, v[64:67] offset:2048\n"
      "ds_write_b128 v17, v[68:71] offset:3072\n"
      "s_waitcnt lgkmcnt(0)\n"
      "v_mov_b32 v16, %[lf]\n"
      "v_mov_b32 v17, 1\n"
      "ds_write_b8 v16, v17 offset:164\n"
      LTW(252, 25)
      "s_mov_b32 %[t0], 81920\n"
      "buffer_load_dwordx4 v[56:59], %[v0], %[rv], %[t0] offen nt\n"
      "buffer_load_dwordx4 v[60:63], %[v1], %[rv], %[t0] offen offset:1024 nt\n"
      "buffer_load_dwordx4 v[64:67], %[v0], %[rv], %[t0] offen offset:2048 nt\n"
      "buffer_load_dwordx4 v[68:71], %[v1], %[rv], %[t0] offen offset:3072 nt\n"
      "s_waitcnt vmcnt(16)\n"
      LTW(250, 26)
      "s_cmp_ge_i32 %[c1], 17\n"
      "s_cbranch_scc1 .LgA_25_d\n"
      "v_mov_b32 v16, %[lc]\n"
      "s_mov_b32 %[t1], 0x100000\n"
      ".LgA_25_l:\n"
      "ds_read_b128 v[20:23], v16 offset:16\n"
      "s_waitcnt lgkmcnt(0)\n"
      "v_min_i32 v20, v20, v21\n"
      "v_min_i32 v22, v22, v23\n"
      "s_nop 1\n"
      "v_readfirstlane_b32 %[c0], v20\n"
      "v_readfirstlane_b32 %[c1], v22\n"
      "s_cmp_ge_i32 %[c1], 17\n"
      "s_cbranch_scc1 .LgA_25_d\n"
      "s_sleep 1\n"
      "s_sub_u32 %[t1], %[t1], 1\n"
      "s_cbranch_scc0 .LgA_25_l\n"
      ".LgA_25_d:\n"
      "v_add_u32 v17, 131072, %[pa]\n"
      "ds_write_b128 v17, v[72:75] offset:0\n"
      "ds_write_b128 v17, v[76:79] offset:1024\n"
      "ds_write_b128 v17, v[80:83] offset:2048\n"
      "ds_write_b128 v17, v[84:87] offset:3072\n"
      "s_waitcnt lgkmcnt(0)\n"
      "v_mov_b32 v16, %[lf]\n"
      "v_mov_b32 v17, 1\n"
      "ds_write_b8 v16, v17 offset:168\n"
      LTW(252, 26)
      "s_mov_b32 %[t0], 4276224\n"
      "buffer_load_dwordx4 v[72:75], %[v0], %[rv], %[t0] offen nt\n"
      "buffer_load_dwordx4 v[76:79], %[v1], %[rv], %[t0] offen offset:1024 nt\n"
      "buffer_load_dwordx4 v[80:83], %[v0], %[rv], %[t0] offen offset:2048 nt\n"
      "buffer_load_dwordx4 v[84:87], %[v1], %[rv], %[t0] offen offset:3072 nt\n"
      "s_waitcnt vmcnt(16)\n"
      LTW(250, 27)
      "s_cmp_ge_i32 %[c2], 18\n"
      "s_cbranch_scc1 .LgA_26_d\n"
      "v_mov_b32 v16, %[lc]\n"
      "s_mov_b32 %[t1], 0x100000\n"
      ".LgA_26_l:\n"
      "ds_read_b128 v[20:23], v16 offset:32\n"
      "s_waitcnt lgkmcnt(0)\n"
      "v_min_i32 v20, v20, v21\n"
      "v_min_i32 v22, v22, v23\n"
      "s_nop 1\n"
      "v_readfirstlane_b32 %[c2], v20\n"
      "v_readfirstlane_b32 %[c3], v22\n"
      "s_cmp_ge_i32 %[c2], 18\n"
      "s_cbranch_scc1 .LgA_26_d\n"
      "s_sleep 1\n"
      "s_sub_u32 %[t1], %[t1], 1\n"
      "s_cbranch_scc0 .LgA_26_l\n"
      ".LgA_26_d:\n"
      "v_add_u32 v17, 0, %[pa]\n"
      "ds_write_b128 v17, v[88:91] offset:0\n"
      "ds_write_b128 v17, v[92:95] offset:1024\n"
      "ds_write_b128 v17, v[96:99] offset:2048\n"
      "ds_write_b128 v17, v[100:103] offset:3072\n"
      "s_waitcnt lgkmcnt(0)\n"
      "v_mov_b32 v16, %[lf]\n"
      "v_mov_b32 v17, 1\n"
      "ds_write_b8 v16, v17 offset:172\n"
      LTW(252, 27)
      "s_mov_b32 %[t0], 98304\n"
      "buffer_load_dwordx4 v[88:91], %[k0], %[rk], %[t0] offen nt\n"
      "buffer_load_dwordx4 v[92:95], %[k1], %[rk], %[t0] offen offset:1024 nt\n"
      "buffer_load_dwordx4 v[96:99], %[k2], %[rk], %[t0] offen offset:2048 nt\n"
      "buffer_load_dwordx4 v[100:103], %[k3], %[rk], %[t0] offen offset:3072 nt\n"
      "s_waitcnt vmcnt(16)\n"
      LTW(250, 28)
      "s_cmp_ge_i32 %[c3], 19\n"
      "s_cbranch_scc1 .LgA_27_d\n"
      "v_mov_b32 v16, %[lc]\n"
      "s_mov_b32 %[t1], 0x100000\n"
      ".LgA_27_l:\n"
      "ds_read_b128 v[20:23], v16 offset:32\n"
      "s_waitcnt lgkmcnt(0)\n"
      "v_min_i32 v20, v20, v21\n"
      "v_min_i32 v22, v22, v23\n"
      "s_nop 1\n"
      "v_readfirstlane_b32 %[c2], v20\n"
      "v_readfirstlane_b32 %[c3], v22\n"
      "s_cmp_ge_i32 %[c3], 19\n"
      "s_cbranch_scc1 .LgA_27_d\n"
      "s_sleep 1\n"
      "s_sub_u32 %[t1], %[t1], 1\n"
      "s_cbranch_scc0 .LgA_27_l\n"
      ".LgA_27_d:\n"
      "v_add_u32 v17, 16384, %[pa]\n"
      "ds_write_b128 v17, v[24:27] offset:0\n"
      "ds_write_b128 v17, v[28:31] offset:1024\n"
      "ds_write_b128 v17, v[32:35] offset:2048\n"
      "ds_write_b128 v17, v[36:39] offset:3072\n"
      "s_waitcnt lgkmcnt(0)\n"
      "v_mov_b32 v16, %[lf]\n"
      "v_mov_b32 v17, 1\n"
      "ds_write_b8 v16, v17 offset:176\n"
      LTW(252, 28)
      "s_add_u32 %[t0], %[jn2], 0\n"
      "s_and_b32 %[t0], %[t0], 7\n"
      "s_lshl_b32 %[t0], %[t0], 16\n"
      "buffer_load_dwordx4 v[24:27], %[o0], %[ro], %[t0] offen nt\n"
      "s_add_u32 %[t0], %[t0], 0x1000\n"
      "buffer_load_dwordx4 v[28:31], %[o1], %[ro], %[t0] offen nt\n"
      "s_add_u32 %[t0], %[t0], 0x1000\n"
      "buffer_load_dwordx4 v[32:35], %[o0], %[ro], %[t0] offen nt\n"
      "s_add_u32 %[t0], %[t0], 0x1000\n"
      "buffer_load_dwordx4 v[36:39], %[o1], %[ro], %[t0] offen nt\n"
      "s_waitcnt vmcnt(16)\n"
      LTW(250, 29)
      "s_cmp_ge_i32 %[c0], 20\n"
      "s_cbranch_scc1 .LgA_28_d\n"
      "v_mov_b32 v16, %[lc]\n"
      "s_mov_b32 %[t1], 0x100000\n"
      ".LgA_28_l:\n"
      "ds_read_b128 v[20:23], v16 offset:16\n"
      "s_waitcnt lgkmcnt(0)\n"
      "v_min_i32 v20, v20, v21\n"
      "v_min_i32 v22, v22, v23\n"
      "s_nop 1\n"
      "v_readfirstlane_b32 %[c0], v20\n"
      "v_readfirstlane_b32 %[c1], v22\n"
      "s_cmp_ge_i32 %[c0], 20\n"
      "s_cbranch_scc1 .LgA_28_d\n"
      "s_sleep 1\n"
      "s_sub_u32 %[t1], %[t1], 1\n"
      "s_cbranch_scc0 .LgA_28_l\n"
      ".LgA_28_d:\n"
      "v_add_u32 v17, 32768, %[pa]\n"
      "ds_write_b128 v17, v[40:43] offset:0\n"
      "ds_write_b128 v17, v[44:47] offset:1024\n"
      "ds_write_b128 v17, v[48:51] offset:2048\n"
      "ds_write_b128 v17, v[52:55] offset:3072\n"
      "s_waitcnt lgkmcnt(0)\n"
      "v_mov_b32 v16, %[lf]\n"
      "v_mov_b32 v17, 1\n"
      "ds_write_b8 v16, v17 offset:180\n"
      LTW(252, 29)
      "s_mov_b32 %[t0], 4292608\n"
      "buffer_load_dwordx4 v[40:43], %[k0], %[rk], %[t0] offen nt\n"
      "buffer_load_dwordx4 v[44:47], %[k1], %[rk], %[t0] offen offset:1024 nt\n"
      "buffer_load_dwordx4 v[48:51], %[k2], %[rk], %[t0] offen offset:2048 nt\n"
      "buffer_load_dwordx4 v[52:55], %[k3], %[rk], %[t0] offen offset:3072 nt\n"
      "s_waitcnt vmcnt(16)\n"
      LTW(250, 30)
      "s_cmp_ge_i32 %[c1], 21\n"
      "s_cbranch_scc1 .LgA_29_d\n"
      "v_mov_b32 v16, %[lc]\n"
      "s_mov_b32 %[t1], 0x100000\n"
      ".LgA_29_l:\n"
      "ds_read_b128 v[20:23], v16 offset:16\n"
      "s_waitcnt lgkmcnt(0)\n"
      "v_min_i32 v20, v20, v21\n"
      "v_min_i32 v22, v22, v23\n"
      "s_nop 1\n"
      "v_readfirstlane_b32 %[c0], v20\n"
      "v_readfirstlane_b32 %[c1], v22\n"
      "s_cmp_ge_i32 %[c1], 21\n"
      "s_cbranch_scc1 .LgA_29_d\n"
      "s_sleep 1\n"
      "s_sub_u32 %[t1], %[t1], 1\n"
      "s_cbranch_scc0 .LgA_29_l\n"
      ".LgA_29_d:\n"
      "v_add_u32 v17, 49152, %[pa]\n"
      "ds_write_b128 v17, v[56:59] offset:0\n"
      "ds_write_b128 v17, v[60:63] offset:1024\n"
      "ds_write_b128 v17, v[64:67] offset:2048\n"
      "ds_write_b128 v17, v[68:71] offset:3072\n"
      "s_waitcnt lgkmcnt(0)\n"
      "v_mov_b32 v16, %[lf]\n"
      "v_mov_b32 v17, 1\n"
      "ds_write_b8 v16, v17 offset:184\n"
      LTW(252, 30)
      "s_add_u32 %[t0], %[jn2], 1\n"
      "s_and_b32 %[t0], %[t0], 7\n"
      "s_lshl_b32 %[t0], %[t0], 16\n"
      "buffer_load_dwordx4 v[56:59], %[o0], %[ro], %[t0] offen nt\n"
      "s_add_u32 %[t0], %[t0], 0x1000\n"
      "buffer_load_dwordx4 v[60:63], %[o1], %[ro], %[t0] offen nt\n"
      "s_add_u32 %[t0], %[t0], 0x1000\n"
      "buffer_load_dwordx4 v[64:67], %[o0], %[ro], %[t0] offen nt\n"
      "s_add_u32 %[t0], %[t0], 0x1000\n"
      "buffer_load_dwordx4 v[68:71], %[o1], %[ro], %[t0] offen nt\n"
      "s_waitcnt vmcnt(16)\n"
      LTW(250, 31)
      "s_cmp_ge_i32 %[c2], 22\n"
      "s_cbranch_scc1 .LgA_30_d\n"
      "v_mov_b32 v16, %[lc]\n"
      "s_mov_b32 %[t1], 0x100000\n"
      ".LgA_30_l:\n"
      "ds_read_b128 v[20:23], v16 offset:32\n"
      "s_waitcnt lgkmcnt(0)\n"
      "v_min_i32 v20, v20, v21\n"
      "v_min_i32 v22, v22, v23\n"
      "s_nop 1\n"
      "v_readfirstlane_b32 %[c2], v20\n"
      "v_readfirstlane_b32 %[c3], v22\n"
      "s_cmp_ge_i32 %[c2], 22\n"
      "s_cbranch_scc1 .LgA_30_d\n"
      "s_sleep 1\n"
      "s_sub_u32 %[t1], %[t1], 1\n"
      "s_cbranch_scc0 .LgA_30_l\n"
      ".LgA_30_d:\n"
      "v_add_u32 v17, 65536, %[pa]\n"
      "ds_write_b128 v17, v[72:75] offset:0\n"
      "ds_write_b128 v17, v[76:79] offset:1024\n"
      "ds_write_b128 v17, v[80:83] offset:2048\n"
      "ds_write_b128 v17, v[84:87] offset:3072\n"
      "s_waitcnt lgkmcnt(0)\n"
      "v_mov_b32 v16, %[lf]\n"
      "v_mov_b32 v17, 1\n"
      "ds_write_b8 v16, v17 offset:188\n"
      LTW(252, 31)
      "s_mov_b32 %[t0], 98304\n"
      "buffer_load_dwordx4 v[72:75], %[v0], %[rv], %[t0] offen nt\n"
      "buffer_load_dwordx4 v[76:79], %[v1], %[rv], %[t0] offen offset:1024 nt\n"
      "buffer_load_dwordx4 v[80:83], %[v0], %[rv], %[t0] offen offset:2048 nt\n"
      "buffer_load_dwordx4 v[84:87], %[v1], %[rv], %[t0] offen offset:3072 nt\n"
      "s_waitcnt vmcnt(16)\n"
      LTW(250, 32)
      "s_cmp_ge_i32 %[c3], 23\n"
      "s_cbranch_scc1 .LgA_31_d\n"
      "v_mov_b32 v16, %[lc]\n"
      "s_mov_b32 %[t1], 0x100000\n"
      ".LgA_31_l:\n"
      "ds_read_b128 v[20:23], v16 offset:32\n"
      "s_waitcnt lgkmcnt(0)\n"
      "v_min_i32 v20, v20, v21\n"
      "v_min_i32 v22, v22, v23\n"
      "s_nop 1\n"
      "v_readfirstlane_b32 %[c2], v20\n"
      "v_readfirstlane_b32 %[c3], v22\n"
      "s_cmp_ge_i32 %[c3], 23\n"
      "s_cbranch_scc1 .LgA_31_d\n"
      "s_sleep 1\n"
      "s_sub_u32 %[t1], %[t1], 1\n"
      "s_cbranch_scc0 .LgA_31_l\n"
      ".LgA_31_d:\n"
      "v_add_u32 v17, 81920, %[pa]\n"
      "ds_write_b128 v17, v[88:91] offset:0\n"
      "ds_write_b128 v17, v[92:95] offset:1024\n"
      "ds_write_b128 v17, v[96:99] offset:2048\n"
      "ds_write_b128 v17, v[100:103] offset:3072\n"
      "s_waitcnt lgkmcnt(0)\n"
      "v_mov_b32 v16, %[lf]\n"
      "v_mov_b32 v17, 1\n"
      "ds_write_b8 v16, v17 offset:192\n"
      LTW(252, 32)
      "s_add_u32 %[t0], %[jn2], 2\n"
      "s_and_b32 %[t0], %[t0], 7\n"
      "s_lshl_b32 %[t0], %[t0], 16\n"
      "buffer_load_dwordx4 v[88:91], %[o0], %[ro], %[t0] offen nt\n"
      "s_add_u32 %[t0], %[t0], 0x1000\n"
      "buffer_load_dwordx4 v[92:95], %[o1], %[ro], %[t0] offen nt\n"
      "s_add_u32 %[t0], %[t0], 0x1000\n"
      "buffer_load_dwordx4 v[96:99], %[o0], %[ro], %[t0] offen nt\n"
      "s_add_u32 %[t0], %[t0], 0x1000\n"
      "buffer_load_dwordx4 v[100:103], %[o1], %[ro], %[t0] offen nt\n"
      "s_mov_b32 %[t0], 4292608\n"
      "buffer_load_dwordx4 v[104:107], %[v0], %[rv], %[t0] offen nt\n"
      "buffer_load_dwordx4 v[108:111], %[v1], %[rv], %[t0] offen offset:1024 nt\n"
      "buffer_load_dwordx4 v[112:115], %[v0], %[rv], %[t0] offen offset:2048 nt\n"
      "buffer_load_dwordx4 v[116:119], %[v1], %[rv], %[t0] offen offset:3072 nt\n"
      "s_waitcnt vmcnt(16)\n"
      LTW(250, 33)
      "s_cmp_ge_i32 %[c0], 24\n"
      "s_cbranch_scc1 .LgA_32_d\n"
      "v_mov_b32 v16, %[lc]\n"
      "s_mov_b32 %[t1], 0x100000\n"
      ".LgA_32_l:\n"
      "ds_read_b128 v[20:23], v16 offset:16\n"
      "s_waitcnt lgkmcnt(0)\n"
      "v_min_i32 v20, v20, v21\n"
      "v_min_i32 v22, v22, v23\n"
      "s_nop 1\n"
      "v_readfirstlane_b32 %[c0], v20\n"
      "v_readfirstlane_b32 %[c1], v22\n"
      "s_cmp_ge_i32 %[c0], 24\n"
      "s_cbranch_scc1 .LgA_32_d\n"
      "s_sleep 1\n"
      "s_sub_u32 %[t1], %[t1], 1\n"
      "s_cbranch_scc0 .LgA_32_l\n"
      ".LgA_32_d:\n"
      "v_add_u32 v17, 98304, %[pa]\n"
      "ds_write_b128 v17, v[40:43] offset:0\n"
      "ds_write_b128 v17, v[44:47] offset:1024\n"
      "ds_write_b128 v17, v[48:51] offset:2048\n"
      "ds_write_b128 v17, v[52:55] offset:3072\n"
      "s_waitcnt lgkmcnt(0)\n"
      "v_mov_b32 v16, %[lf]\n"
      "v_mov_b32 v17, 1\n"
      "ds_write_b8 v16, v17 offset:196\n"
      LTW(252, 33)
      "s_add_u32 %[t0], %[jn2], 3\n"
      "s_and_b32 %[t0], %[t0], 7\n"
      "s_lshl_b32 %[t0], %[t0], 16\n"
      "buffer_load_dwordx4 v[40:43], %[o0], %[ro], %[t0] offen nt\n"
      "s_add_u32 %[t0], %[t0], 0x1000\n"
      "buffer_load_dwordx4 v[44:47], %[o1], %[ro], %[t0] offen nt\n"
      "s_add_u32 %[t0], %[t0], 0x1000\n"
      "buffer_load_dwordx4 v[48:51], %[o0], %[ro], %[t0] offen nt\n"
      "s_add_u32 %[t0], %[t0], 0x1000\n"
      "buffer_load_dwordx4 v[52:55], %[o1], %[ro], %[t0] offen nt\n"
      "s_mov_b32 %[t0], 114688\n"
      "buffer_load_dwordx4 v[120:123], %[k0], %[rk], %[t0] offen nt\n"
      "buffer_load_dwordx4 v[124:127], %[k1], %[rk], %[t0] offen offset:1024 nt\n"
      "buffer_load_dwordx4 v[128:131], %[k2], %[rk], %[t0] offen offset:2048 nt\n"
      "buffer_load_dwordx4 v[132:135], %[k3], %[rk], %[t0] offen offset:3072 nt\n"
      "s_waitcnt vmcnt(16)\n"
      LTW(250, 34)
      "s_cmp_ge_i32 %[c1], 25\n"
      "s_cbranch_scc1 .LgA_33_d\n"
      "v_mov_b32 v16, %[lc]\n"
      "s_mov_b32 %[t1], 0x100000\n"
      ".LgA_33_l:\n"
      "ds_read_b128 v[20:23], v16 offset:16\n"
      "s_waitcnt lgkmcnt(0)\n"
      "v_min_i32 v20, v20, v21\n"
      "v_min_i32 v22, v22, v23\n"
      "s_nop 1\n"
      "v_readfirstlane_b32 %[c0], v20\n"
      "v_readfirstlane_b32 %[c1], v22\n"
      "s_cmp_ge_i32 %[c1], 25\n"
      "s_cbranch_scc1 .LgA_33_d\n"
      "s_sleep 1\n"
      "s_sub_u32 %[t1], %[t1], 1\n"
      "s_cbranch_scc0 .LgA_33_l\n"
      ".LgA_33_d:\n"
      "v_add_u32 v17, 114688, %[pa]\n"
      "ds_write_b128 v17, v[72:75] offset:0\n"
      "ds_write_b128 v17, v[76:79] offset:1024\n"
      "ds_write_b128 v17, v[80:83] offset:2048\n"
      "ds_write_b128 v17, v[84:87] offset:3072\n"
      "s_waitcnt lgkmcnt(0)\n"
      "v_mov_b32 v16, %[lf]\n"
      "v_mov_b32 v17, 1\n"
      "ds_write_b8 v16, v17 offset:200\n"
      LTW(252, 34)
      "s_add_u32 %[t0], %[jn2], 4\n"
      "s_and_b32 %[t0], %[t0], 7\n"
      "s_lshl_b32 %[t0], %[t0], 16\n"
      "buffer_load_dwordx4 v[72:75], %[o0], %[ro], %[t0] offen nt\n"
      "s_add_u32 %[t0], %[t0], 0x1000\n"
      "buffer_load_dwordx4 v[76:79], %[o1], %[ro], %[t0] offen nt\n"
      "s_add_u32 %[t0], %[t0], 0x1000\n"
      "buffer_load_dwordx4 v[80:83], %[o0], %[ro], %[t0] offen nt\n"
      "s_add_u32 %[t0], %[t0], 0x1000\n"
      "buffer_load_dwordx4 v[84:87], %[o1], %[ro], %[t0] offen nt\n"
      "s_mov_b32 %[t0], 4308992\n"
      "buffer_load_dwordx4 v[136:139], %[k0], %[rk], %[t0] offen nt\n"
      "buffer_load_dwordx4 v[140:143], %[k1], %[rk], %[t0] offen offset:1024 nt\n"
      "buffer_load_dwordx4 v[144:147], %[k2], %[rk], %[t0] offen offset:2048 nt\n"
      "buffer_load_dwordx4 v[148:151], %[k3], %[rk], %[t0] offen offset:3072 nt\n"
      "s_waitcnt vmcnt(16)\n"
      LTW(250, 35)
      "s_cmp_ge_i32 %[c2], 26\n"
      "s_cbranch_scc1 .LgA_34_d\n"
      "v_mov_b32 v16, %[lc]\n"
      "s_mov_b32 %[t1], 0x100000\n"
      ".LgA_34_l:\n"
      "ds_read_b128 v[20:23], v16 offset:32\n"
      "s_waitcnt lgkmcnt(0)\n"
      "v_min_i32 v20, v20, v21\n"
      "v_min_i32 v22, v22, v23\n"
      "s_nop 1\n"
      "v_readfirstlane_b32 %[c2], v20\n"
      "v_readfirstlane_b32 %[c3], v22\n"
      "s_cmp_ge_i32 %[c2], 26\n"
      "s_cbranch_scc1 .LgA_34_d\n"
      "s_sleep 1\n"
      "s_sub_u32 %[t1], %[t1], 1\n"
      "s_cbranch_scc0 .LgA_34_l\n"
      ".LgA_34_d:\n"
      "v_add_u32 v17, 131072, %[pa]\n"
      "ds_write_b128 v17, v[104:107] offset:0\n"
      "ds_write_b128 v17, v[108:111] offset:1024\n"
      "ds_write_b128 v17, v[112:115] offset:2048\n"
      "ds_write_b128 v17, v[116:119] offset:3072\n"
      "s_waitcnt lgkmcnt(0)\n"
      "v_mov_b32 v16, %[lf]\n"
      "v_mov_b32 v17, 1\n"
      "ds_write_b8 v16, v17 offset:204\n"
      LTW(252, 35)
      LTW(250, 40)
      "s_cmp_ge_i32 %[c3], 31\n"
      "s_cbranch_scc1 .LgA_35_d\n"
      "v_mov_b32 v16, %[lc]\n"
      "s_mov_b32 %[t1], 0x100000\n"
      ".LgA_35_l:\n"
      "ds_read_b128 v[20:23], v16 offset:32\n"
      "s_waitcnt lgkmcnt(0)\n"
      "v_min_i32 v20, v20, v21\n"
      "v_min_i32 v22, v22, v23\n"
      "s_nop 1\n"
      "v_readfirstlane_b32 %[c2], v20\n"
      "v_readfirstlane_b32 %[c3], v22\n"
      "s_cmp_ge_i32 %[c3], 31\n"
      "s_cbranch_scc1 .LgA_35_d\n"
      "s_sleep 1\n"
      "s_sub_u32 %[t1], %[t1], 1\n"
      "s_cbranch_scc0 .LgA_35_l\n"
      ".LgA_35_d:\n"
      "v_add_u32 v17, 65536, %[pa]\n"
      "ds_write_b128 v17, v[24:27] offset:0\n"
      "ds_write_b128 v17, v[28:31] offset:1024\n"
      "ds_write_b128 v17, v[32:35] offset:2048\n"
      "ds_write_b128 v17, v[36:39] offset:3072\n"
      "s_waitcnt lgkmcnt(0)\n"
      "s_add_u32 %[t0], %[jn2], 5\n"
      "s_and_b32 %[t0], %[t0], 7\n"
      "s_lshl_b32 %[t0], %[t0], 16\n"
      "buffer_load_dwordx4 v[24:27], %[o0], %[ro], %[t0] offen nt\n"
      "s_add_u32 %[t0], %[t0], 0x1000\n"
      "buffer_load_dwordx4 v[28:31], %[o1], %[ro], %[t0] offen nt\n"
      "s_add_u32 %[t0], %[t0], 0x1000\n"
      "buffer_load_dwordx4 v[32:35], %[o0], %[ro], %[t0] offen nt\n"
      "s_add_u32 %[t0], %[t0], 0x1000\n"
      "buffer_load_dwordx4 v[36:39], %[o1], %[ro], %[t0] offen nt\n"
      "s_mov_b32 %[t0], 114688\n"
      "buffer_load_dwordx4 v[104:107], %[v0], %[rv], %[t0] offen nt\n"
      "buffer_load_dwordx4 v[108:111], %[v1], %[rv], %[t0] offen offset:1024 nt\n"
      "buffer_load_dwordx4 v[112:115], %[v0], %[rv], %[t0] offen offset:2048 nt\n"
      "buffer_load_dwordx4 v[116:119], %[v1], %[rv], %[t0] offen offset:3072 nt\n"
      "s_waitcnt vmcnt(16)\n"
      LTW(250, 36)
      "v_add_u32 v17, 0, %[pa]\n"
      "ds_write_b128 v17, v[120:123] offset:0\n"
      "ds_write_b128 v17, v[124:127] offset:1024\n"
      "ds_write_b128 v17, v[128:131] offset:2048\n"
      "ds_write_b128 v17, v[132:135] offset:3072\n"
      "s_waitcnt lgkmcnt(0)\n"
      "v_mov_b32 v16, %[lf]\n"
      "v_mov_b32 v17, 1\n"
      "ds_write_b8 v16, v17 offset:208\n"
      LTW(252, 36)
      LTW(250, 41)
      "s_cmp_ge_i32 %[c0], 32\n"
      "s_cbranch_scc1 .LgA_36_d\n"
      "v_mov_b32 v16, %[lc]\n"
      "s_mov_b32 %[t1], 0x100000\n"
      ".LgA_36_l:\n"
      "ds_read_b128 v[20:23], v16 offset:16\n"
      "s_waitcnt lgkmcnt(0)\n"
      "v_min_i32 v20, v20, v21\n"
      "v_min_i32 v22, v22, v23\n"
      "s_nop 1\n"
      "v_readfirstlane_b32 %[c0], v20\n"
      "v_readfirstlane_b32 %[c1], v22\n"
      "s_cmp_ge_i32 %[c0], 32\n"
      "s_cbranch_scc1 .LgA_36_d\n"
      "s_sleep 1\n"
      "s_sub_u32 %[t1], %[t1], 1\n"
      "s_cbranch_scc0 .LgA_36_l\n"
      ".LgA_36_d:\n"
      "v_add_u32 v17, 81920, %[pa]\n"
      "ds_write_b128 v17, v[56:59] offset:0\n"
      "ds_write_b128 v17, v[60:63] offset:1024\n"
      "ds_write_b128 v17, v[64:67] offset:2048\n"
      "ds_write_b128 v17, v[68:71] offset:3072\n"
      "s_waitcnt lgkmcnt(0)\n"
      "s_add_u32 %[t0], %[jn2], 6\n"
      "s_and_b32 %[t0], %[t0], 7\n"
      "s_lshl_b32 %[t0], %[t0], 16\n"
      "buffer_load_dwordx4 v[56:59], %[o0], %[ro], %[t0] offen nt\n"
      "s_add_u32 %[t0], %[t0], 0x1000\n"
      "buffer_load_dwordx4 v[60:63], %[o1], %[ro], %[t0] offen nt\n"
      "s_add_u32 %[t0], %[t0], 0x1000\n"
      "buffer_load_dwordx4 v[64:67], %[o0], %[ro], %[t0] offen nt\n"
      "s_add_u32 %[t0], %[t0], 0x1000\n"
      "buffer_load_dwordx4 v[68:71], %[o1], %[ro], %[t0] offen nt\n"
      "s_mov_b32 %[t0], 4308992\n"
      "buffer_load_dwordx4 v[120:123], %[v0], %[rv], %[t0] offen nt\n"
      "buffer_load_dwordx4 v[124:127], %[v1], %[rv], %[t0] offen offset:1024 nt\n"
      "buffer_load_dwordx4 v[128:131], %[v0], %[rv], %[t0] offen offset:2048 nt\n"
      "buffer_load_dwordx4 v[132:135], %[v1], %[rv], %[t0] offen offset:3072 nt\n"
      "s_waitcnt vmcnt(16)\n"
      LTW(250, 37)
      "v_add_u32 v17, 16384, %[pa]\n"
      "ds_write_b128 v17, v[136:139] offset:0\n"
      "ds_write_b128 v17, v[140:143] offset:1024\n"
      "ds_write_b128 v17, v[144:147] offset:2048\n"
      "ds_write_b128 v17, v[148:151] offset:3072\n"
      "s_waitcnt lgkmcnt(0)\n"
      "v_mov_b32 v16, %[lf]\n"
      "v_mov_b32 v17, 1\n"
      "ds_write_b8 v16, v17 offset:212\n"
      LTW(252, 37)
      LTW(250, 42)
      "s_cmp_ge_i32 %[c1], 33\n"
      "s_cbranch_scc1 .LgA_37_d\n"
      "v_mov_b32 v16, %[lc]\n"
      "s_mov_b32 %[t1], 0x100000\n"
      ".LgA_37_l:\n"
      "ds_read_b128 v[20:23], v16 offset:16\n"
      "s_waitcnt lgkmcnt(0)\n"
      "v_min_i32 v20, v20, v21\n"
      "v_min_i32 v22, v22, v23\n"
      "s_nop 1\n"
      "v_readfirstlane_b32 %[c0], v20\n"
      "v_readfirstlane_b32 %[c1], v22\n"
      "s_cmp_ge_i32 %[c1], 33\n"
      "s_cbranch_scc1 .LgA_37_d\n"
      "s_sleep 1\n"
      "s_sub_u32 %[t1], %[t1], 1\n"
      "s_cbranch_scc0 .LgA_37_l\n"
      ".LgA_37_d:\n"
      "v_add_u32 v17, 98304, %[pa]\n"
      "ds_write_b128 v17, v[88:91] offset:0\n"
      "ds_write_b128 v17, v[92:95] offset:1024\n"
      "ds_write_b128 v17, v[96:99] offset:2048\n"
      "ds_write_b128 v17, v[100:103] offset:3072\n"
      "s_waitcnt lgkmcnt(0)\n"
      "s_add_u32 %[t0], %[jn2], 7\n"
      "s_and_b32 %[t0], %[t0], 7\n"
      "s_lshl_b32 %[t0], %[t0], 16\n"
      "buffer_load_dwordx4 v[88:91], %[o0], %[ro], %[t0] offen nt\n"
      "s_add_u32 %[t0], %[t0], 0x1000\n"
      "buffer_load_dwordx4 v[92:95], %[o1], %[ro], %[t0] offen nt\n"
      "s_add_u32 %[t0], %[t0], 0x1000\n"
      "buffer_load_dwordx4 v[96:99], %[o0], %[ro], %[t0] offen nt\n"
      "s_add_u32 %[t0], %[t0], 0x1000\n"
      "buffer_load_dwordx4 v[100:103], %[o1], %[ro], %[t0] offen nt\n"
      "s_waitcnt vmcnt(12)\n"
      LTW(250, 38)
      "v_add_u32 v17, 32768, %[pa]\n"
      "ds_write_b128 v17, v[104:107] offset:0\n"
      "ds_write_b128 v17, v[108:111] offset:1024\n"
      "ds_write_b128 v17, v[112:115] offset:2048\n"
      "ds_write_b128 v17, v[116:119] offset:3072\n"
      "s_waitcnt lgkmcnt(0)\n"
      "v_mov_b32 v16, %[lf]\n"
      "v_mov_b32 v17, 1\n"
      "ds_write_b8 v16, v17 offset:216\n"
      LTW(252, 38)
      LTW(250, 43)
      "s_cmp_ge_i32 %[c2], 34\n"
      "s_cbranch_scc1 .LgA_38_d\n"
      "v_mov_b32 v16, %[lc]\n"
      "s_mov_b32 %[t1], 0x100000\n"
      ".LgA_38_l:\n"
      "ds_read_b128 v[20:23], v16 offset:32\n"
      "s_waitcnt lgkmcnt(0)\n"
      "v_min_i32 v20, v20, v21\n"
      "v_min_i32 v22, v22, v23\n"
      "s_nop 1\n"
      "v_readfirstlane_b32 %[c2], v20\n"
      "v_readfirstlane_b32 %[c3], v22\n"
      "s_cmp_ge_i32 %[c2], 34\n"
      "s_cbranch_scc1 .LgA_38_d\n"
      "s_sleep 1\n"
      "s_sub_u32 %[t1], %[t1], 1\n"
      "s_cbranch_scc0 .LgA_38_l\n"
      ".LgA_38_d:\n"
      "v_add_u32 v17, 114688, %[pa]\n"
      "ds_write_b128 v17, v[40:43] offset:0\n"
      "ds_write_b128 v17, v[44:47] offset:1024\n"
      "ds_write_b128 v17, v[48:51] offset:2048\n"
      "ds_write_b128 v17, v[52:55] offset:3072\n"
      "s_waitcnt lgkmcnt(0)\n"
      "s_waitcnt vmcnt(4)\n"
      LTW(250, 39)
      "v_add_u32 v17, 49152, %[pa]\n"
      "ds_write_b128 v17, v[120:123] offset:0\n"
      "ds_write_b128 v17, v[124:127] offset:1024\n"
      "ds_write_b128 v17, v[128:131] offset:2048\n"
      "ds_write_b128 v17, v[132:135] offset:3072\n"
      "s_waitcnt lgkmcnt(0)\n"
      "v_mov_b32 v16, %[lf]\n"
      "v_mov_b32 v17, 1\n"
      "ds_write_b8 v16, v17 offset:220\n"
      LTW(252, 39)
      "v_mov_b32 v16, %[lf]\n"
      "v_mov_b32 v17, 1\n"
      "ds_write_b8 v16, v17 offset:224\n"
      LTW(252, 40)
      "v_mov_b32 v16, %[lf]\n"
      "v_mov_b32 v17, 1\n"
      "ds_write_b8 v16, v17 offset:228\n"
      LTW(252, 41)
      "v_mov_b32 v16, %[lf]\n"
      "v_mov_b32 v17, 1\n"
      "ds_write_b8 v16, v17 offset:232\n"
      LTW(252, 42)
      "v_mov_b32 v16, %[lf]\n"
      "v_mov_b32 v17, 1\n"
      "ds_write_b8 v16, v17 offset:236\n"
      LTW(252, 43)
      LTW(250, 44)
      "s_cmp_ge_i32 %[c3], 35\n"
      "s_cbranch_scc1 .LgA_39_d\n"
      "v_mov_b32 v16, %[lc]\n"
      "s_mov_b32 %[t1], 0x100000\n"
      ".LgA_39_l:\n"
      "ds_read_b128 v[20:23], v16 offset:32\n"
      "s_waitcnt lgkmcnt(0)\n"
      "v_min_i32 v20, v20, v21\n"
      "v_min_i32 v22, v22, v23\n"
      "s_nop 1\n"
      "v_readfirstlane_b32 %[c2], v20\n"
      "v_readfirstlane_b32 %[c3], v22\n"
      "s_cmp_ge_i32 %[c3], 35\n"
      "s_cbranch_scc1 .LgA_39_d\n"
      "s_sleep 1\n"
      "s_sub_u32 %[t1], %[t1], 1\n"
      "s_cbranch_scc0 .LgA_39_l\n"
      ".LgA_39_d:\n"
      "v_add_u32 v17, 131072, %[pa]\n"
      "ds_write_b128 v17, v[72:75] offset:0\n"
      "ds_write_b128 v17, v[76:79] offset:1024\n"
      "ds_write_b128 v17, v[80:83] offset:2048\n"
      "ds_write_b128 v17, v[84:87] offset:3072\n"
      "s_waitcnt lgkmcnt(0)\n"
      "v_mov_b32 v16, %[lf]\n"
      "v_mov_b32 v17, 1\n"
      "ds_write_b8 v16, v17 offset:240\n"
      LTW(252, 44)
      LTW(250, 45)
      "s_cmp_ge_i32 %[c0], 36\n"
      "s_cbranch_scc1 .LgA_40_d\n"
      "v_mov_b32 v16, %[lc]\n"
      "s_mov_b32 %[t1], 0x100000\n"
      ".LgA_40_l:\n"
      "ds_read_b128 v[20:23], v16 offset:16\n"
      "s_waitcnt lgkmcnt(0)\n"
      "v_min_i32 v20, v20, v21\n"
      "v_min_i32 v22, v22, v23\n"
      "s_nop 1\n"
      "v_readfirstlane_b32 %[c0], v20\n"
      "v_readfirstlane_b32 %[c1], v22\n"
      "s_cmp_ge_i32 %[c0], 36\n"
      "s_cbranch_scc1 .LgA_40_d\n"
      "s_sleep 1\n"
      "s_sub_u32 %[t1], %[t1], 1\n"
      "s_cbranch_scc0 .LgA_40_l\n"
      ".LgA_40_d:\n"
      "v_add_u32 v17, 0, %[pa]\n"
      "ds_write_b128 v17, v[24:27] offset:0\n"
      "ds_write_b128 v17, v[28:31] offset:1024\n"
      "ds_write_b128 v17, v[32:35] offset:2048\n"
      "ds_write_b128 v17, v[36:39] offset:3072\n"
      "s_waitcnt lgkmcnt(0)\n"
      "v_mov_b32 v16, %[lf]\n"
      "v_mov_b32 v17, 1\n"
      "ds_write_b8 v16, v17 offset:244\n"
      LTW(252, 45)
      LTW(250, 46)
      "s_cmp_ge_i32 %[c1], 37\n"
      "s_cbranch_scc1 .LgA_41_d\n"
      "v_mov_b32 v16, %[lc]\n"
      "s_mov_b32 %[t1], 0x100000\n"
      ".LgA_41_l:\n"
      "ds_read_b128 v[20:23], v16 offset:16\n"
      "s_waitcnt lgkmcnt(0)\n"
      "v_min_i32 v20, v20, v21\n"
      "v_min_i32 v22, v22, v23\n"
      "s_nop 1\n"
      "v_readfirstlane_b32 %[c0], v20\n"
      "v_readfirstlane_b32 %[c1], v22\n"
      "s_cmp_ge_i32 %[c1], 37\n"
      "s_cbranch_scc1 .LgA_41_d\n"
      "s_sleep 1\n"
      "s_sub_u32 %[t1], %[t1], 1\n"
      "s_cbranch_scc0 .LgA_41_l\n"
      ".LgA_41_d:\n"
      "v_add_u32 v17, 16384, %[pa]\n"
      "ds_write_b128 v17, v[56:59] offset:0\n"
      "ds_write_b128 v17, v[60:63] offset:1024\n"
      "ds_write_b128 v17, v[64:67] offset:2048\n"
      "ds_write_b128 v17, v[68:71] offset:3072\n"
      "s_waitcnt lgkmcnt(0)\n"
      "v_mov_b32 v16, %[lf]\n"
      "v_mov_b32 v17, 1\n"
      "ds_write_b8 v16, v17 offset:248\n"
      LTW(252, 46)
      "s_waitcnt vmcnt(0)\n"
      LTW(250, 47)
      "s_cmp_ge_i32 %[c2], 38\n"
      "s_cbranch_scc1 .LgA_42_d\n"
      "v_mov_b32 v16, %[lc]\n"
      "s_mov_b32 %[t1], 0x100000\n"
      ".LgA_42_l:\n"
      "ds_read_b128 v[20:23], v16 offset:32\n"
      "s_waitcnt lgkmcnt(0)\n"
      "v_min_i32 v20, v20, v21\n"
      "v_min_i32 v22, v22, v23\n"
      "s_nop 1\n"
      "v_readfirstlane_b32 %[c2], v20\n"
      "v_readfirstlane_b32 %[c3], v22\n"
      "s_cmp_ge_i32 %[c2], 38\n"
      "s_cbranch_scc1 .LgA_42_d\n"
      "s_sleep 1\n"
      "s_sub_u32 %[t1], %[t1], 1\n"
      "s_cbranch_scc0 .LgA_42_l\n"
      ".LgA_42_d:\n"
      "v_add_u32 v17, 32768, %[pa]\n"
      "ds_write_b128 v17, v[88:91] offset:0\n"
      "ds_write_b128 v17, v[92:95] offset:1024\n"
      "ds_write_b128 v17, v[96:99] offset:2048\n"
      "ds_write_b128 v17, v[100:103] offset:3072\n"
      "s_waitcnt lgkmcnt(0)\n"
      "v_mov_b32 v16, %[lf]\n"
      "v_mov_b32 v17, 1\n"
      "ds_write_b8 v16, v17 offset:252\n"
      LTW(252, 47)
      "s_waitcnt vmcnt(0) lgkmcnt(0)\n"
      LTE
      LTS(196)
      LOADER_OPS);
    {
      typedef __attribute__((ext_vector_type(32))) unsigned u32x32;
      typedef __attribute__((ext_vector_type(16))) unsigned u32x16;
      const int r = xg * 4 + hw, sw = r >> 3, nb = r & 7;
      const u32x4 r_sf = raw_rsrc(ws + WS_CNT + 65536 + ((pb * 4 + jn) * 4 + sw) * 128, 64);
      const u32x4 r_s = raw_rsrc(ws + WS_SLAB + (size_t)(pb * 4 + jn) * 8 * 49152 + r * 1536, 7 * 49152 + 1536);
      const int fl = lane_id_v(), l16 = fl * 16, l8 = fl * 8, fo2 = (fl & 7) * 8, lm = lbase + CTRL + 48 + hw * 4;
      u32x32 ta; u32x16 tb;
#define RK(k, a, b)                                                                                                      \
      "s_bitcmp1_b32 %[m], " #k "\ns_cbranch_scc0 .Lrd" #k "\ns_mov_b32 %[so], " #k "*49152\n"                          \
      "buffer_load_dwordx4 v[" #a ":" #a "+3], %[l16], %[rs], %[so] offen sc1\n"                                          \
      "buffer_load_dwordx2 v[" #b ":" #b "+1], %[l8], %[rs], %[so] offen offset:1024 sc1\n.Lrd" #k ":\n"
      asm volatile(
      "s_mov_b32 %[ct], 0x100000\n.Lrwl:\ns_sleep 2\nds_read_b32 v248, %[lm]\ns_waitcnt lgkmcnt(0)\nv_readfirstlane_b32 %[m], v248\n"
      "s_cmp_eq_u32 %[m], 1\ns_cbranch_scc1 .Lrwe\ns_sub_u32 %[ct], %[ct], 1\ns_cbranch_scc0 .Lrwl\n.Lrwe:\n"
      "s_mov_b32 %[dn], 0\ns_mov_b32 %[ct], 0x100000\n.Lrdl:\n"
      "buffer_load_dwordx2 v[248:249], %[fo], %[rf], 0 offen sc1\ns_waitcnt vmcnt(0)\n"
      "v_cmp_eq_u64 vcc, %[tg], v[248:249]\ns_and_b32 %[m], vcc_lo, 255\ns_andn2_b32 %[m], %[m], %[dn]\n"
      RK(0, 32, 64) RK(1, 36, 66) RK(2, 40, 68) RK(3, 44, 70) RK(4, 48, 72) RK(5, 52, 74) RK(6, 56, 76) RK(7, 60, 78)
      "s_or_b32 %[dn], %[dn], %[m]\ns_cmp_eq_u32 %[dn], 255\ns_cbranch_scc1 .Lrde\n"
      "s_sleep 1\ns_sub_u32 %[ct], %[ct], 1\ns_cbranch_scc0 .Lrdl\n.Lrde:\n"
      LTS(200)
      "s_waitcnt vmcnt(0)\n"
      : "={v[32:63]}"(ta), "={v[64:79]}"(tb), [dn] "=&s"(c0), [ct] "=&s"(c1), [m] "=&s"(c2), [so] "=&s"(c3)
      : [fo] "v"(fo2), [rf] "s"(r_sf), [tg] "s"(tag), [l16] "v"(l16), [l8] "v"(l8), [lm] "v"(lm), [rs] "s"(r_s), [rt] "s"(r_lt)
      : "memory", "vcc", "scc", "v16", "v248", "v249");
#undef RK
      bf16* const op = out + (size_t)(bp * 192 + sw * 48 + (fl & 15)) * (2 * DM) + p * DM + jn * 128 + 16 * nb + 4 * (fl >> 4);
#pragma unroll
      for (int e = 0; e < 3; ++e) {
        float a[4];
#pragma unroll
        for (int i = 0; i < 4; ++i) {
          float t[8];
#pragma unroll
          for (int k = 0; k < 8; ++k) {
            const unsigned w = e < 2 ? ta[4 * k + 2 * e + (i >> 1)] : tb[2 * k + (i >> 1)];
            t[k] = __uint_as_float((i & 1) ? (w & 0xffff0000u) : (w << 16));
          }
          a[i] = ((t[0] + t[1]) + (t[2] + t[3])) + ((t[4] + t[5]) + (t[6] + t[7]));
        }
        *(u32x2*)(op + (size_t)e * 16 * 2 * DM) = (u32x2){cvtpk(a[0], a[1]), cvtpk(a[2], a[3])};
      }
    }
#undef LTS
    asm volatile(".LendL:\n");
    return;
  }

  if (wid == 0) *(volatile lds_int*)(size_t)(unsigned)(lbase + CTRL + lane_id() * 4) = lane_id() < 16 ? -1 : 0;
  int junk = 0;
  {
    int csz;
    asm volatile("s_mov_b32 %0, .LendC-.Lpf0\ns_max_u32 %0, %0, .LendL-.Lpf0\n" : "=s"(csz) : : "scc");
    const u32x4 r_c = raw_rsrc((const void*)pc0, csz);
    const int co = (((jj * 4 + wid) & 63) << 10) + fresh(lane_id()) * 16;
    asm volatile("buffer_load_dword %0, %1, %2, 0 offen\n" : "=&v"(junk) : "v"(co), "s"(r_c) : "memory");
  }
  const int beta = wid >> 1;
  f32x4 acc[8][3];
  bf16x8 qf[4][3];
  {
    const int l = fresh(lane_id()), q = l & 15, g = l >> 4;
    typedef __attribute__((ext_vector_type(32))) float f32x32;
    const int wb = lbase + UBUF + (8 * g + (q >> 2)) * 256 + ((4 * (g & 1) + (q >> 2)) << 5) + ((q & 3) << 3);
    const int xv = lbase + UBUF + 16384 + (48 * wid + q) * 128 + ((g ^ (q >> 1)) << 4);
    f32x32 a0, a1, a2;
    int cb, bu, t0, t1;
#define QTS(k)
#define QTS_O
    asm volatile(
      "v_mov_b32 v0, 0\n"
      "v_mov_b32 v1, 0\n"
      "v_mov_b32 v2, 0\n"
      "v_mov_b32 v3, 0\n"
      "v_mov_b32 v4, 0\n"
      "v_mov_b32 v5, 0\n"
      "v_mov_b32 v6, 0\n"
      "v_mov_b32 v7, 0\n"
      "v_mov_b32 v8, 0\n"
      "v_mov_b32 v9, 0\n"
      "v_mov_b32 v10, 0\n"
      "v_mov_b32 v11, 0\n"
      "v_mov_b32 v12, 0\n"
      "v_mov_b32 v13, 0\n"
      "v_mov_b32 v14, 0\n"
      "v_mov_b32 v15, 0\n"
      "v_mov_b32 v16, 0\n"
      "v_mov_b32 v17, 0\n"
      "v_mov_b32 v18, 0\n"
      "v_mov_b32 v19, 0\n"
      "v_mov_b32 v20, 0\n"
      "v_mov_b32 v21, 0\n"
      "v_mov_b32 v22, 0\n"
      "v_mov_b32 v23, 0\n"
      "v_mov_b32 v24, 0\n"
      "v_mov_b32 v25, 0\n"
      "v_mov_b32 v26, 0\n"
      "v_mov_b32 v27, 0\n"
      "v_mov_b32 v28, 0\n"
      "v_mov_b32 v29, 0\n"
      "v_mov_b32 v30, 0\n"
      "v_mov_b32 v31, 0\n"
      "v_mov_b32 v32, 0\n"
      "v_mov_b32 v33, 0\n"
      "v_mov_b32 v34, 0\n"
      "v_mov_b32 v35, 0\n"
      "v_mov_b32 v36, 0\n"
      "v_mov_b32 v37, 0\n"
      "v_mov_b32 v38, 0\n"
      "v_mov_b32 v39, 0\n"
      "v_mov_b32 v40, 0\n"
      "v_mov_b32 v41, 0\n"
      "v_mov_b32 v42, 0\n"
      "v_mov_b32 v43, 0\n"
      "v_mov_b32 v44, 0\n"
      "v_mov_b32 v45, 0\n"
      "v_mov_b32 v46, 0\n"
      "v_mov_b32 v47, 0\n"
      "v_mov_b32 v48, 0\n"
      "v_mov_b32 v49, 0\n"
      "v_mov_b32 v50, 0\n"
      "v_mov_b32 v51, 0\n"
      "v_mov_b32 v52, 0\n"
      "v_mov_b32 v53, 0\n"
      "v_mov_b32 v54, 0\n"
      "v_mov_b32 v55, 0\n"
      "v_mov_b32 v56, 0\n"
      "v_mov_b32 v57, 0\n"
      "v_mov_b32 v58, 0\n"
      "v_mov_b32 v59, 0\n"
      "v_mov_b32 v60, 0\n"
      "v_mov_b32 v61, 0\n"
      "v_mov_b32 v62, 0\n"
      "v_mov_b32 v63, 0\n"
      "v_mov_b32 v64, 0\n"
      "v_mov_b32 v65, 0\n"
      "v_mov_b32 v66, 0\n"
      "v_mov_b32 v67, 0\n"
      "v_mov_b32 v68, 0\n"
      "v_mov_b32 v69, 0\n"
      "v_mov_b32 v70, 0\n"
      "v_mov_b32 v71, 0\n"
      "v_mov_b32 v72, 0\n"
      "v_mov_b32 v73, 0\n"
      "v_mov_b32 v74, 0\n"
      "v_mov_b32 v75, 0\n"
      "v_mov_b32 v76, 0\n"
      "v_mov_b32 v77, 0\n"
      "v_mov_b32 v78, 0\n"
      "v_mov_b32 v79, 0\n"
      "v_mov_b32 v80, 0\n"
      "v_mov_b32 v81, 0\n"
      "v_mov_b32 v82, 0\n"
      "v_mov_b32 v83, 0\n"
      "v_mov_b32 v84, 0\n"
      "v_mov_b32 v85, 0\n"
      "v_mov_b32 v86, 0\n"
      "v_mov_b32 v87, 0\n"
      "v_mov_b32 v88, 0\n"
      "v_mov_b32 v89, 0\n"
      "v_mov_b32 v90, 0\n"
      "v_mov_b32 v91, 0\n"
      "v_mov_b32 v92, 0\n"
      "v_mov_b32 v93, 0\n"
      "v_mov_b32 v94, 0\n"
      "v_mov_b32 v95, 0\n"
      "v_xor_b32 v176, 0, v240\n"
      "v_xor_b32 v177, 32, v240\n"
      "v_xor_b32 v178, 64, v240\n"
      "v_xor_b32 v179, 96, v240\n"
      "v_xor_b32 v180, 128, v240\n"
      "v_xor_b32 v181, 160, v240\n"
      "v_xor_b32 v182, 192, v240\n"
      "v_xor_b32 v183, 224, v240\n"
      "v_mov_b32 v184, v232\n"
      "v_xor_b32 v185, 64, v232\n"
      "v_mov_b32 v187, %[sf]\n"
      "v_mov_b32 v189, %[sc]\n"
      "v_mov_b32 v188, -1\n"
      "s_mov_b32 %[cb], 0\n"
      "s_mov_b32 %[t1], 0\n"
      "s_mov_b32 %[bu], 1\n"
      "s_waitcnt lgkmcnt(0)\n"
      "s_barrier\n"
      "s_cmp_eq_u32 %[t1], 0x01010101\n"
      "s_cbranch_scc1 .Lqp0_d\n"
      "s_mov_b32 %[t0], 0x100000\n"
      ".Lqp0_l:\n"
      "s_sleep 1\n"
      "ds_read_b32 v186, v187 offset:0\n"
      "s_waitcnt lgkmcnt(0)\n"
      "v_readfirstlane_b32 %[t1], v186\n"
      "s_cmp_eq_u32 %[t1], 0x01010101\n"
      "s_cbranch_scc1 .Lqp0_d\n"
      "s_sub_u32 %[t0], %[t0], 1\n"
      "s_cbranch_scc0 .Lqp0_l\n"
      ".Lqp0_d:\n"
      QTS(0)
      "ds_read_b128 v[96:99], v184 offset:0\n"
      "ds_read_b128 v[100:103], v184 offset:2048\n"
      "ds_read_b128 v[104:107], v184 offset:4096\n"
      "ds_read_b64_tr_b16 v[144:145], v176 offset:0\n"
      "ds_read_b64_tr_b16 v[146:147], v176 offset:1024\n"
      "ds_read_b64_tr_b16 v[148:149], v177 offset:0\n"
      "ds_read_b64_tr_b16 v[150:151], v177 offset:1024\n"
      "ds_read_b64_tr_b16 v[152:153], v178 offset:0\n"
      "ds_read_b64_tr_b16 v[154:155], v178 offset:1024\n"
      "ds_read_b64_tr_b16 v[156:157], v179 offset:0\n"
      "ds_read_b64_tr_b16 v[158:159], v179 offset:1024\n"
      "ds_write_b32 v189, v188\n"
      ".Lqp_loop:\n"
      "ds_read_b128 v[108:111], v185 offset:0\n"
      "ds_read_b64_tr_b16 v[160:161], v180 offset:0\n"
      "ds_read_b64_tr_b16 v[162:163], v180 offset:1024\n"
      "s_waitcnt lgkmcnt(10)\n"
      "v_mfma_f32_16x16x32_bf16 v[0:3], v[144:147], v[96:99], v[0:3]\n"
      "v_mfma_f32_16x16x32_bf16 v[4:7], v[144:147], v[100:103], v[4:7]\n"
      "v_mfma_f32_16x16x32_bf16 v[8:11], v[144:147], v[104:107], v[8:11]\n"
      "ds_read_b128 v[112:115], v185 offset:2048\n"
      "ds_read_b64_tr_b16 v[164:165], v181 offset:0\n"
      "ds_read_b64_tr_b16 v[166:167], v181 offset:1024\n"
      "s_waitcnt lgkmcnt(11)\n"
      "v_mfma_f32_16x16x32_bf16 v[12:15], v[148:151], v[96:99], v[12:15]\n"
      "v_mfma_f32_16x16x32_bf16 v[16:19], v[148:151], v[100:103], v[16:19]\n"
      "v_mfma_f32_16x16x32_bf16 v[20:23], v[148:151], v[104:107], v[20:23]\n"
      "ds_read_b128 v[116:119], v185 offset:4096\n"
      "ds_read_b64_tr_b16 v[168:169], v182 offset:0\n"
      "ds_read_b64_tr_b16 v[170:171], v182 offset:1024\n"
      "s_waitcnt lgkmcnt(12)\n"
      "v_mfma_f32_16x16x32_bf16 v[24:27], v[152:155], v[96:99], v[24:27]\n"
      "v_mfma_f32_16x16x32_bf16 v[28:31], v[152:155], v[100:103], v[28:31]\n"
      "v_mfma_f32_16x16x32_bf16 v[32:35], v[152:155], v[104:107], v[32:35]\n"
      "ds_read_b64_tr_b16 v[172:173], v183 offset:0\n"
      "ds_read_b64_tr_b16 v[174:175], v183 offset:1024\n"
      "s_waitcnt lgkmcnt(12)\n"
      "v_mfma_f32_16x16x32_bf16 v[36:39], v[156:159], v[96:99], v[36:39]\n"
      "v_mfma_f32_16x16x32_bf16 v[40:43], v[156:159], v[100:103], v[40:43]\n"
      "v_mfma_f32_16x16x32_bf16 v[44:47], v[156:159], v[104:107], v[44:47]\n"
      "ds_read_b64_tr_b16 v[144:145], v176 offset:8192\n"
      "ds_read_b64_tr_b16 v[146:147], v176 offset:9216\n"
      "s_waitcnt lgkmcnt(10)\n"
      "v_mfma_f32_16x16x32_bf16 v[48:51], v[160:163], v[96:99], v[48:51]\n"
      "v_mfma_f32_16x16x32_bf16 v[52:55], v[160:163], v[100:103], v[52:55]\n"
      "v_mfma_f32_16x16x32_bf16 v[56:59], v[160:163], v[104:107], v[56:59]\n"
      "ds_read_b64_tr_b16 v[148:149], v177 offset:8192\n"
      "ds_read_b64_tr_b16 v[150:151], v177 offset:9216\n"
      "s_waitcnt lgkmcnt(9)\n"
      "v_mfma_f32_16x16x32_bf16 v[60:63], v[164:167], v[96:99], v[60:63]\n"
      "v_mfma_f32_16x16x32_bf16 v[64:67], v[164:167], v[100:103], v[64:67]\n"
      "v_mfma_f32_16x16x32_bf16 v[68:71], v[164:167], v[104:107], v[68:71]\n"
      "ds_read_b64_tr_b16 v[152:153], v178 offset:8192\n"
      "ds_read_b64_tr_b16 v[154:155], v178 offset:9216\n"
      "s_waitcnt lgkmcnt(8)\n"
      "v_mfma_f32_16x16x32_bf16 v[72:75], v[168:171], v[96:99], v[72:75]\n"
      "v_mfma_f32_16x16x32_bf16 v[76:79], v[168:171], v[100:103], v[76:79]\n"
      "v_mfma_f32_16x16x32_bf16 v[80:83], v[168:171], v[104:107], v[80:83]\n"
      "ds_read_b64_tr_b16 v[156:157], v179 offset:8192\n"
      "ds_read_b64_tr_b16 v[158:159], v179 offset:9216\n"
      "s_waitcnt lgkmcnt(8)\n"
      "v_mfma_f32_16x16x32_bf16 v[84:87], v[172:175], v[96:99], v[84:87]\n"
      "v_mfma_f32_16x16x32_bf16 v[88:91], v[172:175], v[100:103], v[88:91]\n"
      "v_mfma_f32_16x16x32_bf16 v[92:95], v[172:175], v[104:107], v[92:95]\n"
      "ds_read_b64_tr_b16 v[160:161], v180 offset:8192\n"
      "ds_read_b64_tr_b16 v[162:163], v180 offset:9216\n"
      "s_waitcnt lgkmcnt(8)\n"
      "v_mfma_f32_16x16x32_bf16 v[0:3], v[144:147], v[108:111], v[0:3]\n"
      "v_mfma_f32_16x16x32_bf16 v[4:7], v[144:147], v[112:115], v[4:7]\n"
      "v_mfma_f32_16x16x32_bf16 v[8:11], v[144:147], v[116:119], v[8:11]\n"
      "ds_read_b32 v186, v187 offset:4\n"
      "ds_read_b64_tr_b16 v[164:165], v181 offset:8192\n"
      "ds_read_b64_tr_b16 v[166:167], v181 offset:9216\n"
      "s_waitcnt lgkmcnt(9)\n"
      "v_mfma_f32_16x16x32_bf16 v[12:15], v[148:151], v[108:111], v[12:15]\n"
      "v_mfma_f32_16x16x32_bf16 v[16:19], v[148:151], v[112:115], v[16:19]\n"
      "v_mfma_f32_16x16x32_bf16 v[20:23], v[148:151], v[116:119], v[20:23]\n"
      "ds_read_b64_tr_b16 v[168:169], v182 offset:8192\n"
      "ds_read_b64_tr_b16 v[170:171], v182 offset:9216\n"
      "s_waitcnt lgkmcnt(9)\n"
      "v_mfma_f32_16x16x32_bf16 v[24:27], v[152:155], v[108:111], v[24:27]\n"
      "v_mfma_f32_16x16x32_bf16 v[28:31], v[152:155], v[112:115], v[28:31]\n"
      "v_mfma_f32_16x16x32_bf16 v[32:35], v[152:155], v[116:119], v[32:35]\n"
      "ds_read_b64_tr_b16 v[172:173], v183 offset:8192\n"
      "ds_read_b64_tr_b16 v[174:175], v183 offset:9216\n"
      "s_waitcnt lgkmcnt(9)\n"
      "v_mfma_f32_16x16x32_bf16 v[36:39], v[156:159], v[108:111], v[36:39]\n"
      "v_mfma_f32_16x16x32_bf16 v[40:43], v[156:159], v[112:115], v[40:43]\n"
      "v_mfma_f32_16x16x32_bf16 v[44:47], v[156:159], v[116:119], v[44:47]\n"
      "s_waitcnt lgkmcnt(6)\n"
      "v_readfirstlane_b32 %[t1], v186\n"
      "s_add_u32 %[bu], %[bu], 1\n"
      "s_cmp_eq_u32 %[bu], 3\n"
      "s_cselect_b32 %[bu], 0, %[bu]\n"
      "s_mov_b32 %[t0], 0xa000\n"
      "s_cselect_b32 %[t0], 0xfffec000, %[t0]\n"
      "v_add_u32 v176, %[t0], v176\n"
      "v_add_u32 v177, %[t0], v177\n"
      "v_add_u32 v178, %[t0], v178\n"
      "v_add_u32 v179, %[t0], v179\n"
      "v_add_u32 v180, %[t0], v180\n"
      "v_add_u32 v181, %[t0], v181\n"
      "v_add_u32 v182, %[t0], v182\n"
      "v_add_u32 v183, %[t0], v183\n"
      "v_add_u32 v184, %[t0], v184\n"
      "v_add_u32 v185, %[t0], v185\n"
      "s_cmp_eq_u32 %[t1], 0x01010101\n"
      "s_cbranch_scc1 .Lqp1_d\n"
      "s_mov_b32 %[t0], 0x100000\n"
      ".Lqp1_l:\n"
      "s_sleep 1\n"
      "ds_read_b32 v186, v187 offset:4\n"
      "s_waitcnt lgkmcnt(0)\n"
      "v_readfirstlane_b32 %[t1], v186\n"
      "s_cmp_eq_u32 %[t1], 0x01010101\n"
      "s_cbranch_scc1 .Lqp1_d\n"
      "s_sub_u32 %[t0], %[t0], 1\n"
      "s_cbranch_scc0 .Lqp1_l\n"
      ".Lqp1_d:\n"
      QTS(1)
      "ds_read_b128 v[120:123], v184 offset:0\n"
      "ds_read_b128 v[124:127], v184 offset:2048\n"
      "ds_read_b128 v[128:131], v184 offset:4096\n"
      "ds_read_b64_tr_b16 v[144:145], v176 offset:0\n"
      "ds_read_b64_tr_b16 v[146:147], v176 offset:1024\n"
      "v_mfma_f32_16x16x32_bf16 v[48:51], v[160:163], v[108:111], v[48:51]\n"
      "v_mfma_f32_16x16x32_bf16 v[52:55], v[160:163], v[112:115], v[52:55]\n"
      "v_mfma_f32_16x16x32_bf16 v[56:59], v[160:163], v[116:119], v[56:59]\n"
      "ds_read_b64_tr_b16 v[148:149], v177 offset:0\n"
      "ds_read_b64_tr_b16 v[150:151], v177 offset:1024\n"
      "s_waitcnt lgkmcnt(11)\n"
      "v_mfma_f32_16x16x32_bf16 v[60:63], v[164:167], v[108:111], v[60:63]\n"
      "v_mfma_f32_16x16x32_bf16 v[64:67], v[164:167], v[112:115], v[64:67]\n"
      "v_mfma_f32_16x16x32_bf16 v[68:71], v[164:167], v[116:119], v[68:71]\n"
      "ds_read_b64_tr_b16 v[152:153], v178 offset:0\n"
      "ds_read_b64_tr_b16 v[154:155], v178 offset:1024\n"
      "s_waitcnt lgkmcnt(11)\n"
      "v_mfma_f32_16x16x32_bf16 v[72:75], v[168:171], v[108:111], v[72:75]\n"
      "v_mfma_f32_16x16x32_bf16 v[76:79], v[168:171], v[112:115], v[76:79]\n"
      "v_mfma_f32_16x16x32_bf16 v[80:83], v[168:171], v[116:119], v[80:83]\n"
      "ds_read_b64_tr_b16 v[156:157], v179 offset:0\n"
      "ds_read_b64_tr_b16 v[158:159], v179 offset:1024\n"
      "s_waitcnt lgkmcnt(11)\n"
      "s_add_u32 %[t0], %[cb], 0\n"
      "v_mov_b32 v188, %[t0]\n"
      "ds_write_b32 v189, v188\n"
      "v_mfma_f32_16x16x32_bf16 v[84:87], v[172:175], v[108:111], v[84:87]\n"
      "v_mfma_f32_16x16x32_bf16 v[88:91], v[172:175], v[112:115], v[88:91]\n"
      "v_mfma_f32_16x16x32_bf16 v[92:95], v[172:175], v[116:119], v[92:95]\n"
      "ds_read_b128 v[132:135], v185 offset:0\n"
      "ds_read_b64_tr_b16 v[160:161], v180 offset:0\n"
      "ds_read_b64_tr_b16 v[162:163], v180 offset:1024\n"
      "s_waitcnt lgkmcnt(10)\n"
      "v_mfma_f32_16x16x32_bf16 v[0:3], v[144:147], v[120:123], v[0:3]\n"
      "v_mfma_f32_16x16x32_bf16 v[4:7], v[144:147], v[124:127], v[4:7]\n"
      "v_mfma_f32_16x16x32_bf16 v[8:11], v[144:147], v[128:131], v[8:11]\n"
      "ds_read_b128 v[136:139], v185 offset:2048\n"
      "ds_read_b64_tr_b16 v[164:165], v181 offset:0\n"
      "ds_read_b64_tr_b16 v[166:167], v181 offset:1024\n"
      "s_waitcnt lgkmcnt(11)\n"
      "v_mfma_f32_16x16x32_bf16 v[12:15], v[148:151], v[120:123], v[12:15]\n"
      "v_mfma_f32_16x16x32_bf16 v[16:19], v[148:151], v[124:127], v[16:19]\n"
      "v_mfma_f32_16x16x32_bf16 v[20:23], v[148:151], v[128:131], v[20:23]\n"
      "ds_read_b128 v[140:143], v185 offset:4096\n"
      "ds_read_b64_tr_b16 v[168:169], v182 offset:0\n"
      "ds_read_b64_tr_b16 v[170:171], v182 offset:1024\n"
      "s_waitcnt lgkmcnt(12)\n"
      "v_mfma_f32_16x16x32_bf16 v[24:27], v[152:155], v[120:123], v[24:27]\n"
      "v_mfma_f32_16x16x32_bf16 v[28:31], v[152:155], v[124:127], v[28:31]\n"
      "v_mfma_f32_16x16x32_bf16 v[32:35], v[152:155], v[128:131], v[32:35]\n"
      "ds_read_b64_tr_b16 v[172:173], v183 offset:0\n"
      "ds_read_b64_tr_b16 v[174:175], v183 offset:1024\n"
      "s_waitcnt lgkmcnt(12)\n"
      "v_mfma_f32_16x16x32_bf16 v[36:39], v[156:159], v[120:123], v[36:39]\n"
      "v_mfma_f32_16x16x32_bf16 v[40:43], v[156:159], v[124:127], v[40:43]\n"
      "v_mfma_f32_16x16x32_bf16 v[44:47], v[156:159], v[128:131], v[44:47]\n"
      "ds_read_b64_tr_b16 v[144:145], v176 offset:8192\n"
      "ds_read_b64_tr_b16 v[146:147], v176 offset:9216\n"
      "s_waitcnt lgkmcnt(10)\n"
      "v_mfma_f32_16x16x32_bf16 v[48:51], v[160:163], v[120:123], v[48:51]\n"
      "v_mfma_f32_16x16x32_bf16 v[52:55], v[160:163], v[124:127], v[52:55]\n"
      "v_mfma_f32_16x16x32_bf16 v[56:59], v[160:163], v[128:131], v[56:59]\n"
      "ds_read_b64_tr_b16 v[148:149], v177 offset:8192\n"
      "ds_read_b64_tr_b16 v[150:151], v177 offset:9216\n"
      "s_waitcnt lgkmcnt(9)\n"
      "v_mfma_f32_16x16x32_bf16 v[60:63], v[164:167], v[120:123], v[60:63]\n"
      "v_mfma_f32_16x16x32_bf16 v[64:67], v[164:167], v[124:127], v[64:67]\n"
      "v_mfma_f32_16x16x32_bf16 v[68:71], v[164:167], v[128:131], v[68:71]\n"
      "ds_read_b64_tr_b16 v[152:153], v178 offset:8192\n"
      "ds_read_b64_tr_b16 v[154:155], v178 offset:9216\n"
      "s_waitcnt lgkmcnt(8)\n"
      "v_mfma_f32_16x16x32_bf16 v[72:75], v[168:171], v[120:123], v[72:75]\n"
      "v_mfma_f32_16x16x32_bf16 v[76:79], v[168:171], v[124:127], v[76:79]\n"
      "v_mfma_f32_16x16x32_bf16 v[80:83], v[168:171], v[128:131], v[80:83]\n"
      "ds_read_b64_tr_b16 v[156:157], v179 offset:8192\n"
      "ds_read_b64_tr_b16 v[158:159], v179 offset:9216\n"
      "s_waitcnt lgkmcnt(8)\n"
      "v_mfma_f32_16x16x32_bf16 v[84:87], v[172:175], v[120:123], v[84:87]\n"
      "v_mfma_f32_16x16x32_bf16 v[88:91], v[172:175], v[124:127], v[88:91]\n"
      "v_mfma_f32_16x16x32_bf16 v[92:95], v[172:175], v[128:131], v[92:95]\n"
      "ds_read_b64_tr_b16 v[160:161], v180 offset:8192\n"
      "ds_read_b64_tr_b16 v[162:163], v180 offset:9216\n"
      "s_waitcnt lgkmcnt(8)\n"
      "v_mfma_f32_16x16x32_bf16 v[0:3], v[144:147], v[132:135], v[0:3]\n"
      "v_mfma_f32_16x16x32_bf16 v[4:7], v[144:147], v[136:139], v[4:7]\n"
      "v_mfma_f32_16x16x32_bf16 v[8:11], v[144:147], v[140:143], v[8:11]\n"
      "ds_read_b32 v186, v187 offset:8\n"
      "ds_read_b64_tr_b16 v[164:165], v181 offset:8192\n"
      "ds_read_b64_tr_b16 v[166:167], v181 offset:9216\n"
      "s_waitcnt lgkmcnt(9)\n"
      "v_mfma_f32_16x16x32_bf16 v[12:15], v[148:151], v[132:135], v[12:15]\n"
      "v_mfma_f32_16x16x32_bf16 v[16:19], v[148:151], v[136:139], v[16:19]\n"
      "v_mfma_f32_16x16x32_bf16 v[20:23], v[148:151], v[140:143], v[20:23]\n"
      "ds_read_b64_tr_b16 v[168:169], v182 offset:8192\n"
      "ds_read_b64_tr_b16 v[170:171], v182 offset:9216\n"
      "s_waitcnt lgkmcnt(9)\n"
      "v_mfma_f32_16x16x32_bf16 v[24:27], v[152:155], v[132:135], v[24:27]\n"
      "v_mfma_f32_16x16x32_bf16 v[28:31], v[152:155], v[136:139], v[28:31]\n"
      "v_mfma_f32_16x16x32_bf16 v[32:35], v[152:155], v[140:143], v[32:35]\n"
      "ds_read_b64_tr_b16 v[172:173], v183 offset:8192\n"
      "ds_read_b64_tr_b16 v[174:175], v183 offset:9216\n"
      "s_waitcnt lgkmcnt(9)\n"
      "v_mfma_f32_16x16x32_bf16 v[36:39], v[156:159], v[132:135], v[36:39]\n"
      "v_mfma_f32_16x16x32_bf16 v[40:43], v[156:159], v[136:139], v[40:43]\n"
      "v_mfma_f32_16x16x32_bf16 v[44:47], v[156:159], v[140:143], v[44:47]\n"
      "s_waitcnt lgkmcnt(6)\n"
      "v_readfirstlane_b32 %[t1], v186\n"
      "s_add_u32 %[bu], %[bu], 1\n"
      "s_cmp_eq_u32 %[bu], 3\n"
      "s_cselect_b32 %[bu], 0, %[bu]\n"
      "s_mov_b32 %[t0], 0xa000\n"
      "s_cselect_b32 %[t0], 0xfffec000, %[t0]\n"
      "v_add_u32 v176, %[t0], v176\n"
      "v_add_u32 v177, %[t0], v177\n"
      "v_add_u32 v178, %[t0], v178\n"
      "v_add_u32 v179, %[t0], v179\n"
      "v_add_u32 v180, %[t0], v180\n"
      "v_add_u32 v181, %[t0], v181\n"
      "v_add_u32 v182, %[t0], v182\n"
      "v_add_u32 v183, %[t0], v183\n"
      "v_add_u32 v184, %[t0], v184\n"
      "v_add_u32 v185, %[t0], v185\n"
      "s_cmp_eq_u32 %[cb], 6\n"
      "s_cbranch_scc1 .Lqp_last\n"
      "s_cmp_eq_u32 %[t1], 0x01010101\n"
      "s_cbranch_scc1 .Lqp2_d\n"
      "s_mov_b32 %[t0], 0x100000\n"
      ".Lqp2_l:\n"
      "s_sleep 1\n"
      "ds_read_b32 v186, v187 offset:8\n"
      "s_waitcnt lgkmcnt(0)\n"
      "v_readfirstlane_b32 %[t1], v186\n"
      "s_cmp_eq_u32 %[t1], 0x01010101\n"
      "s_cbranch_scc1 .Lqp2_d\n"
      "s_sub_u32 %[t0], %[t0], 1\n"
      "s_cbranch_scc0 .Lqp2_l\n"
      ".Lqp2_d:\n"
      QTS(2)
      ".Lqp_last:\n"
      "ds_read_b128 v[96:99], v184 offset:0\n"
      "ds_read_b128 v[100:103], v184 offset:2048\n"
      "ds_read_b128 v[104:107], v184 offset:4096\n"
      "ds_read_b64_tr_b16 v[144:145], v176 offset:0\n"
      "ds_read_b64_tr_b16 v[146:147], v176 offset:1024\n"
      "v_mfma_f32_16x16x32_bf16 v[48:51], v[160:163], v[132:135], v[48:51]\n"
      "v_mfma_f32_16x16x32_bf16 v[52:55], v[160:163], v[136:139], v[52:55]\n"
      "v_mfma_f32_16x16x32_bf16 v[56:59], v[160:163], v[140:143], v[56:59]\n"
      "ds_read_b64_tr_b16 v[148:149], v177 offset:0\n"
      "ds_read_b64_tr_b16 v[150:151], v177 offset:1024\n"
      "s_waitcnt lgkmcnt(11)\n"
      "v_mfma_f32_16x16x32_bf16 v[60:63], v[164:167], v[132:135], v[60:63]\n"
      "v_mfma_f32_16x16x32_bf16 v[64:67], v[164:167], v[136:139], v[64:67]\n"
      "v_mfma_f32_16x16x32_bf16 v[68:71], v[164:167], v[140:143], v[68:71]\n"
      "ds_read_b64_tr_b16 v[152:153], v178 offset:0\n"
      "ds_read_b64_tr_b16 v[154:155], v178 offset:1024\n"
      "s_waitcnt lgkmcnt(11)\n"
      "v_mfma_f32_16x16x32_bf16 v[72:75], v[168:171], v[132:135], v[72:75]\n"
      "v_mfma_f32_16x16x32_bf16 v[76:79], v[168:171], v[136:139], v[76:79]\n"
      "v_mfma_f32_16x16x32_bf16 v[80:83], v[168:171], v[140:143], v[80:83]\n"
      "ds_read_b64_tr_b16 v[156:157], v179 offset:0\n"
      "ds_read_b64_tr_b16 v[158:159], v179 offset:1024\n"
      "s_waitcnt lgkmcnt(11)\n"
      "s_add_u32 %[t0], %[cb], 1\n"
      "v_mov_b32 v188, %[t0]\n"
      "ds_write_b32 v189, v188\n"
      "v_mfma_f32_16x16x32_bf16 v[84:87], v[172:175], v[132:135], v[84:87]\n"
      "v_mfma_f32_16x16x32_bf16 v[88:91], v[172:175], v[136:139], v[88:91]\n"
      "v_mfma_f32_16x16x32_bf16 v[92:95], v[172:175], v[140:143], v[92:95]\n"
      "s_add_u32 %[cb], %[cb], 2\n"
      "v_add_u32 v187, 8, v187\n"
      "s_cmp_lt_u32 %[cb], 8\n"
      "s_cbranch_scc1 .Lqp_loop\n"
      "s_waitcnt lgkmcnt(0)\n"
      "s_nop 7\n"
      "s_nop 7\n"
      : "={v[0:31]}"(a0), "={v[32:63]}"(a1), "={v[64:95]}"(a2), [cb] "=&s"(cb), [bu] "=&s"(bu), [t0] "=&s"(t0), [t1] "=&s"(t1) QTS_O
      : "{v232}"(xv), "{v240}"(wb), [sf] "s"(lbase + CTRL + 64), [sc] "s"(lbase + CTRL + 16 + wid * 4)
      : "memory", "scc", "vcc", "v96", "v97", "v98", "v99", "v100", "v101", "v102", "v103", "v104", "v105", "v106", "v107", "v108", "v109", "v110", "v111", "v112", "v113", "v114", "v115", "v116", "v117", "v118", "v119", "v120", "v121", "v122", "v123", "v124", "v125", "v126", "v127", "v128", "v129", "v130", "v131", "v132", "v133", "v134", "v135", "v136", "v137", "v138", "v139", "v140", "v141", "v142", "v143", "v144", "v145", "v146", "v147", "v148", "v149", "v150", "v151", "v152", "v153", "v154", "v155", "v156", "v157", "v158", "v159", "v160", "v161", "v162", "v163", "v164", "v165", "v166", "v167", "v168", "v169", "v170", "v171", "v172", "v173", "v174", "v175", "v176", "v177", "v178", "v179", "v180", "v181", "v182", "v183", "v184", "v185", "v186", "v187", "v188", "v189");
#pragma unroll
    for (int i = 0; i < 24; ++i)
#pragma unroll
      for (int e = 0; e < 4; ++e) acc[i / 3][i % 3][e] = i < 8 ? a0[4 * i + e] : i < 16 ? a1[4 * (i - 8) + e] : a2[4 * (i - 16) + e];
    asm volatile("s_waitcnt vmcnt(0)\n" : : "v"(junk) : "memory");
#pragma unroll
    for (int ks = 0; ks < 4; ++ks)
#pragma unroll
      for (int qb = 0; qb < 3; ++qb) {
        const f32x4 a = acc[2 * ks][qb], c = acc[2 * ks + 1][qb];
        auto r0 = __builtin_amdgcn_permlane32_swap(cvtpk(a[0], a[1]), cvtpk(c[0], c[1]), false, false);
        auto r1 = __builtin_amdgcn_permlane32_swap(cvtpk(a[2], a[3]), cvtpk(c[2], c[3]), false, false);
        auto s0 = __builtin_amdgcn_permlane16_swap(r0[0], r0[1], false, false);
        auto s1 = __builtin_amdgcn_permlane16_swap(r1[0], r1[1], false, false);
        u32x4 w = {s0[0], s1[0], s0[1], s1[1]};
        qf[ks][qb] = __builtin_bit_cast(bf16x8, w);
      }
  }
  TS();
  float lrun[3];
  {
    typedef __attribute__((ext_vector_type(32))) float f32x32;
    typedef __attribute__((ext_vector_type(32))) unsigned u32x32;
    typedef __attribute__((ext_vector_type(16))) unsigned u32x16;
    typedef __attribute__((ext_vector_type(3))) float f32x3;
    const int lq = fresh(lane_id()) & 15, lg = fresh(lane_id()) >> 4;
    const int kb0 = lbase + lq * 256 + ((lg ^ lq) << 4);
    const int vb0 = lbase + (4 * lg + (lq >> 2)) * 256 + ((4 * (lg & 1) + (lq >> 2)) << 5) + ((lq & 3) << 3);
    u32x32 qa; u32x16 qc;
#pragma unroll
    for (int i = 0; i < 12; ++i) {
      const u32x4 w = __builtin_bit_cast(u32x4, qf[i / 3][i % 3]);
#pragma unroll
      for (int e = 0; e < 4; ++e) { if (i < 8) qa[4 * i + e] = w[e]; else qc[4 * (i - 8) + e] = w[e]; }
    }
    f32x32 a0, a1, a2; f32x3 lo;
    int sk = (beta ? 0 : 8) * SLOT, sv = (1 + beta) * SLOT, sf = lbase + CTRL + 64 + (8 + beta) * 4, si = 8 + beta, cnt, t0, t1;
    const int sc = lbase + CTRL + 16 + wid * 4;
#define ATS
#define ATS_O
#define ATS_I
#define ATS_C
    asm volatile(
      "s_nop 1\n"
      "v_mov_b32 v235, 0xff800000\n"
      "v_mov_b32 v236, 0xff800000\n"
      "v_mov_b32 v237, 0xff800000\n"
      "v_mov_b32 v254, %[sf]\n"
      "ds_read_b32 v253, v254 offset:0\n"
      "s_waitcnt lgkmcnt(0)\n"
      "v_readfirstlane_b32 %[t1], v253\n"
      "s_cmp_eq_u32 %[t1], 0x01010101\n"
      "s_cbranch_scc1 .Latt0_d\n"
      "s_mov_b32 %[t0], 0x100000\n"
      "v_mov_b32 v254, %[sf]\n"
      ".Latt0_l:\n"
      "s_sleep 1\n"
      "ds_read_b32 v253, v254 offset:0\n"
      "s_waitcnt lgkmcnt(0)\n"
      "v_readfirstlane_b32 %[t1], v253\n"
      "s_cmp_eq_u32 %[t1], 0x01010101\n"
      "s_cbranch_scc1 .Latt0_d\n"
      "s_sub_u32 %[t0], %[t0], 1\n"
      "s_cbranch_scc0 .Latt0_l\n"
      ".Latt0_d:\n"
      "v_add_u32 v247, %[sk], v251\n"
      "v_xor_b32 v248, 64, v247\n"
      "v_xor_b32 v249, 128, v247\n"
      "v_xor_b32 v250, 192, v247\n"
      "ds_read_b128 v[216:219], v247 offset:0\n"
      "ds_read_b128 v[220:223], v248 offset:0\n"
      "ds_read_b128 v[224:227], v249 offset:0\n"
      "s_waitcnt lgkmcnt(2)\n"
      "v_mfma_f32_16x16x32_bf16 v[144:147], v[216:219], v[96:99], 0\n"
      "ds_read_b128 v[228:231], v250 offset:0\n"
      "v_mov_b32 v0, 0\n"
      "v_mov_b32 v1, 0\n"
      "v_mov_b32 v2, 0\n"
      "v_mfma_f32_16x16x32_bf16 v[148:151], v[216:219], v[100:103], 0\n"
      "v_mov_b32 v3, 0\n"
      "v_mov_b32 v4, 0\n"
      "v_mfma_f32_16x16x32_bf16 v[152:155], v[216:219], v[104:107], 0\n"
      "v_mov_b32 v5, 0\n"
      "v_mov_b32 v6, 0\n"
      "v_mov_b32 v7, 0\n"
      "s_waitcnt lgkmcnt(2)\n"
      "v_mfma_f32_16x16x32_bf16 v[144:147], v[220:223], v[108:111], v[144:147]\n"
      "ds_read_b128 v[216:219], v247 offset:4096\n"
      "v_mov_b32 v8, 0\n"
      "v_mov_b32 v9, 0\n"
      "v_mfma_f32_16x16x32_bf16 v[148:151], v[220:223], v[112:115], v[148:151]\n"
      "v_mov_b32 v10, 0\n"
      "v_mov_b32 v11, 0\n"
      "v_mfma_f32_16x16x32_bf16 v[152:155], v[220:223], v[116:119], v[152:155]\n"
      "v_mov_b32 v12, 0\n"
      "v_mov_b32 v13, 0\n"
      "v_mov_b32 v14, 0\n"
      "s_waitcnt lgkmcnt(2)\n"
      "v_mfma_f32_16x16x32_bf16 v[144:147], v[224:227], v[120:123], v[144:147]\n"
      "ds_read_b128 v[220:223], v248 offset:4096\n"
      "v_mov_b32 v15, 0\n"
      "v_mov_b32 v16, 0\n"
      "v_mfma_f32_16x16x32_bf16 v[148:151], v[224:227], v[124:127], v[148:151]\n"
      "v_mov_b32 v17, 0\n"
      "v_mov_b32 v18, 0\n"
      "v_mfma_f32_16x16x32_bf16 v[152:155], v[224:227], v[128:131], v[152:155]\n"
      "v_mov_b32 v19, 0\n"
      "v_mov_b32 v20, 0\n"
      "v_mov_b32 v21, 0\n"
      "s_waitcnt lgkmcnt(2)\n"
      "v_mfma_f32_16x16x32_bf16 v[144:147], v[228:231], v[132:135], v[144:147]\n"
      "ds_read_b128 v[224:227], v249 offset:4096\n"
      "v_mov_b32 v22, 0\n"
      "v_mov_b32 v23, 0\n"
      "v_mfma_f32_16x16x32_bf16 v[148:151], v[228:231], v[136:139], v[148:151]\n"
      "v_mov_b32 v24, 0\n"
      "v_mov_b32 v25, 0\n"
      "v_mov_b32 v26, 0\n"
      "v_mfma_f32_16x16x32_bf16 v[152:155], v[228:231], v[140:143], v[152:155]\n"
      "v_mov_b32 v27, 0\n"
      "v_mov_b32 v28, 0\n"
      "s_waitcnt lgkmcnt(2)\n"
      "v_mfma_f32_16x16x32_bf16 v[192:195], v[216:219], v[96:99], 0\n"
      "ds_read_b128 v[228:231], v250 offset:4096\n"
      "v_mov_b32 v29, 0\n"
      "v_mov_b32 v30, 0\n"
      "v_mfma_f32_16x16x32_bf16 v[196:199], v[216:219], v[100:103], 0\n"
      "v_mov_b32 v31, 0\n"
      "v_mov_b32 v32, 0\n"
      "v_mov_b32 v33, 0\n"
      "v_mfma_f32_16x16x32_bf16 v[200:203], v[216:219], v[104:107], 0\n"
      "v_mov_b32 v34, 0\n"
      "v_mov_b32 v35, 0\n"
      "s_waitcnt lgkmcnt(2)\n"
      "v_mfma_f32_16x16x32_bf16 v[192:195], v[220:223], v[108:111], v[192:195]\n"
      "ds_read_b128 v[216:219], v247 offset:8192\n"
      "v_mov_b32 v36, 0\n"
      "v_mov_b32 v37, 0\n"
      "v_mfma_f32_16x16x32_bf16 v[196:199], v[220:223], v[112:115], v[196:199]\n"
      "v_mov_b32 v38, 0\n"
      "v_mov_b32 v39, 0\n"
      "v_mov_b32 v40, 0\n"
      "v_mfma_f32_16x16x32_bf16 v[200:203], v[220:223], v[116:119], v[200:203]\n"
      "v_mov_b32 v41, 0\n"
      "v_mov_b32 v42, 0\n"
      "s_waitcnt lgkmcnt(2)\n"
      "v_mfma_f32_16x16x32_bf16 v[192:195], v[224:227], v[120:123], v[192:195]\n"
      "ds_read_b128 v[220:223], v248 offset:8192\n"
      "v_mov_b32 v43, 0\n"
      "v_mov_b32 v44, 0\n"
      "v_mov_b32 v45, 0\n"
      "v_mfma_f32_16x16x32_bf16 v[196:199], v[224:227], v[124:127], v[196:199]\n"
      "v_mov_b32 v46, 0\n"
      "v_mov_b32 v47, 0\n"
      "v_mfma_f32_16x16x32_bf16 v[200:203], v[224:227], v[128:131], v[200:203]\n"
      "v_max3_f32 v232, v235, v144, v145\n"
      "v_max3_f32 v233, v236, v148, v149\n"
      "s_waitcnt lgkmcnt(2)\n"
      "v_mfma_f32_16x16x32_bf16 v[192:195], v[228:231], v[132:135], v[192:195]\n"
      "ds_read_b128 v[224:227], v249 offset:8192\n"
      "v_max3_f32 v234, v237, v152, v153\n"
      "v_max3_f32 v232, v232, v146, v147\n"
      "v_max3_f32 v233, v233, v150, v151\n"
      "v_mfma_f32_16x16x32_bf16 v[196:199], v[228:231], v[136:139], v[196:199]\n"
      "v_max3_f32 v234, v234, v154, v155\n"
      "v_mov_b32 v48, 0\n"
      "v_mfma_f32_16x16x32_bf16 v[200:203], v[228:231], v[140:143], v[200:203]\n"
      "v_mov_b32 v49, 0\n"
      "v_mov_b32 v50, 0\n"
      "s_waitcnt lgkmcnt(2)\n"
      "v_mfma_f32_16x16x32_bf16 v[156:159], v[216:219], v[96:99], 0\n"
      "ds_read_b128 v[228:231], v250 offset:8192\n"
      "v_mov_b32 v51, 0\n"
      "v_mov_b32 v52, 0\n"
      "v_mov_b32 v53, 0\n"
      "v_mfma_f32_16x16x32_bf16 v[160:163], v[216:219], v[100:103], 0\n"
      "v_mov_b32 v54, 0\n"
      "v_mov_b32 v55, 0\n"
      "v_mfma_f32_16x16x32_bf16 v[164:167], v[216:219], v[104:107], 0\n"
      "v_mov_b32 v56, 0\n"
      "v_mov_b32 v57, 0\n"
      "v_mov_b32 v58, 0\n"
      "s_waitcnt lgkmcnt(2)\n"
      "v_mfma_f32_16x16x32_bf16 v[156:159], v[220:223], v[108:111], v[156:159]\n"
      "ds_read_b128 v[216:219], v247 offset:12288\n"
      "v_mov_b32 v59, 0\n"
      "v_mov_b32 v60, 0\n"
      "v_mfma_f32_16x16x32_bf16 v[160:163], v[220:223], v[112:115], v[160:163]\n"
      "v_mov_b32 v61, 0\n"
      "v_mov_b32 v62, 0\n"
      "v_mfma_f32_16x16x32_bf16 v[164:167], v[220:223], v[116:119], v[164:167]\n"
      "v_mov_b32 v63, 0\n"
      "v_mov_b32 v64, 0\n"
      "v_mov_b32 v65, 0\n"
      "s_waitcnt lgkmcnt(2)\n"
      "v_mfma_f32_16x16x32_bf16 v[156:159], v[224:227], v[120:123], v[156:159]\n"
      "ds_read_b128 v[220:223], v248 offset:12288\n"
      "v_mov_b32 v66, 0\n"
      "v_mov_b32 v67, 0\n"
      "v_mfma_f32_16x16x32_bf16 v[160:163], v[224:227], v[124:127], v[160:163]\n"
      "v_mov_b32 v68, 0\n"
      "v_mov_b32 v69, 0\n"
      "v_mfma_f32_16x16x32_bf16 v[164:167], v[224:227], v[128:131], v[164:167]\n"
      "v_mov_b32 v70, 0\n"
      "v_mov_b32 v71, 0\n"
      "v_max3_f32 v232, v232, v192, v193\n"
      "s_waitcnt lgkmcnt(2)\n"
      "v_mfma_f32_16x16x32_bf16 v[156:159], v[228:231], v[132:135], v[156:159]\n"
      "ds_read_b128 v[224:227], v249 offset:12288\n"
      "v_max3_f32 v233, v233, v196, v197\n"
      "v_max3_f32 v234, v234, v200, v201\n"
      "v_mfma_f32_16x16x32_bf16 v[160:163], v[228:231], v[136:139], v[160:163]\n"
      "v_max3_f32 v232, v232, v194, v195\n"
      "v_max3_f32 v233, v233, v198, v199\n"
      "v_max3_f32 v234, v234, v202, v203\n"
      "v_mfma_f32_16x16x32_bf16 v[164:167], v[228:231], v[140:143], v[164:167]\n"
      "v_mov_b32 v72, 0\n"
      "v_mov_b32 v73, 0\n"
      "s_waitcnt lgkmcnt(2)\n"
      "v_mfma_f32_16x16x32_bf16 v[204:207], v[216:219], v[96:99], 0\n"
      "ds_read_b128 v[228:231], v250 offset:12288\n"
      "s_add_u32 %[t0], %[si], 0\n"
      "v_mov_b32 v74, 0\n"
      "v_mov_b32 v75, 0\n"
      "v_mfma_f32_16x16x32_bf16 v[208:211], v[216:219], v[100:103], 0\n"
      "v_mov_b32 v253, %[t0]\n"
      "v_mov_b32 v254, %[sc]\n"
      "v_mov_b32 v76, 0\n"
      "v_mov_b32 v77, 0\n"
      "v_mov_b32 v78, 0\n"
      "v_mfma_f32_16x16x32_bf16 v[212:215], v[216:219], v[104:107], 0\n"
      "ds_write_b32 v254, v253 offset:0\n"
      "v_mov_b32 v79, 0\n"
      "v_mov_b32 v80, 0\n"
      "s_waitcnt lgkmcnt(3)\n"
      "v_mfma_f32_16x16x32_bf16 v[204:207], v[220:223], v[108:111], v[204:207]\n"
      "v_mov_b32 v81, 0\n"
      "v_mov_b32 v82, 0\n"
      "v_mfma_f32_16x16x32_bf16 v[208:211], v[220:223], v[112:115], v[208:211]\n"
      "v_mov_b32 v83, 0\n"
      "v_mov_b32 v84, 0\n"
      "v_mov_b32 v85, 0\n"
      "v_mfma_f32_16x16x32_bf16 v[212:215], v[220:223], v[116:119], v[212:215]\n"
      "v_mov_b32 v86, 0\n"
      "v_mov_b32 v87, 0\n"
      "s_waitcnt lgkmcnt(2)\n"
      "v_mfma_f32_16x16x32_bf16 v[204:207], v[224:227], v[120:123], v[204:207]\n"
      "s_add_u32 %[sk], %[sk], 0x10000\n"
      "s_cmp_ge_u32 %[sk], 0x24000\n"
      "v_mov_b32 v88, 0\n"
      "v_mov_b32 v89, 0\n"
      "v_mov_b32 v90, 0\n"
      "v_mfma_f32_16x16x32_bf16 v[208:211], v[224:227], v[124:127], v[208:211]\n"
      "s_cselect_b32 %[t0], 0x24000, 0\n"
      "v_mov_b32 v91, 0\n"
      "v_mov_b32 v92, 0\n"
      "v_mfma_f32_16x16x32_bf16 v[212:215], v[224:227], v[128:131], v[212:215]\n"
      "s_sub_u32 %[sk], %[sk], %[t0]\n"
      "v_mov_b32 v93, 0\n"
      "v_mov_b32 v94, 0\n"
      "s_waitcnt lgkmcnt(1)\n"
      "v_mfma_f32_16x16x32_bf16 v[204:207], v[228:231], v[132:135], v[204:207]\n"
      "v_mov_b32 v95, 0\n"
      "v_max3_f32 v232, v232, v156, v157\n"
      "v_max3_f32 v233, v233, v160, v161\n"
      "v_mfma_f32_16x16x32_bf16 v[208:211], v[228:231], v[136:139], v[208:211]\n"
      "v_max3_f32 v234, v234, v164, v165\n"
      "v_max3_f32 v232, v232, v158, v159\n"
      "v_mfma_f32_16x16x32_bf16 v[212:215], v[228:231], v[140:143], v[212:215]\n"
      "v_max3_f32 v233, v233, v162, v163\n"
      "v_max3_f32 v234, v234, v166, v167\n"
      ATS
      "v_max3_f32 v232, v232, v204, v205\n"
      "s_nop 1\n"
      "v_max3_f32 v233, v233, v208, v209\n"
      "s_nop 1\n"
      "v_max3_f32 v234, v234, v212, v213\n"
      "v_max3_f32 v232, v232, v206, v207\n"
      "v_max3_f32 v233, v233, v210, v211\n"
      "v_max3_f32 v234, v234, v214, v215\n"
      "v_mov_b32 v241, v232\n"
      "v_mov_b32 v242, v233\n"
      "v_mov_b32 v243, v234\n"
      "v_permlane32_swap_b32 v232, v241\n"
      "v_permlane32_swap_b32 v233, v242\n"
      "v_permlane32_swap_b32 v234, v243\n"
      "v_max_f32 v232, v232, v241\n"
      "v_max_f32 v233, v233, v242\n"
      "v_max_f32 v234, v234, v243\n"
      "v_mov_b32 v241, v232\n"
      "v_mov_b32 v242, v233\n"
      "v_mov_b32 v243, v234\n"
      "v_permlane16_swap_b32 v232, v241\n"
      "v_permlane16_swap_b32 v233, v242\n"
      "v_permlane16_swap_b32 v234, v243\n"
      "v_max_f32 v232, v232, v241\n"
      "v_max_f32 v233, v233, v242\n"
      "v_max_f32 v234, v234, v243\n"
      "v_mul_f32 v244, 0xbe0293ee, v232\n"
      "v_mul_f32 v245, 0xbe0293ee, v233\n"
      "v_mul_f32 v246, 0xbe0293ee, v234\n"
      "v_mov_b32 v241, 0\n"
      "v_mov_b32 v242, 0\n"
      "v_mov_b32 v243, 0\n"
      "v_mov_b32 v238, 0\n"
      "v_mov_b32 v239, 0\n"
      "v_mov_b32 v240, 0\n"
      "v_mov_b32 v254, %[sf]\n"
      "ds_read_b32 v253, v254 offset:16\n"
      "v_fmamk_f32 v144, v144, 0x3e0293ee, v244\n"
      "v_fmamk_f32 v148, v148, 0x3e0293ee, v245\n"
      "v_fmamk_f32 v152, v152, 0x3e0293ee, v246\n"
      "v_fmamk_f32 v145, v145, 0x3e0293ee, v244\n"
      "v_fmamk_f32 v149, v149, 0x3e0293ee, v245\n"
      "v_fmamk_f32 v153, v153, 0x3e0293ee, v246\n"
      "v_exp_f32 v144, v144\n"
      "v_exp_f32 v148, v148\n"
      "v_exp_f32 v152, v152\n"
      "v_exp_f32 v145, v145\n"
      "v_exp_f32 v149, v149\n"
      "v_exp_f32 v153, v153\n"
      "v_add_f32 v238, v144, v238\n"
      "v_add_f32 v239, v148, v239\n"
      "v_add_f32 v240, v152, v240\n"
      "v_add_f32 v238, v145, v238\n"
      "v_add_f32 v239, v149, v239\n"
      "v_add_f32 v240, v153, v240\n"
      "v_cvt_pk_bf16_f32 v144, v144, v145\n"
      "v_cvt_pk_bf16_f32 v148, v148, v149\n"
      "v_cvt_pk_bf16_f32 v152, v152, v153\n"
      "v_fmamk_f32 v146, v146, 0x3e0293ee, v244\n"
      "v_fmamk_f32 v150, v150, 0x3e0293ee, v245\n"
      "v_fmamk_f32 v154, v154, 0x3e0293ee, v246\n"
      "v_fmamk_f32 v147, v147, 0x3e0293ee, v244\n"
      "v_fmamk_f32 v151, v151, 0x3e0293ee, v245\n"
      "v_fmamk_f32 v155, v155, 0x3e0293ee, v246\n"
      "v_exp_f32 v146, v146\n"
      "v_exp_f32 v150, v150\n"
      "v_exp_f32 v154, v154\n"
      "v_exp_f32 v147, v147\n"
      "v_exp_f32 v151, v151\n"
      "v_exp_f32 v155, v155\n"
      "v_add_f32 v238, v146, v238\n"
      "v_add_f32 v239, v150, v239\n"
      "v_add_f32 v240, v154, v240\n"
      "v_add_f32 v238, v147, v238\n"
      "v_add_f32 v239, v151, v239\n"
      "v_add_f32 v240, v155, v240\n"
      "v_cvt_pk_bf16_f32 v145, v146, v147\n"
      "v_cvt_pk_bf16_f32 v149, v150, v151\n"
      "v_cvt_pk_bf16_f32 v153, v154, v155\n"
      "v_fmamk_f32 v192, v192, 0x3e0293ee, v244\n"
      "v_fmamk_f32 v196, v196, 0x3e0293ee, v245\n"
      "v_fmamk_f32 v200, v200, 0x3e0293ee, v246\n"
      "v_fmamk_f32 v193, v193, 0x3e0293ee, v244\n"
      "v_fmamk_f32 v197, v197, 0x3e0293ee, v245\n"
      "v_fmamk_f32 v201, v201, 0x3e0293ee, v246\n"
      "v_exp_f32 v192, v192\n"
      "v_exp_f32 v196, v196\n"
      "v_exp_f32 v200, v200\n"
      "v_exp_f32 v193, v193\n"
      "v_exp_f32 v197, v197\n"
      "v_exp_f32 v201, v201\n"
      "v_add_f32 v238, v192, v238\n"
      "v_add_f32 v239, v196, v239\n"
      "v_add_f32 v240, v200, v240\n"
      "v_add_f32 v238, v193, v238\n"
      "v_add_f32 v239, v197, v239\n"
      "v_add_f32 v240, v201, v240\n"
      "v_cvt_pk_bf16_f32 v146, v192, v193\n"
      "v_cvt_pk_bf16_f32 v150, v196, v197\n"
      "v_cvt_pk_bf16_f32 v154, v200, v201\n"
      "v_fmamk_f32 v194, v194, 0x3e0293ee, v244\n"
      "v_fmamk_f32 v198, v198, 0x3e0293ee, v245\n"
      "v_fmamk_f32 v202, v202, 0x3e0293ee, v246\n"
      "v_fmamk_f32 v195, v195, 0x3e0293ee, v244\n"
      "v_fmamk_f32 v199, v199, 0x3e0293ee, v245\n"
      "v_fmamk_f32 v203, v203, 0x3e0293ee, v246\n"
      "v_exp_f32 v194, v194\n"
      "v_exp_f32 v198, v198\n"
      "v_exp_f32 v202, v202\n"
      "v_exp_f32 v195, v195\n"
      "v_exp_f32 v199, v199\n"
      "v_exp_f32 v203, v203\n"
      "v_add_f32 v238, v194, v238\n"
      "v_add_f32 v239, v198, v239\n"
      "v_add_f32 v240, v202, v240\n"
      "v_add_f32 v238, v195, v238\n"
      "v_add_f32 v239, v199, v239\n"
      "v_add_f32 v240, v203, v240\n"
      "v_cvt_pk_bf16_f32 v147, v194, v195\n"
      "v_cvt_pk_bf16_f32 v151, v198, v199\n"
      "v_cvt_pk_bf16_f32 v155, v202, v203\n"
      "s_waitcnt lgkmcnt(0)\n"
      "v_readfirstlane_b32 %[t1], v253\n"
      "s_cmp_eq_u32 %[t1], 0x01010101\n"
      "s_cbranch_scc1 .Latt1_d\n"
      "s_mov_b32 %[t0], 0x100000\n"
      "v_mov_b32 v254, %[sf]\n"
      ".Latt1_l:\n"
      "s_sleep 1\n"
      "ds_read_b32 v253, v254 offset:16\n"
      "s_waitcnt lgkmcnt(0)\n"
      "v_readfirstlane_b32 %[t1], v253\n"
      "s_cmp_eq_u32 %[t1], 0x01010101\n"
      "s_cbranch_scc1 .Latt1_d\n"
      "s_sub_u32 %[t0], %[t0], 1\n"
      "s_cbranch_scc0 .Latt1_l\n"
      ".Latt1_d:\n"
      "v_add_u32 v247, %[sk], v251\n"
      "v_xor_b32 v248, 64, v247\n"
      "v_xor_b32 v249, 128, v247\n"
      "v_xor_b32 v250, 192, v247\n"
      "ds_read_b128 v[216:219], v247 offset:0\n"
      "ds_read_b128 v[220:223], v248 offset:0\n"
      "ds_read_b128 v[224:227], v249 offset:0\n"
      "v_fmamk_f32 v156, v156, 0x3e0293ee, v244\n"
      "v_fmamk_f32 v160, v160, 0x3e0293ee, v245\n"
      "v_fmamk_f32 v164, v164, 0x3e0293ee, v246\n"
      "v_fmamk_f32 v157, v157, 0x3e0293ee, v244\n"
      "v_fmamk_f32 v161, v161, 0x3e0293ee, v245\n"
      "v_fmamk_f32 v165, v165, 0x3e0293ee, v246\n"
      "v_exp_f32 v156, v156\n"
      "v_exp_f32 v160, v160\n"
      "v_exp_f32 v164, v164\n"
      "v_exp_f32 v157, v157\n"
      "v_exp_f32 v161, v161\n"
      "v_exp_f32 v165, v165\n"
      "v_add_f32 v238, v156, v238\n"
      "v_add_f32 v239, v160, v239\n"
      "v_add_f32 v240, v164, v240\n"
      "v_add_f32 v238, v157, v238\n"
      "v_add_f32 v239, v161, v239\n"
      "v_add_f32 v240, v165, v240\n"
      "v_cvt_pk_bf16_f32 v156, v156, v157\n"
      "v_cvt_pk_bf16_f32 v160, v160, v161\n"
      "v_cvt_pk_bf16_f32 v164, v164, v165\n"
      "v_fmamk_f32 v158, v158, 0x3e0293ee, v244\n"
      "v_fmamk_f32 v162, v162, 0x3e0293ee, v245\n"
      "v_fmamk_f32 v166, v166, 0x3e0293ee, v246\n"
      "v_fmamk_f32 v159, v159, 0x3e0293ee, v244\n"
      "v_fmamk_f32 v163, v163, 0x3e0293ee, v245\n"
      "v_fmamk_f32 v167, v167, 0x3e0293ee, v246\n"
      "v_exp_f32 v158, v158\n"
      "v_exp_f32 v162, v162\n"
      "v_exp_f32 v166, v166\n"
      "v_exp_f32 v159, v159\n"
      "v_exp_f32 v163, v163\n"
      "v_exp_f32 v167, v167\n"
      "v_add_f32 v238, v158, v238\n"
      "v_add_f32 v239, v162, v239\n"
      "v_add_f32 v240, v166, v240\n"
      "v_add_f32 v238, v159, v238\n"
      "v_add_f32 v239, v163, v239\n"
      "v_add_f32 v240, v167, v240\n"
      "v_cvt_pk_bf16_f32 v157, v158, v159\n"
      "v_cvt_pk_bf16_f32 v161, v162, v163\n"
      "v_cvt_pk_bf16_f32 v165, v166, v167\n"
      "v_fmamk_f32 v204, v204, 0x3e0293ee, v244\n"
      "v_fmamk_f32 v208, v208, 0x3e0293ee, v245\n"
      "v_fmamk_f32 v212, v212, 0x3e0293ee, v246\n"
      "v_fmamk_f32 v205, v205, 0x3e0293ee, v244\n"
      "v_fmamk_f32 v209, v209, 0x3e0293ee, v245\n"
      "v_fmamk_f32 v213, v213, 0x3e0293ee, v246\n"
      "v_exp_f32 v204, v204\n"
      "v_exp_f32 v208, v208\n"
      "v_exp_f32 v212, v212\n"
      "v_exp_f32 v205, v205\n"
      "v_exp_f32 v209, v209\n"
      "v_exp_f32 v213, v213\n"
      "v_add_f32 v238, v204, v238\n"
      "v_add_f32 v239, v208, v239\n"
      "v_add_f32 v240, v212, v240\n"
      "v_add_f32 v238, v205, v238\n"
      "v_add_f32 v239, v209, v239\n"
      "v_add_f32 v240, v213, v240\n"
      "v_cvt_pk_bf16_f32 v158, v204, v205\n"
      "v_cvt_pk_bf16_f32 v162, v208, v209\n"
      "v_cvt_pk_bf16_f32 v166, v212, v213\n"
      "v_fmamk_f32 v206, v206, 0x3e0293ee, v244\n"
      "v_fmamk_f32 v210, v210, 0x3e0293ee, v245\n"
      "v_fmamk_f32 v214, v214, 0x3e0293ee, v246\n"
      "v_fmamk_f32 v207, v207, 0x3e0293ee, v244\n"
      "v_fmamk_f32 v211, v211, 0x3e0293ee, v245\n"
      "v_fmamk_f32 v215, v215, 0x3e0293ee, v246\n"
      "v_exp_f32 v206, v206\n"
      "v_exp_f32 v210, v210\n"
      "v_exp_f32 v214, v214\n"
      "v_exp_f32 v207, v207\n"
      "v_exp_f32 v211, v211\n"
      "v_exp_f32 v215, v215\n"
      "v_add_f32 v238, v206, v238\n"
      "v_add_f32 v239, v210, v239\n"
      "v_add_f32 v240, v214, v240\n"
      "v_add_f32 v238, v207, v238\n"
      "v_add_f32 v239, v211, v239\n"
      "v_add_f32 v240, v215, v240\n"
      "v_cvt_pk_bf16_f32 v159, v206, v207\n"
      "v_cvt_pk_bf16_f32 v163, v210, v211\n"
      "v_cvt_pk_bf16_f32 v167, v214, v215\n"
      "s_add_u32 %[sf], %[sf], 8\n"
      "s_add_u32 %[si], %[si], 2\n"
      "s_mov_b32 %[cnt], 3\n"
      ".Latt_loop:\n"
      ATS
      "s_waitcnt lgkmcnt(2)\n"
      "v_mfma_f32_16x16x32_bf16 v[168:171], v[216:219], v[96:99], 0\n"
      "ds_read_b128 v[228:231], v250 offset:0\n"
      "v_mul_f32 v0, v241, v0\n"
      "v_mul_f32 v4, v242, v4\n"
      "v_mul_f32 v8, v243, v8\n"
      "v_mfma_f32_16x16x32_bf16 v[172:175], v[216:219], v[100:103], 0\n"
      "v_mul_f32 v1, v241, v1\n"
      "v_mul_f32 v5, v242, v5\n"
      "v_mfma_f32_16x16x32_bf16 v[176:179], v[216:219], v[104:107], 0\n"
      "v_mul_f32 v9, v243, v9\n"
      "v_mul_f32 v2, v241, v2\n"
      "v_mul_f32 v6, v242, v6\n"
      "s_waitcnt lgkmcnt(2)\n"
      "v_mfma_f32_16x16x32_bf16 v[168:171], v[220:223], v[108:111], v[168:171]\n"
      "ds_read_b128 v[216:219], v247 offset:4096\n"
      "v_mul_f32 v10, v243, v10\n"
      "v_mul_f32 v3, v241, v3\n"
      "v_mfma_f32_16x16x32_bf16 v[172:175], v[220:223], v[112:115], v[172:175]\n"
      "v_mul_f32 v7, v242, v7\n"
      "v_mul_f32 v11, v243, v11\n"
      "v_mfma_f32_16x16x32_bf16 v[176:179], v[220:223], v[116:119], v[176:179]\n"
      "v_mul_f32 v12, v241, v12\n"
      "v_mul_f32 v16, v242, v16\n"
      "v_mul_f32 v20, v243, v20\n"
      "s_waitcnt lgkmcnt(2)\n"
      "v_mfma_f32_16x16x32_bf16 v[168:171], v[224:227], v[120:123], v[168:171]\n"
      "ds_read_b128 v[220:223], v248 offset:4096\n"
      "v_mul_f32 v13, v241, v13\n"
      "v_mul_f32 v17, v242, v17\n"
      "v_mfma_f32_16x16x32_bf16 v[172:175], v[224:227], v[124:127], v[172:175]\n"
      "v_mul_f32 v21, v243, v21\n"
      "v_mul_f32 v14, v241, v14\n"
      "v_mfma_f32_16x16x32_bf16 v[176:179], v[224:227], v[128:131], v[176:179]\n"
      "v_mul_f32 v18, v242, v18\n"
      "v_mul_f32 v22, v243, v22\n"
      "v_mul_f32 v15, v241, v15\n"
      "s_waitcnt lgkmcnt(2)\n"
      "v_mfma_f32_16x16x32_bf16 v[168:171], v[228:231], v[132:135], v[168:171]\n"
      "ds_read_b128 v[224:227], v249 offset:4096\n"
      "v_mul_f32 v19, v242, v19\n"
      "v_mul_f32 v23, v243, v23\n"
      "v_mfma_f32_16x16x32_bf16 v[172:175], v[228:231], v[136:139], v[172:175]\n"
      "v_mul_f32 v24, v241, v24\n"
      "v_mul_f32 v28, v242, v28\n"
      "v_mul_f32 v32, v243, v32\n"
      "v_mfma_f32_16x16x32_bf16 v[176:179], v[228:231], v[140:143], v[176:179]\n"
      "v_mul_f32 v25, v241, v25\n"
      "v_mul_f32 v29, v242, v29\n"
      "s_waitcnt lgkmcnt(2)\n"
      "v_mfma_f32_16x16x32_bf16 v[192:195], v[216:219], v[96:99], 0\n"
      "ds_read_b128 v[228:231], v250 offset:4096\n"
      "v_mul_f32 v33, v243, v33\n"
      "v_mul_f32 v26, v241, v26\n"
      "v_mfma_f32_16x16x32_bf16 v[196:199], v[216:219], v[100:103], 0\n"
      "v_mul_f32 v30, v242, v30\n"
      "v_mul_f32 v34, v243, v34\n"
      "v_mul_f32 v27, v241, v27\n"
      "v_mfma_f32_16x16x32_bf16 v[200:203], v[216:219], v[104:107], 0\n"
      "v_mul_f32 v31, v242, v31\n"
      "v_mul_f32 v35, v243, v35\n"
      "s_waitcnt lgkmcnt(2)\n"
      "v_mfma_f32_16x16x32_bf16 v[192:195], v[220:223], v[108:111], v[192:195]\n"
      "ds_read_b128 v[216:219], v247 offset:8192\n"
      "v_mul_f32 v36, v241, v36\n"
      "v_mul_f32 v40, v242, v40\n"
      "v_mfma_f32_16x16x32_bf16 v[196:199], v[220:223], v[112:115], v[196:199]\n"
      "v_mov_b32 v254, %[sf]\n"
      "v_mul_f32 v44, v243, v44\n"
      "v_mul_f32 v37, v241, v37\n"
      "v_mul_f32 v41, v242, v41\n"
      "v_mfma_f32_16x16x32_bf16 v[200:203], v[220:223], v[116:119], v[200:203]\n"
      "ds_read_b32 v253, v254 offset:0\n"
      "v_mul_f32 v45, v243, v45\n"
      "v_mul_f32 v38, v241, v38\n"
      "s_waitcnt lgkmcnt(3)\n"
      "v_mfma_f32_16x16x32_bf16 v[192:195], v[224:227], v[120:123], v[192:195]\n"
      "ds_read_b128 v[220:223], v248 offset:8192\n"
      "v_mul_f32 v42, v242, v42\n"
      "v_mul_f32 v46, v243, v46\n"
      "v_mul_f32 v39, v241, v39\n"
      "v_mfma_f32_16x16x32_bf16 v[196:199], v[224:227], v[124:127], v[196:199]\n"
      "v_mul_f32 v43, v242, v43\n"
      "v_mul_f32 v47, v243, v47\n"
      "v_mfma_f32_16x16x32_bf16 v[200:203], v[224:227], v[128:131], v[200:203]\n"
      "v_max3_f32 v235, v232, v168, v169\n"
      "v_max3_f32 v236, v233, v172, v173\n"
      "s_waitcnt lgkmcnt(3)\n"
      "v_mfma_f32_16x16x32_bf16 v[192:195], v[228:231], v[132:135], v[192:195]\n"
      "ds_read_b128 v[224:227], v249 offset:8192\n"
      "v_max3_f32 v237, v234, v176, v177\n"
      "v_max3_f32 v235, v235, v170, v171\n"
      "v_max3_f32 v236, v236, v174, v175\n"
      "v_mfma_f32_16x16x32_bf16 v[196:199], v[228:231], v[136:139], v[196:199]\n"
      "v_max3_f32 v237, v237, v178, v179\n"
      "v_mul_f32 v48, v241, v48\n"
      "v_mfma_f32_16x16x32_bf16 v[200:203], v[228:231], v[140:143], v[200:203]\n"
      "v_mul_f32 v52, v242, v52\n"
      "v_mul_f32 v56, v243, v56\n"
      "s_waitcnt lgkmcnt(3)\n"
      "v_mfma_f32_16x16x32_bf16 v[180:183], v[216:219], v[96:99], 0\n"
      "ds_read_b128 v[228:231], v250 offset:8192\n"
      "v_mul_f32 v49, v241, v49\n"
      "v_mul_f32 v53, v242, v53\n"
      "v_mul_f32 v57, v243, v57\n"
      "v_mfma_f32_16x16x32_bf16 v[184:187], v[216:219], v[100:103], 0\n"
      "v_mul_f32 v50, v241, v50\n"
      "v_mul_f32 v54, v242, v54\n"
      "v_mfma_f32_16x16x32_bf16 v[188:191], v[216:219], v[104:107], 0\n"
      "v_mul_f32 v58, v243, v58\n"
      "v_mul_f32 v51, v241, v51\n"
      "v_mul_f32 v55, v242, v55\n"
      "s_waitcnt lgkmcnt(2)\n"
      "v_mfma_f32_16x16x32_bf16 v[180:183], v[220:223], v[108:111], v[180:183]\n"
      "ds_read_b128 v[216:219], v247 offset:12288\n"
      "v_mul_f32 v59, v243, v59\n"
      "v_mul_f32 v60, v241, v60\n"
      "v_mfma_f32_16x16x32_bf16 v[184:187], v[220:223], v[112:115], v[184:187]\n"
      "v_mul_f32 v64, v242, v64\n"
      "v_mul_f32 v68, v243, v68\n"
      "v_mfma_f32_16x16x32_bf16 v[188:191], v[220:223], v[116:119], v[188:191]\n"
      "v_mul_f32 v61, v241, v61\n"
      "v_mul_f32 v65, v242, v65\n"
      "v_mul_f32 v69, v243, v69\n"
      "s_waitcnt lgkmcnt(2)\n"
      "v_mfma_f32_16x16x32_bf16 v[180:183], v[224:227], v[120:123], v[180:183]\n"
      "ds_read_b128 v[220:223], v248 offset:12288\n"
      "v_mul_f32 v62, v241, v62\n"
      "v_mul_f32 v66, v242, v66\n"
      "v_mfma_f32_16x16x32_bf16 v[184:187], v[224:227], v[124:127], v[184:187]\n"
      "v_mul_f32 v70, v243, v70\n"
      "v_mul_f32 v63, v241, v63\n"
      "v_mfma_f32_16x16x32_bf16 v[188:191], v[224:227], v[128:131], v[188:191]\n"
      "v_mul_f32 v67, v242, v67\n"
      "v_mul_f32 v71, v243, v71\n"
      "v_max3_f32 v235, v235, v192, v193\n"
      "s_waitcnt lgkmcnt(2)\n"
      "v_mfma_f32_16x16x32_bf16 v[180:183], v[228:231], v[132:135], v[180:183]\n"
      "ds_read_b128 v[224:227], v249 offset:12288\n"
      "v_max3_f32 v236, v236, v196, v197\n"
      "v_max3_f32 v237, v237, v200, v201\n"
      "v_mfma_f32_16x16x32_bf16 v[184:187], v[228:231], v[136:139], v[184:187]\n"
      "v_max3_f32 v235, v235, v194, v195\n"
      "v_max3_f32 v236, v236, v198, v199\n"
      "v_max3_f32 v237, v237, v202, v203\n"
      "v_mfma_f32_16x16x32_bf16 v[188:191], v[228:231], v[140:143], v[188:191]\n"
      "v_mul_f32 v72, v241, v72\n"
      "v_mul_f32 v76, v242, v76\n"
      "s_waitcnt lgkmcnt(2)\n"
      "v_mfma_f32_16x16x32_bf16 v[204:207], v[216:219], v[96:99], 0\n"
      "ds_read_b128 v[228:231], v250 offset:12288\n"
      "v_readfirstlane_b32 %[t1], v253\n"
      "s_add_u32 %[t0], %[si], 2\n"
      "v_mul_f32 v80, v243, v80\n"
      "v_mul_f32 v73, v241, v73\n"
      "v_mfma_f32_16x16x32_bf16 v[208:211], v[216:219], v[100:103], 0\n"
      "v_mov_b32 v253, %[t0]\n"
      "v_mov_b32 v254, %[sc]\n"
      "v_mul_f32 v77, v242, v77\n"
      "v_mul_f32 v81, v243, v81\n"
      "v_mul_f32 v74, v241, v74\n"
      "v_mfma_f32_16x16x32_bf16 v[212:215], v[216:219], v[104:107], 0\n"
      "ds_write_b32 v254, v253 offset:0\n"
      "s_cmp_eq_u32 %[t1], 0x01010101\n"
      "s_cbranch_scc1 .Latt2_d\n"
      "s_mov_b32 %[t0], 0x100000\n"
      "v_mov_b32 v254, %[sf]\n"
      ".Latt2_l:\n"
      "s_sleep 1\n"
      "ds_read_b32 v253, v254 offset:0\n"
      "s_waitcnt lgkmcnt(0)\n"
      "v_readfirstlane_b32 %[t1], v253\n"
      "s_cmp_eq_u32 %[t1], 0x01010101\n"
      "s_cbranch_scc1 .Latt2_d\n"
      "s_sub_u32 %[t0], %[t0], 1\n"
      "s_cbranch_scc0 .Latt2_l\n"
      ".Latt2_d:\n"
      "v_mul_f32 v78, v242, v78\n"
      "v_mul_f32 v82, v243, v82\n"
      "s_waitcnt lgkmcnt(3)\n"
      "v_mfma_f32_16x16x32_bf16 v[204:207], v[220:223], v[108:111], v[204:207]\n"
      "v_add_u32 v247, %[sv], v252\n"
      "v_mul_f32 v75, v241, v75\n"
      "v_mul_f32 v79, v242, v79\n"
      "v_mfma_f32_16x16x32_bf16 v[208:211], v[220:223], v[112:115], v[208:211]\n"
      "ds_read_b64_tr_b16 v[216:217], v247 offset:0\n"
      "v_mul_f32 v83, v243, v83\n"
      "v_mul_f32 v84, v241, v84\n"
      "v_mul_f32 v88, v242, v88\n"
      "v_mfma_f32_16x16x32_bf16 v[212:215], v[220:223], v[116:119], v[212:215]\n"
      "ds_read_b64_tr_b16 v[218:219], v247 offset:4096\n"
      "v_mul_f32 v92, v243, v92\n"
      "v_mul_f32 v85, v241, v85\n"
      "s_waitcnt lgkmcnt(4)\n"
      "v_mfma_f32_16x16x32_bf16 v[204:207], v[224:227], v[120:123], v[204:207]\n"
      "ds_read_b64_tr_b16 v[220:221], v247 offset:8192\n"
      "ds_read_b64_tr_b16 v[222:223], v247 offset:12288\n"
      "v_mul_f32 v89, v242, v89\n"
      "v_mul_f32 v93, v243, v93\n"
      "v_mul_f32 v86, v241, v86\n"
      "v_mfma_f32_16x16x32_bf16 v[208:211], v[224:227], v[124:127], v[208:211]\n"
      "s_add_u32 %[sk], %[sk], 0x10000\n"
      "s_cmp_ge_u32 %[sk], 0x24000\n"
      "v_mul_f32 v90, v242, v90\n"
      "v_mul_f32 v94, v243, v94\n"
      "v_mfma_f32_16x16x32_bf16 v[212:215], v[224:227], v[128:131], v[212:215]\n"
      "s_cselect_b32 %[t0], 0x24000, 0\n"
      "s_sub_u32 %[sk], %[sk], %[t0]\n"
      "v_mul_f32 v87, v241, v87\n"
      "v_mul_f32 v91, v242, v91\n"
      "s_waitcnt lgkmcnt(5)\n"
      "v_mfma_f32_16x16x32_bf16 v[204:207], v[228:231], v[132:135], v[204:207]\n"
      "v_xor_b32 v249, 32, v247\n"
      "v_mul_f32 v95, v243, v95\n"
      "v_max3_f32 v235, v235, v180, v181\n"
      "v_max3_f32 v236, v236, v184, v185\n"
      "v_mfma_f32_16x16x32_bf16 v[208:211], v[228:231], v[136:139], v[208:211]\n"
      "ds_read_b64_tr_b16 v[224:225], v249 offset:0\n"
      "v_max3_f32 v237, v237, v188, v189\n"
      "v_max3_f32 v235, v235, v182, v183\n"
      "v_mfma_f32_16x16x32_bf16 v[212:215], v[228:231], v[140:143], v[212:215]\n"
      "ds_read_b64_tr_b16 v[226:227], v249 offset:4096\n"
      "v_max3_f32 v236, v236, v186, v187\n"
      "v_max3_f32 v237, v237, v190, v191\n"
      ATS
      "s_waitcnt lgkmcnt(4)\n"
      "v_mfma_f32_16x16x32_bf16 v[0:3], v[216:219], v[144:147], v[0:3]\n"
      "ds_read_b64_tr_b16 v[228:229], v249 offset:8192\n"
      "v_mfma_f32_16x16x32_bf16 v[4:7], v[216:219], v[148:151], v[4:7]\n"
      "ds_read_b64_tr_b16 v[230:231], v249 offset:12288\n"
      "v_mfma_f32_16x16x32_bf16 v[8:11], v[216:219], v[152:155], v[8:11]\n"
      "v_max3_f32 v235, v235, v204, v205\n"
      "v_max3_f32 v236, v236, v208, v209\n"
      "v_max3_f32 v237, v237, v212, v213\n"
      "v_max3_f32 v235, v235, v206, v207\n"
      "v_max3_f32 v236, v236, v210, v211\n"
      "v_max3_f32 v237, v237, v214, v215\n"
      "v_mov_b32 v241, v235\n"
      "v_mov_b32 v242, v236\n"
      "v_mov_b32 v243, v237\n"
      "v_permlane32_swap_b32 v235, v241\n"
      "v_permlane32_swap_b32 v236, v242\n"
      "v_permlane32_swap_b32 v237, v243\n"
      "v_max_f32 v235, v235, v241\n"
      "v_max_f32 v236, v236, v242\n"
      "v_max_f32 v237, v237, v243\n"
      "v_mov_b32 v241, v235\n"
      "v_mov_b32 v242, v236\n"
      "s_waitcnt lgkmcnt(4)\n"
      "v_mfma_f32_16x16x32_bf16 v[0:3], v[220:223], v[156:159], v[0:3]\n"
      "v_xor_b32 v250, 64, v247\n"
      "v_mov_b32 v243, v237\n"
      "v_permlane16_swap_b32 v235, v241\n"
      "v_permlane16_swap_b32 v236, v242\n"
      "v_permlane16_swap_b32 v237, v243\n"
      "v_max_f32 v235, v235, v241\n"
      "v_mfma_f32_16x16x32_bf16 v[4:7], v[220:223], v[160:163], v[4:7]\n"
      "ds_read_b64_tr_b16 v[216:217], v250 offset:0\n"
      "v_max_f32 v236, v236, v242\n"
      "v_max_f32 v237, v237, v243\n"
      "v_sub_f32 v241, 0, v244\n"
      "v_sub_f32 v242, 0, v245\n"
      "v_sub_f32 v243, 0, v246\n"
      "v_mfma_f32_16x16x32_bf16 v[8:11], v[220:223], v[164:167], v[8:11]\n"
      "ds_read_b64_tr_b16 v[218:219], v250 offset:4096\n"
      "v_mul_f32 v244, 0xbe0293ee, v235\n"
      "v_mul_f32 v245, 0xbe0293ee, v236\n"
      "v_mul_f32 v246, 0xbe0293ee, v237\n"
      "v_add_f32 v241, v244, v241\n"
      "v_add_f32 v242, v245, v242\n"
      "v_add_f32 v243, v246, v243\n"
      "s_waitcnt lgkmcnt(4)\n"
      "v_mfma_f32_16x16x32_bf16 v[12:15], v[224:227], v[144:147], v[12:15]\n"
      "ds_read_b64_tr_b16 v[220:221], v250 offset:8192\n"
      "v_exp_f32 v241, v241\n"
      "v_exp_f32 v242, v242\n"
      "v_exp_f32 v243, v243\n"
      "v_mfma_f32_16x16x32_bf16 v[16:19], v[224:227], v[148:151], v[16:19]\n"
      "ds_read_b64_tr_b16 v[222:223], v250 offset:12288\n"
      "v_mul_f32 v238, v241, v238\n"
      "v_mul_f32 v239, v242, v239\n"
      "v_mul_f32 v240, v243, v240\n"
      "v_fmamk_f32 v168, v168, 0x3e0293ee, v244\n"
      "v_mfma_f32_16x16x32_bf16 v[20:23], v[224:227], v[152:155], v[20:23]\n"
      "v_fmamk_f32 v172, v172, 0x3e0293ee, v245\n"
      "v_fmamk_f32 v176, v176, 0x3e0293ee, v246\n"
      "v_fmamk_f32 v169, v169, 0x3e0293ee, v244\n"
      "v_fmamk_f32 v173, v173, 0x3e0293ee, v245\n"
      "v_fmamk_f32 v177, v177, 0x3e0293ee, v246\n"
      "v_exp_f32 v168, v168\n"
      "s_waitcnt lgkmcnt(4)\n"
      "v_mfma_f32_16x16x32_bf16 v[12:15], v[228:231], v[156:159], v[12:15]\n"
      "v_xor_b32 v248, 96, v247\n"
      "v_exp_f32 v172, v172\n"
      "v_exp_f32 v176, v176\n"
      "v_mfma_f32_16x16x32_bf16 v[16:19], v[228:231], v[160:163], v[16:19]\n"
      "ds_read_b64_tr_b16 v[224:225], v248 offset:0\n"
      "v_exp_f32 v169, v169\n"
      "v_exp_f32 v173, v173\n"
      "v_exp_f32 v177, v177\n"
      "v_mfma_f32_16x16x32_bf16 v[20:23], v[228:231], v[164:167], v[20:23]\n"
      "ds_read_b64_tr_b16 v[226:227], v248 offset:4096\n"
      "v_add_f32 v238, v168, v238\n"
      "v_add_f32 v239, v172, v239\n"
      "v_add_f32 v240, v176, v240\n"
      "v_add_f32 v238, v169, v238\n"
      "v_add_f32 v239, v173, v239\n"
      "s_waitcnt lgkmcnt(4)\n"
      "v_mfma_f32_16x16x32_bf16 v[24:27], v[216:219], v[144:147], v[24:27]\n"
      "ds_read_b64_tr_b16 v[228:229], v248 offset:8192\n"
      "v_add_f32 v240, v177, v240\n"
      "v_cvt_pk_bf16_f32 v168, v168, v169\n"
      "v_cvt_pk_bf16_f32 v172, v172, v173\n"
      "v_cvt_pk_bf16_f32 v176, v176, v177\n"
      "v_fmamk_f32 v170, v170, 0x3e0293ee, v244\n"
      "v_mfma_f32_16x16x32_bf16 v[28:31], v[216:219], v[148:151], v[28:31]\n"
      "ds_read_b64_tr_b16 v[230:231], v248 offset:12288\n"
      "v_fmamk_f32 v174, v174, 0x3e0293ee, v245\n"
      "v_fmamk_f32 v178, v178, 0x3e0293ee, v246\n"
      "v_fmamk_f32 v171, v171, 0x3e0293ee, v244\n"
      "v_fmamk_f32 v175, v175, 0x3e0293ee, v245\n"
      "v_fmamk_f32 v179, v179, 0x3e0293ee, v246\n"
      "v_exp_f32 v170, v170\n"
      "v_mfma_f32_16x16x32_bf16 v[32:35], v[216:219], v[152:155], v[32:35]\n"
      "v_exp_f32 v174, v174\n"
      "v_exp_f32 v178, v178\n"
      "s_cmp_eq_u32 %[cnt], 0\n"
      "s_cbranch_scc1 .Latt_exit\n"
      "s_waitcnt lgkmcnt(4)\n"
      "v_mfma_f32_16x16x32_bf16 v[24:27], v[220:223], v[156:159], v[24:27]\n"
      "v_xor_b32 v249, 128, v247\n"
      "ds_read_b64_tr_b16 v[216:217], v249 offset:0\n"
      "v_exp_f32 v171, v171\n"
      "v_exp_f32 v175, v175\n"
      "v_exp_f32 v179, v179\n"
      "v_mfma_f32_16x16x32_bf16 v[28:31], v[220:223], v[160:163], v[28:31]\n"
      "ds_read_b64_tr_b16 v[218:219], v249 offset:4096\n"
      "v_mov_b32 v254, %[sf]\n"
      "v_add_f32 v238, v170, v238\n"
      "v_add_f32 v239, v174, v239\n"
      "v_add_f32 v240, v178, v240\n"
      "v_add_f32 v238, v171, v238\n"
      "v_add_f32 v239, v175, v239\n"
      "v_mfma_f32_16x16x32_bf16 v[32:35], v[220:223], v[164:167], v[32:35]\n"
      "ds_read_b32 v253, v254 offset:24\n"
      "v_add_f32 v240, v179, v240\n"
      "v_cvt_pk_bf16_f32 v169, v170, v171\n"
      "v_cvt_pk_bf16_f32 v173, v174, v175\n"
      "v_cvt_pk_bf16_f32 v177, v178, v179\n"
      "v_fmamk_f32 v192, v192, 0x3e0293ee, v244\n"
      "s_waitcnt lgkmcnt(5)\n"
      "v_mfma_f32_16x16x32_bf16 v[36:39], v[224:227], v[144:147], v[36:39]\n"
      "ds_read_b64_tr_b16 v[220:221], v249 offset:8192\n"
      "v_fmamk_f32 v196, v196, 0x3e0293ee, v245\n"
      "v_fmamk_f32 v200, v200, 0x3e0293ee, v246\n"
      "v_fmamk_f32 v193, v193, 0x3e0293ee, v244\n"
      "v_fmamk_f32 v197, v197, 0x3e0293ee, v245\n"
      "v_fmamk_f32 v201, v201, 0x3e0293ee, v246\n"
      "v_exp_f32 v192, v192\n"
      "v_mfma_f32_16x16x32_bf16 v[40:43], v[224:227], v[148:151], v[40:43]\n"
      "ds_read_b64_tr_b16 v[222:223], v249 offset:12288\n"
      "v_exp_f32 v196, v196\n"
      "v_exp_f32 v200, v200\n"
      "v_mfma_f32_16x16x32_bf16 v[44:47], v[224:227], v[152:155], v[44:47]\n"
      "v_exp_f32 v193, v193\n"
      "v_exp_f32 v197, v197\n"
      "v_exp_f32 v201, v201\n"
      "s_waitcnt lgkmcnt(5)\n"
      "v_mfma_f32_16x16x32_bf16 v[36:39], v[228:231], v[156:159], v[36:39]\n"
      "v_xor_b32 v250, 160, v247\n"
      "v_add_f32 v238, v192, v238\n"
      "v_add_f32 v239, v196, v239\n"
      "v_add_f32 v240, v200, v240\n"
      "v_add_f32 v238, v193, v238\n"
      "v_add_f32 v239, v197, v239\n"
      "v_mfma_f32_16x16x32_bf16 v[40:43], v[228:231], v[160:163], v[40:43]\n"
      "ds_read_b64_tr_b16 v[224:225], v250 offset:0\n"
      "v_add_f32 v240, v201, v240\n"
      "v_cvt_pk_bf16_f32 v170, v192, v193\n"
      "v_cvt_pk_bf16_f32 v174, v196, v197\n"
      "v_cvt_pk_bf16_f32 v178, v200, v201\n"
      "v_fmamk_f32 v194, v194, 0x3e0293ee, v244\n"
      "v_mfma_f32_16x16x32_bf16 v[44:47], v[228:231], v[164:167], v[44:47]\n"
      "ds_read_b64_tr_b16 v[226:227], v250 offset:4096\n"
      "v_fmamk_f32 v198, v198, 0x3e0293ee, v245\n"
      "v_fmamk_f32 v202, v202, 0x3e0293ee, v246\n"
      "v_fmamk_f32 v195, v195, 0x3e0293ee, v244\n"
      "v_fmamk_f32 v199, v199, 0x3e0293ee, v245\n"
      "v_fmamk_f32 v203, v203, 0x3e0293ee, v246\n"
      "s_waitcnt lgkmcnt(5)\n"
      "v_mfma_f32_16x16x32_bf16 v[48:51], v[216:219], v[144:147], v[48:51]\n"
      "ds_read_b64_tr_b16 v[228:229], v250 offset:8192\n"
      "v_exp_f32 v194, v194\n"
      "v_exp_f32 v198, v198\n"
      "v_exp_f32 v202, v202\n"
      "v_mfma_f32_16x16x32_bf16 v[52:55], v[216:219], v[148:151], v[52:55]\n"
      "ds_read_b64_tr_b16 v[230:231], v250 offset:12288\n"
      "v_exp_f32 v195, v195\n"
      "v_exp_f32 v199, v199\n"
      "v_exp_f32 v203, v203\n"
      "v_mfma_f32_16x16x32_bf16 v[56:59], v[216:219], v[152:155], v[56:59]\n"
      "v_add_f32 v238, v194, v238\n"
      "v_add_f32 v239, v198, v239\n"
      "v_add_f32 v240, v202, v240\n"
      "v_add_f32 v238, v195, v238\n"
      "v_add_f32 v239, v199, v239\n"
      "s_waitcnt lgkmcnt(4)\n"
      "v_mfma_f32_16x16x32_bf16 v[48:51], v[220:223], v[156:159], v[48:51]\n"
      "v_xor_b32 v248, 192, v247\n"
      "v_add_f32 v240, v203, v240\n"
      "v_cvt_pk_bf16_f32 v171, v194, v195\n"
      "v_cvt_pk_bf16_f32 v175, v198, v199\n"
      "v_cvt_pk_bf16_f32 v179, v202, v203\n"
      "v_fmamk_f32 v180, v180, 0x3e0293ee, v244\n"
      "v_mfma_f32_16x16x32_bf16 v[52:55], v[220:223], v[160:163], v[52:55]\n"
      "ds_read_b64_tr_b16 v[216:217], v248 offset:0\n"
      "v_fmamk_f32 v184, v184, 0x3e0293ee, v245\n"
      "v_fmamk_f32 v188, v188, 0x3e0293ee, v246\n"
      "v_fmamk_f32 v181, v181, 0x3e0293ee, v244\n"
      "v_fmamk_f32 v185, v185, 0x3e0293ee, v245\n"
      "v_fmamk_f32 v189, v189, 0x3e0293ee, v246\n"
      "v_mfma_f32_16x16x32_bf16 v[56:59], v[220:223], v[164:167], v[56:59]\n"
      "ds_read_b64_tr_b16 v[218:219], v248 offset:4096\n"
      "v_exp_f32 v180, v180\n"
      "v_exp_f32 v184, v184\n"
      "v_exp_f32 v188, v188\n"
      "s_waitcnt lgkmcnt(4)\n"
      "v_mfma_f32_16x16x32_bf16 v[60:63], v[224:227], v[144:147], v[60:63]\n"
      "ds_read_b64_tr_b16 v[220:221], v248 offset:8192\n"
      "v_exp_f32 v181, v181\n"
      "v_exp_f32 v185, v185\n"
      "v_exp_f32 v189, v189\n"
      "v_mfma_f32_16x16x32_bf16 v[64:67], v[224:227], v[148:151], v[64:67]\n"
      "ds_read_b64_tr_b16 v[222:223], v248 offset:12288\n"
      "v_add_f32 v238, v180, v238\n"
      "v_add_f32 v239, v184, v239\n"
      "v_add_f32 v240, v188, v240\n"
      "v_add_f32 v238, v181, v238\n"
      "v_mfma_f32_16x16x32_bf16 v[68:71], v[224:227], v[152:155], v[68:71]\n"
      "v_add_f32 v239, v185, v239\n"
      "v_add_f32 v240, v189, v240\n"
      "v_cvt_pk_bf16_f32 v180, v180, v181\n"
      "v_cvt_pk_bf16_f32 v184, v184, v185\n"
      "v_cvt_pk_bf16_f32 v188, v188, v189\n"
      "v_fmamk_f32 v182, v182, 0x3e0293ee, v244\n"
      "s_waitcnt lgkmcnt(4)\n"
      "v_mfma_f32_16x16x32_bf16 v[60:63], v[228:231], v[156:159], v[60:63]\n"
      "v_xor_b32 v249, 224, v247\n"
      "v_fmamk_f32 v186, v186, 0x3e0293ee, v245\n"
      "v_fmamk_f32 v190, v190, 0x3e0293ee, v246\n"
      "v_fmamk_f32 v183, v183, 0x3e0293ee, v244\n"
      "v_fmamk_f32 v187, v187, 0x3e0293ee, v245\n"
      "v_fmamk_f32 v191, v191, 0x3e0293ee, v246\n"
      "v_mfma_f32_16x16x32_bf16 v[64:67], v[228:231], v[160:163], v[64:67]\n"
      "ds_read_b64_tr_b16 v[224:225], v249 offset:0\n"
      "v_exp_f32 v182, v182\n"
      "v_exp_f32 v186, v186\n"
      "v_exp_f32 v190, v190\n"
      "v_mfma_f32_16x16x32_bf16 v[68:71], v[228:231], v[164:167], v[68:71]\n"
      "ds_read_b64_tr_b16 v[226:227], v249 offset:4096\n"
      "v_exp_f32 v183, v183\n"
      "v_exp_f32 v187, v187\n"
      "v_exp_f32 v191, v191\n"
      "s_waitcnt lgkmcnt(4)\n"
      "v_mfma_f32_16x16x32_bf16 v[72:75], v[216:219], v[144:147], v[72:75]\n"
      "ds_read_b64_tr_b16 v[228:229], v249 offset:8192\n"
      "ds_read_b64_tr_b16 v[230:231], v249 offset:12288\n"
      "v_readfirstlane_b32 %[t1], v253\n"
      "v_add_f32 v238, v182, v238\n"
      "v_add_f32 v239, v186, v239\n"
      "v_add_f32 v240, v190, v240\n"
      "v_add_f32 v238, v183, v238\n"
      "v_mfma_f32_16x16x32_bf16 v[76:79], v[216:219], v[148:151], v[76:79]\n"
      "s_add_u32 %[t0], %[si], 0\n"
      "v_mov_b32 v253, %[t0]\n"
      "v_mov_b32 v254, %[sc]\n"
      "v_add_f32 v239, v187, v239\n"
      "v_add_f32 v240, v191, v240\n"
      "v_cvt_pk_bf16_f32 v181, v182, v183\n"
      "v_cvt_pk_bf16_f32 v185, v186, v187\n"
      "v_cvt_pk_bf16_f32 v189, v190, v191\n"
      "v_fmamk_f32 v204, v204, 0x3e0293ee, v244\n"
      "v_mfma_f32_16x16x32_bf16 v[80:83], v[216:219], v[152:155], v[80:83]\n"
      "ds_write_b32 v254, v253 offset:16\n"
      "s_cmp_eq_u32 %[t1], 0x01010101\n"
      "s_cbranch_scc1 .Latt3_d\n"
      "s_mov_b32 %[t0], 0x100000\n"
      "v_mov_b32 v254, %[sf]\n"
      ".Latt3_l:\n"
      "s_sleep 1\n"
      "ds_read_b32 v253, v254 offset:24\n"
      "s_waitcnt lgkmcnt(0)\n"
      "v_readfirstlane_b32 %[t1], v253\n"
      "s_cmp_eq_u32 %[t1], 0x01010101\n"
      "s_cbranch_scc1 .Latt3_d\n"
      "s_sub_u32 %[t0], %[t0], 1\n"
      "s_cbranch_scc0 .Latt3_l\n"
      ".Latt3_d:\n"
      "v_fmamk_f32 v208, v208, 0x3e0293ee, v245\n"
      "v_fmamk_f32 v212, v212, 0x3e0293ee, v246\n"
      "v_fmamk_f32 v205, v205, 0x3e0293ee, v244\n"
      "v_fmamk_f32 v209, v209, 0x3e0293ee, v245\n"
      "v_fmamk_f32 v213, v213, 0x3e0293ee, v246\n"
      "s_waitcnt lgkmcnt(5)\n"
      "v_mfma_f32_16x16x32_bf16 v[72:75], v[220:223], v[156:159], v[72:75]\n"
      "v_add_u32 v247, %[sk], v251\n"
      "v_xor_b32 v248, 64, v247\n"
      "v_exp_f32 v204, v204\n"
      "v_exp_f32 v208, v208\n"
      "v_exp_f32 v212, v212\n"
      "v_mfma_f32_16x16x32_bf16 v[76:79], v[220:223], v[160:163], v[76:79]\n"
      "v_xor_b32 v249, 128, v247\n"
      "v_xor_b32 v250, 192, v247\n"
      "v_exp_f32 v205, v205\n"
      "v_exp_f32 v209, v209\n"
      "v_exp_f32 v213, v213\n"
      "v_mfma_f32_16x16x32_bf16 v[80:83], v[220:223], v[164:167], v[80:83]\n"
      "ds_read_b128 v[216:219], v247 offset:0\n"
      "v_add_f32 v238, v204, v238\n"
      "v_add_f32 v239, v208, v239\n"
      "v_add_f32 v240, v212, v240\n"
      "v_add_f32 v238, v205, v238\n"
      "s_waitcnt lgkmcnt(4)\n"
      "v_mfma_f32_16x16x32_bf16 v[84:87], v[224:227], v[144:147], v[84:87]\n"
      "ds_read_b128 v[220:223], v248 offset:0\n"
      "s_add_u32 %[sv], %[sv], 0x10000\n"
      "v_add_f32 v239, v209, v239\n"
      "v_add_f32 v240, v213, v240\n"
      "v_cvt_pk_bf16_f32 v182, v204, v205\n"
      "v_cvt_pk_bf16_f32 v186, v208, v209\n"
      "v_cvt_pk_bf16_f32 v190, v212, v213\n"
      "v_fmamk_f32 v206, v206, 0x3e0293ee, v244\n"
      "v_mfma_f32_16x16x32_bf16 v[88:91], v[224:227], v[148:151], v[88:91]\n"
      "s_cmp_ge_u32 %[sv], 0x24000\n"
      "s_cselect_b32 %[t0], 0x24000, 0\n"
      "v_fmamk_f32 v210, v210, 0x3e0293ee, v245\n"
      "v_fmamk_f32 v214, v214, 0x3e0293ee, v246\n"
      "v_fmamk_f32 v207, v207, 0x3e0293ee, v244\n"
      "v_fmamk_f32 v211, v211, 0x3e0293ee, v245\n"
      "v_fmamk_f32 v215, v215, 0x3e0293ee, v246\n"
      "v_mfma_f32_16x16x32_bf16 v[92:95], v[224:227], v[152:155], v[92:95]\n"
      "s_sub_u32 %[sv], %[sv], %[t0]\n"
      "v_exp_f32 v206, v206\n"
      "v_exp_f32 v210, v210\n"
      "v_exp_f32 v214, v214\n"
      "s_waitcnt lgkmcnt(3)\n"
      "v_mfma_f32_16x16x32_bf16 v[84:87], v[228:231], v[156:159], v[84:87]\n"
      "ds_read_b128 v[224:227], v249 offset:0\n"
      "v_exp_f32 v207, v207\n"
      "v_exp_f32 v211, v211\n"
      "v_exp_f32 v215, v215\n"
      "v_mfma_f32_16x16x32_bf16 v[88:91], v[228:231], v[160:163], v[88:91]\n"
      "v_add_f32 v238, v206, v238\n"
      "v_add_f32 v239, v210, v239\n"
      "v_add_f32 v240, v214, v240\n"
      "v_add_f32 v238, v207, v238\n"
      "v_mfma_f32_16x16x32_bf16 v[92:95], v[228:231], v[164:167], v[92:95]\n"
      "v_add_f32 v239, v211, v239\n"
      "v_add_f32 v240, v215, v240\n"
      "v_cvt_pk_bf16_f32 v183, v206, v207\n"
      "v_cvt_pk_bf16_f32 v187, v210, v211\n"
      "v_cvt_pk_bf16_f32 v191, v214, v215\n"
      ATS
      "s_waitcnt lgkmcnt(2)\n"
      "v_mfma_f32_16x16x32_bf16 v[144:147], v[216:219], v[96:99], 0\n"
      "ds_read_b128 v[228:231], v250 offset:0\n"
      "v_mul_f32 v0, v241, v0\n"
      "v_mul_f32 v4, v242, v4\n"
      "v_mul_f32 v8, v243, v8\n"
      "v_mfma_f32_16x16x32_bf16 v[148:151], v[216:219], v[100:103], 0\n"
      "v_mul_f32 v1, v241, v1\n"
      "v_mul_f32 v5, v242, v5\n"
      "v_mfma_f32_16x16x32_bf16 v[152:155], v[216:219], v[104:107], 0\n"
      "v_mul_f32 v9, v243, v9\n"
      "v_mul_f32 v2, v241, v2\n"
      "v_mul_f32 v6, v242, v6\n"
      "s_waitcnt lgkmcnt(2)\n"
      "v_mfma_f32_16x16x32_bf16 v[144:147], v[220:223], v[108:111], v[144:147]\n"
      "ds_read_b128 v[216:219], v247 offset:4096\n"
      "v_mul_f32 v10, v243, v10\n"
      "v_mul_f32 v3, v241, v3\n"
      "v_mfma_f32_16x16x32_bf16 v[148:151], v[220:223], v[112:115], v[148:151]\n"
      "v_mul_f32 v7, v242, v7\n"
      "v_mul_f32 v11, v243, v11\n"
      "v_mfma_f32_16x16x32_bf16 v[152:155], v[220:223], v[116:119], v[152:155]\n"
      "v_mul_f32 v12, v241, v12\n"
      "v_mul_f32 v16, v242, v16\n"
      "v_mul_f32 v20, v243, v20\n"
      "s_waitcnt lgkmcnt(2)\n"
      "v_mfma_f32_16x16x32_bf16 v[144:147], v[224:227], v[120:123], v[144:147]\n"
      "ds_read_b128 v[220:223], v248 offset:4096\n"
      "v_mul_f32 v13, v241, v13\n"
      "v_mul_f32 v17, v242, v17\n"
      "v_mfma_f32_16x16x32_bf16 v[148:151], v[224:227], v[124:127], v[148:151]\n"
      "v_mul_f32 v21, v243, v21\n"
      "v_mul_f32 v14, v241, v14\n"
      "v_mfma_f32_16x16x32_bf16 v[152:155], v[224:227], v[128:131], v[152:155]\n"
      "v_mul_f32 v18, v242, v18\n"
      "v_mul_f32 v22, v243, v22\n"
      "v_mul_f32 v15, v241, v15\n"
      "s_waitcnt lgkmcnt(2)\n"
      "v_mfma_f32_16x16x32_bf16 v[144:147], v[228:231], v[132:135], v[144:147]\n"
      "ds_read_b128 v[224:227], v249 offset:4096\n"
      "v_mul_f32 v19, v242, v19\n"
      "v_mul_f32 v23, v243, v23\n"
      "v_mfma_f32_16x16x32_bf16 v[148:151], v[228:231], v[136:139], v[148:151]\n"
      "v_mul_f32 v24, v241, v24\n"
      "v_mul_f32 v28, v242, v28\n"
      "v_mul_f32 v32, v243, v32\n"
      "v_mfma_f32_16x16x32_bf16 v[152:155], v[228:231], v[140:143], v[152:155]\n"
      "v_mul_f32 v25, v241, v25\n"
      "v_mul_f32 v29, v242, v29\n"
      "s_waitcnt lgkmcnt(2)\n"
      "v_mfma_f32_16x16x32_bf16 v[192:195], v[216:219], v[96:99], 0\n"
      "ds_read_b128 v[228:231], v250 offset:4096\n"
      "v_mul_f32 v33, v243, v33\n"
      "v_mul_f32 v26, v241, v26\n"
      "v_mfma_f32_16x16x32_bf16 v[196:199], v[216:219], v[100:103], 0\n"
      "v_mul_f32 v30, v242, v30\n"
      "v_mul_f32 v34, v243, v34\n"
      "v_mul_f32 v27, v241, v27\n"
      "v_mfma_f32_16x16x32_bf16 v[200:203], v[216:219], v[104:107], 0\n"
      "v_mul_f32 v31, v242, v31\n"
      "v_mul_f32 v35, v243, v35\n"
      "s_waitcnt lgkmcnt(2)\n"
      "v_mfma_f32_16x16x32_bf16 v[192:195], v[220:223], v[108:111], v[192:195]\n"
      "ds_read_b128 v[216:219], v247 offset:8192\n"
      "v_mul_f32 v36, v241, v36\n"
      "v_mul_f32 v40, v242, v40\n"
      "v_mfma_f32_16x16x32_bf16 v[196:199], v[220:223], v[112:115], v[196:199]\n"
      "v_mov_b32 v254, %[sf]\n"
      "v_mul_f32 v44, v243, v44\n"
      "v_mul_f32 v37, v241, v37\n"
      "v_mul_f32 v41, v242, v41\n"
      "v_mfma_f32_16x16x32_bf16 v[200:203], v[220:223], v[116:119], v[200:203]\n"
      "ds_read_b32 v253, v254 offset:16\n"
      "v_mul_f32 v45, v243, v45\n"
      "v_mul_f32 v38, v241, v38\n"
      "s_waitcnt lgkmcnt(3)\n"
      "v_mfma_f32_16x16x32_bf16 v[192:195], v[224:227], v[120:123], v[192:195]\n"
      "ds_read_b128 v[220:223], v248 offset:8192\n"
      "v_mul_f32 v42, v242, v42\n"
      "v_mul_f32 v46, v243, v46\n"
      "v_mul_f32 v39, v241, v39\n"
      "v_mfma_f32_16x16x32_bf16 v[196:199], v[224:227], v[124:127], v[196:199]\n"
      "v_mul_f32 v43, v242, v43\n"
      "v_mul_f32 v47, v243, v47\n"
      "v_mfma_f32_16x16x32_bf16 v[200:203], v[224:227], v[128:131], v[200:203]\n"
      "v_max3_f32 v232, v235, v144, v145\n"
      "v_max3_f32 v233, v236, v148, v149\n"
      "s_waitcnt lgkmcnt(3)\n"
      "v_mfma_f32_16x16x32_bf16 v[192:195], v[228:231], v[132:135], v[192:195]\n"
      "ds_read_b128 v[224:227], v249 offset:8192\n"
      "v_max3_f32 v234, v237, v152, v153\n"
      "v_max3_f32 v232, v232, v146, v147\n"
      "v_max3_f32 v233, v233, v150, v151\n"
      "v_mfma_f32_16x16x32_bf16 v[196:199], v[228:231], v[136:139], v[196:199]\n"
      "v_max3_f32 v234, v234, v154, v155\n"
      "v_mul_f32 v48, v241, v48\n"
      "v_mfma_f32_16x16x32_bf16 v[200:203], v[228:231], v[140:143], v[200:203]\n"
      "v_mul_f32 v52, v242, v52\n"
      "v_mul_f32 v56, v243, v56\n"
      "s_waitcnt lgkmcnt(3)\n"
      "v_mfma_f32_16x16x32_bf16 v[156:159], v[216:219], v[96:99], 0\n"
      "ds_read_b128 v[228:231], v250 offset:8192\n"
      "v_mul_f32 v49, v241, v49\n"
      "v_mul_f32 v53, v242, v53\n"
      "v_mul_f32 v57, v243, v57\n"
      "v_mfma_f32_16x16x32_bf16 v[160:163], v[216:219], v[100:103], 0\n"
      "v_mul_f32 v50, v241, v50\n"
      "v_mul_f32 v54, v242, v54\n"
      "v_mfma_f32_16x16x32_bf16 v[164:167], v[216:219], v[104:107], 0\n"
      "v_mul_f32 v58, v243, v58\n"
      "v_mul_f32 v51, v241, v51\n"
      "v_mul_f32 v55, v242, v55\n"
      "s_waitcnt lgkmcnt(2)\n"
      "v_mfma_f32_16x16x32_bf16 v[156:159], v[220:223], v[108:111], v[156:159]\n"
      "ds_read_b128 v[216:219], v247 offset:12288\n"
      "v_mul_f32 v59, v243, v59\n"
      "v_mul_f32 v60, v241, v60\n"
      "v_mfma_f32_16x16x32_bf16 v[160:163], v[220:223], v[112:115], v[160:163]\n"
      "v_mul_f32 v64, v242, v64\n"
      "v_mul_f32 v68, v243, v68\n"
      "v_mfma_f32_16x16x32_bf16 v[164:167], v[220:223], v[116:119], v[164:167]\n"
      "v_mul_f32 v61, v241, v61\n"
      "v_mul_f32 v65, v242, v65\n"
      "v_mul_f32 v69, v243, v69\n"
      "s_waitcnt lgkmcnt(2)\n"
      "v_mfma_f32_16x16x32_bf16 v[156:159], v[224:227], v[120:123], v[156:159]\n"
      "ds_read_b128 v[220:223], v248 offset:12288\n"
      "v_mul_f32 v62, v241, v62\n"
      "v_mul_f32 v66, v242, v66\n"
      "v_mfma_f32_16x16x32_bf16 v[160:163], v[224:227], v[124:127], v[160:163]\n"
      "v_mul_f32 v70, v243, v70\n"
      "v_mul_f32 v63, v241, v63\n"
      "v_mfma_f32_16x16x32_bf16 v[164:167], v[224:227], v[128:131], v[164:167]\n"
      "v_mul_f32 v67, v242, v67\n"
      "v_mul_f32 v71, v243, v71\n"
      "v_max3_f32 v232, v232, v192, v193\n"
      "s_waitcnt lgkmcnt(2)\n"
      "v_mfma_f32_16x16x32_bf16 v[156:159], v[228:231], v[132:135], v[156:159]\n"
      "ds_read_b128 v[224:227], v249 offset:12288\n"
      "v_max3_f32 v233, v233, v196, v197\n"
      "v_max3_f32 v234, v234, v200, v201\n"
      "v_mfma_f32_16x16x32_bf16 v[160:163], v[228:231], v[136:139], v[160:163]\n"
      "v_max3_f32 v232, v232, v194, v195\n"
      "v_max3_f32 v233, v233, v198, v199\n"
      "v_max3_f32 v234, v234, v202, v203\n"
      "v_mfma_f32_16x16x32_bf16 v[164:167], v[228:231], v[140:143], v[164:167]\n"
      "v_mul_f32 v72, v241, v72\n"
      "v_mul_f32 v76, v242, v76\n"
      "s_waitcnt lgkmcnt(2)\n"
      "v_mfma_f32_16x16x32_bf16 v[204:207], v[216:219], v[96:99], 0\n"
      "ds_read_b128 v[228:231], v250 offset:12288\n"
      "v_readfirstlane_b32 %[t1], v253\n"
      "s_add_u32 %[t0], %[si], 6\n"
      "v_mul_f32 v80, v243, v80\n"
      "v_mul_f32 v73, v241, v73\n"
      "v_mfma_f32_16x16x32_bf16 v[208:211], v[216:219], v[100:103], 0\n"
      "v_mov_b32 v253, %[t0]\n"
      "v_mov_b32 v254, %[sc]\n"
      "v_mul_f32 v77, v242, v77\n"
      "v_mul_f32 v81, v243, v81\n"
      "v_mul_f32 v74, v241, v74\n"
      "v_mfma_f32_16x16x32_bf16 v[212:215], v[216:219], v[104:107], 0\n"
      "ds_write_b32 v254, v253 offset:0\n"
      "s_cmp_eq_u32 %[t1], 0x01010101\n"
      "s_cbranch_scc1 .Latt5_d\n"
      "s_mov_b32 %[t0], 0x100000\n"
      "v_mov_b32 v254, %[sf]\n"
      ".Latt5_l:\n"
      "s_sleep 1\n"
      "ds_read_b32 v253, v254 offset:16\n"
      "s_waitcnt lgkmcnt(0)\n"
      "v_readfirstlane_b32 %[t1], v253\n"
      "s_cmp_eq_u32 %[t1], 0x01010101\n"
      "s_cbranch_scc1 .Latt5_d\n"
      "s_sub_u32 %[t0], %[t0], 1\n"
      "s_cbranch_scc0 .Latt5_l\n"
      ".Latt5_d:\n"
      "v_mul_f32 v78, v242, v78\n"
      "v_mul_f32 v82, v243, v82\n"
      "s_waitcnt lgkmcnt(3)\n"
      "v_mfma_f32_16x16x32_bf16 v[204:207], v[220:223], v[108:111], v[204:207]\n"
      "v_add_u32 v247, %[sv], v252\n"
      "v_mul_f32 v75, v241, v75\n"
      "v_mul_f32 v79, v242, v79\n"
      "v_mfma_f32_16x16x32_bf16 v[208:211], v[220:223], v[112:115], v[208:211]\n"
      "ds_read_b64_tr_b16 v[216:217], v247 offset:0\n"
      "v_mul_f32 v83, v243, v83\n"
      "v_mul_f32 v84, v241, v84\n"
      "v_mul_f32 v88, v242, v88\n"
      "v_mfma_f32_16x16x32_bf16 v[212:215], v[220:223], v[116:119], v[212:215]\n"
      "ds_read_b64_tr_b16 v[218:219], v247 offset:4096\n"
      "v_mul_f32 v92, v243, v92\n"
      "v_mul_f32 v85, v241, v85\n"
      "s_waitcnt lgkmcnt(4)\n"
      "v_mfma_f32_16x16x32_bf16 v[204:207], v[224:227], v[120:123], v[204:207]\n"
      "ds_read_b64_tr_b16 v[220:221], v247 offset:8192\n"
      "ds_read_b64_tr_b16 v[222:223], v247 offset:12288\n"
      "v_mul_f32 v89, v242, v89\n"
      "v_mul_f32 v93, v243, v93\n"
      "v_mul_f32 v86, v241, v86\n"
      "v_mfma_f32_16x16x32_bf16 v[208:211], v[224:227], v[124:127], v[208:211]\n"
      "s_add_u32 %[sk], %[sk], 0x10000\n"
      "s_cmp_ge_u32 %[sk], 0x24000\n"
      "v_mul_f32 v90, v242, v90\n"
      "v_mul_f32 v94, v243, v94\n"
      "v_mfma_f32_16x16x32_bf16 v[212:215], v[224:227], v[128:131], v[212:215]\n"
      "s_cselect_b32 %[t0], 0x24000, 0\n"
      "s_sub_u32 %[sk], %[sk], %[t0]\n"
      "v_mul_f32 v87, v241, v87\n"
      "v_mul_f32 v91, v242, v91\n"
      "s_waitcnt lgkmcnt(5)\n"
      "v_mfma_f32_16x16x32_bf16 v[204:207], v[228:231], v[132:135], v[204:207]\n"
      "v_xor_b32 v249, 32, v247\n"
      "v_mul_f32 v95, v243, v95\n"
      "v_max3_f32 v232, v232, v156, v157\n"
      "v_max3_f32 v233, v233, v160, v161\n"
      "v_mfma_f32_16x16x32_bf16 v[208:211], v[228:231], v[136:139], v[208:211]\n"
      "ds_read_b64_tr_b16 v[224:225], v249 offset:0\n"
      "v_max3_f32 v234, v234, v164, v165\n"
      "v_max3_f32 v232, v232, v158, v159\n"
      "v_mfma_f32_16x16x32_bf16 v[212:215], v[228:231], v[140:143], v[212:215]\n"
      "ds_read_b64_tr_b16 v[226:227], v249 offset:4096\n"
      "v_max3_f32 v233, v233, v162, v163\n"
      "v_max3_f32 v234, v234, v166, v167\n"
      ATS
      "s_waitcnt lgkmcnt(4)\n"
      "v_mfma_f32_16x16x32_bf16 v[0:3], v[216:219], v[168:171], v[0:3]\n"
      "ds_read_b64_tr_b16 v[228:229], v249 offset:8192\n"
      "v_mfma_f32_16x16x32_bf16 v[4:7], v[216:219], v[172:175], v[4:7]\n"
      "ds_read_b64_tr_b16 v[230:231], v249 offset:12288\n"
      "v_mfma_f32_16x16x32_bf16 v[8:11], v[216:219], v[176:179], v[8:11]\n"
      "v_max3_f32 v232, v232, v204, v205\n"
      "v_max3_f32 v233, v233, v208, v209\n"
      "v_max3_f32 v234, v234, v212, v213\n"
      "v_max3_f32 v232, v232, v206, v207\n"
      "v_max3_f32 v233, v233, v210, v211\n"
      "v_max3_f32 v234, v234, v214, v215\n"
      "v_mov_b32 v241, v232\n"
      "v_mov_b32 v242, v233\n"
      "v_mov_b32 v243, v234\n"
      "v_permlane32_swap_b32 v232, v241\n"
      "v_permlane32_swap_b32 v233, v242\n"
      "v_permlane32_swap_b32 v234, v243\n"
      "v_max_f32 v232, v232, v241\n"
      "v_max_f32 v233, v233, v242\n"
      "v_max_f32 v234, v234, v243\n"
      "v_mov_b32 v241, v232\n"
      "v_mov_b32 v242, v233\n"
      "s_waitcnt lgkmcnt(4)\n"
      "v_mfma_f32_16x16x32_bf16 v[0:3], v[220:223], v[180:183], v[0:3]\n"
      "v_xor_b32 v250, 64, v247\n"
      "v_mov_b32 v243, v234\n"
      "v_permlane16_swap_b32 v232, v241\n"
      "v_permlane16_swap_b32 v233, v242\n"
      "v_permlane16_swap_b32 v234, v243\n"
      "v_max_f32 v232, v232, v241\n"
      "v_mfma_f32_16x16x32_bf16 v[4:7], v[220:223], v[184:187], v[4:7]\n"
      "ds_read_b64_tr_b16 v[216:217], v250 offset:0\n"
      "v_max_f32 v233, v233, v242\n"
      "v_max_f32 v234, v234, v243\n"
      "v_sub_f32 v241, 0, v244\n"
      "v_sub_f32 v242, 0, v245\n"
      "v_sub_f32 v243, 0, v246\n"
      "v_mfma_f32_16x16x32_bf16 v[8:11], v[220:223], v[188:191], v[8:11]\n"
      "ds_read_b64_tr_b16 v[218:219], v250 offset:4096\n"
      "v_mul_f32 v244, 0xbe0293ee, v232\n"
      "v_mul_f32 v245, 0xbe0293ee, v233\n"
      "v_mul_f32 v246, 0xbe0293ee, v234\n"
      "v_add_f32 v241, v244, v241\n"
      "v_add_f32 v242, v245, v242\n"
      "v_add_f32 v243, v246, v243\n"
      "s_waitcnt lgkmcnt(4)\n"
      "v_mfma_f32_16x16x32_bf16 v[12:15], v[224:227], v[168:171], v[12:15]\n"
      "ds_read_b64_tr_b16 v[220:221], v250 offset:8192\n"
      "v_exp_f32 v241, v241\n"
      "v_exp_f32 v242, v242\n"
      "v_exp_f32 v243, v243\n"
      "v_mfma_f32_16x16x32_bf16 v[16:19], v[224:227], v[172:175], v[16:19]\n"
      "ds_read_b64_tr_b16 v[222:223], v250 offset:12288\n"
      "v_mul_f32 v238, v241, v238\n"
      "v_mul_f32 v239, v242, v239\n"
      "v_mul_f32 v240, v243, v240\n"
      "v_fmamk_f32 v144, v144, 0x3e0293ee, v244\n"
      "v_mfma_f32_16x16x32_bf16 v[20:23], v[224:227], v[176:179], v[20:23]\n"
      "v_fmamk_f32 v148, v148, 0x3e0293ee, v245\n"
      "v_fmamk_f32 v152, v152, 0x3e0293ee, v246\n"
      "v_fmamk_f32 v145, v145, 0x3e0293ee, v244\n"
      "v_fmamk_f32 v149, v149, 0x3e0293ee, v245\n"
      "v_fmamk_f32 v153, v153, 0x3e0293ee, v246\n"
      "v_exp_f32 v144, v144\n"
      "s_waitcnt lgkmcnt(4)\n"
      "v_mfma_f32_16x16x32_bf16 v[12:15], v[228:231], v[180:183], v[12:15]\n"
      "v_xor_b32 v248, 96, v247\n"
      "v_exp_f32 v148, v148\n"
      "v_exp_f32 v152, v152\n"
      "v_mfma_f32_16x16x32_bf16 v[16:19], v[228:231], v[184:187], v[16:19]\n"
      "ds_read_b64_tr_b16 v[224:225], v248 offset:0\n"
      "v_exp_f32 v145, v145\n"
      "v_exp_f32 v149, v149\n"
      "v_exp_f32 v153, v153\n"
      "v_mfma_f32_16x16x32_bf16 v[20:23], v[228:231], v[188:191], v[20:23]\n"
      "ds_read_b64_tr_b16 v[226:227], v248 offset:4096\n"
      "v_add_f32 v238, v144, v238\n"
      "v_add_f32 v239, v148, v239\n"
      "v_add_f32 v240, v152, v240\n"
      "v_add_f32 v238, v145, v238\n"
      "v_add_f32 v239, v149, v239\n"
      "s_waitcnt lgkmcnt(4)\n"
      "v_mfma_f32_16x16x32_bf16 v[24:27], v[216:219], v[168:171], v[24:27]\n"
      "ds_read_b64_tr_b16 v[228:229], v248 offset:8192\n"
      "v_add_f32 v240, v153, v240\n"
      "v_cvt_pk_bf16_f32 v144, v144, v145\n"
      "v_cvt_pk_bf16_f32 v148, v148, v149\n"
      "v_cvt_pk_bf16_f32 v152, v152, v153\n"
      "v_fmamk_f32 v146, v146, 0x3e0293ee, v244\n"
      "v_mfma_f32_16x16x32_bf16 v[28:31], v[216:219], v[172:175], v[28:31]\n"
      "ds_read_b64_tr_b16 v[230:231], v248 offset:12288\n"
      "v_fmamk_f32 v150, v150, 0x3e0293ee, v245\n"
      "v_fmamk_f32 v154, v154, 0x3e0293ee, v246\n"
      "v_fmamk_f32 v147, v147, 0x3e0293ee, v244\n"
      "v_fmamk_f32 v151, v151, 0x3e0293ee, v245\n"
      "v_fmamk_f32 v155, v155, 0x3e0293ee, v246\n"
      "v_exp_f32 v146, v146\n"
      "v_mfma_f32_16x16x32_bf16 v[32:35], v[216:219], v[176:179], v[32:35]\n"
      "v_exp_f32 v150, v150\n"
      "v_exp_f32 v154, v154\n"
      "s_waitcnt lgkmcnt(4)\n"
      "v_mfma_f32_16x16x32_bf16 v[24:27], v[220:223], v[180:183], v[24:27]\n"
      "v_xor_b32 v249, 128, v247\n"
      "ds_read_b64_tr_b16 v[216:217], v249 offset:0\n"
      "v_exp_f32 v147, v147\n"
      "v_exp_f32 v151, v151\n"
      "v_exp_f32 v155, v155\n"
      "v_mfma_f32_16x16x32_bf16 v[28:31], v[220:223], v[184:187], v[28:31]\n"
      "ds_read_b64_tr_b16 v[218:219], v249 offset:4096\n"
      "v_mov_b32 v254, %[sf]\n"
      "v_add_f32 v238, v146, v238\n"
      "v_add_f32 v239, v150, v239\n"
      "v_add_f32 v240, v154, v240\n"
      "v_add_f32 v238, v147, v238\n"
      "v_add_f32 v239, v151, v239\n"
      "v_mfma_f32_16x16x32_bf16 v[32:35], v[220:223], v[188:191], v[32:35]\n"
      "ds_read_b32 v253, v254 offset:40\n"
      "v_add_f32 v240, v155, v240\n"
      "v_cvt_pk_bf16_f32 v145, v146, v147\n"
      "v_cvt_pk_bf16_f32 v149, v150, v151\n"
      "v_cvt_pk_bf16_f32 v153, v154, v155\n"
      "v_fmamk_f32 v192, v192, 0x3e0293ee, v244\n"
      "s_waitcnt lgkmcnt(5)\n"
      "v_mfma_f32_16x16x32_bf16 v[36:39], v[224:227], v[168:171], v[36:39]\n"
      "ds_read_b64_tr_b16 v[220:221], v249 offset:8192\n"
      "v_fmamk_f32 v196, v196, 0x3e0293ee, v245\n"
      "v_fmamk_f32 v200, v200, 0x3e0293ee, v246\n"
      "v_fmamk_f32 v193, v193, 0x3e0293ee, v244\n"
      "v_fmamk_f32 v197, v197, 0x3e0293ee, v245\n"
      "v_fmamk_f32 v201, v201, 0x3e0293ee, v246\n"
      "v_exp_f32 v192, v192\n"
      "v_mfma_f32_16x16x32_bf16 v[40:43], v[224:227], v[172:175], v[40:43]\n"
      "ds_read_b64_tr_b16 v[222:223], v249 offset:12288\n"
      "v_exp_f32 v196, v196\n"
      "v_exp_f32 v200, v200\n"
      "v_mfma_f32_16x16x32_bf16 v[44:47], v[224:227], v[176:179], v[44:47]\n"
      "v_exp_f32 v193, v193\n"
      "v_exp_f32 v197, v197\n"
      "v_exp_f32 v201, v201\n"
      "s_waitcnt lgkmcnt(5)\n"
      "v_mfma_f32_16x16x32_bf16 v[36:39], v[228:231], v[180:183], v[36:39]\n"
      "v_xor_b32 v250, 160, v247\n"
      "v_add_f32 v238, v192, v238\n"
      "v_add_f32 v239, v196, v239\n"
      "v_add_f32 v240, v200, v240\n"
      "v_add_f32 v238, v193, v238\n"
      "v_add_f32 v239, v197, v239\n"
      "v_mfma_f32_16x16x32_bf16 v[40:43], v[228:231], v[184:187], v[40:43]\n"
      "ds_read_b64_tr_b16 v[224:225], v250 offset:0\n"
      "v_add_f32 v240, v201, v240\n"
      "v_cvt_pk_bf16_f32 v146, v192, v193\n"
      "v_cvt_pk_bf16_f32 v150, v196, v197\n"
      "v_cvt_pk_bf16_f32 v154, v200, v201\n"
      "v_fmamk_f32 v194, v194, 0x3e0293ee, v244\n"
      "v_mfma_f32_16x16x32_bf16 v[44:47], v[228:231], v[188:191], v[44:47]\n"
      "ds_read_b64_tr_b16 v[226:227], v250 offset:4096\n"
      "v_fmamk_f32 v198, v198, 0x3e0293ee, v245\n"
      "v_fmamk_f32 v202, v202, 0x3e0293ee, v246\n"
      "v_fmamk_f32 v195, v195, 0x3e0293ee, v244\n"
      "v_fmamk_f32 v199, v199, 0x3e0293ee, v245\n"
      "v_fmamk_f32 v203, v203, 0x3e0293ee, v246\n"
      "s_waitcnt lgkmcnt(5)\n"
      "v_mfma_f32_16x16x32_bf16 v[48:51], v[216:219], v[168:171], v[48:51]\n"
      "ds_read_b64_tr_b16 v[228:229], v250 offset:8192\n"
      "v_exp_f32 v194, v194\n"
      "v_exp_f32 v198, v198\n"
      "v_exp_f32 v202, v202\n"
      "v_mfma_f32_16x16x32_bf16 v[52:55], v[216:219], v[172:175], v[52:55]\n"
      "ds_read_b64_tr_b16 v[230:231], v250 offset:12288\n"
      "v_exp_f32 v195, v195\n"
      "v_exp_f32 v199, v199\n"
      "v_exp_f32 v203, v203\n"
      "v_mfma_f32_16x16x32_bf16 v[56:59], v[216:219], v[176:179], v[56:59]\n"
      "v_add_f32 v238, v194, v238\n"
      "v_add_f32 v239, v198, v239\n"
      "v_add_f32 v240, v202, v240\n"
      "v_add_f32 v238, v195, v238\n"
      "v_add_f32 v239, v199, v239\n"
      "s_waitcnt lgkmcnt(4)\n"
      "v_mfma_f32_16x16x32_bf16 v[48:51], v[220:223], v[180:183], v[48:51]\n"
      "v_xor_b32 v248, 192, v247\n"
      "v_add_f32 v240, v203, v240\n"
      "v_cvt_pk_bf16_f32 v147, v194, v195\n"
      "v_cvt_pk_bf16_f32 v151, v198, v199\n"
      "v_cvt_pk_bf16_f32 v155, v202, v203\n"
      "v_fmamk_f32 v156, v156, 0x3e0293ee, v244\n"
      "v_mfma_f32_16x16x32_bf16 v[52:55], v[220:223], v[184:187], v[52:55]\n"
      "ds_read_b64_tr_b16 v[216:217], v248 offset:0\n"
      "v_fmamk_f32 v160, v160, 0x3e0293ee, v245\n"
      "v_fmamk_f32 v164, v164, 0x3e0293ee, v246\n"
      "v_fmamk_f32 v157, v157, 0x3e0293ee, v244\n"
      "v_fmamk_f32 v161, v161, 0x3e0293ee, v245\n"
      "v_fmamk_f32 v165, v165, 0x3e0293ee, v246\n"
      "v_mfma_f32_16x16x32_bf16 v[56:59], v[220:223], v[188:191], v[56:59]\n"
      "ds_read_b64_tr_b16 v[218:219], v248 offset:4096\n"
      "v_exp_f32 v156, v156\n"
      "v_exp_f32 v160, v160\n"
      "v_exp_f32 v164, v164\n"
      "s_waitcnt lgkmcnt(4)\n"
      "v_mfma_f32_16x16x32_bf16 v[60:63], v[224:227], v[168:171], v[60:63]\n"
      "ds_read_b64_tr_b16 v[220:221], v248 offset:8192\n"
      "v_exp_f32 v157, v157\n"
      "v_exp_f32 v161, v161\n"
      "v_exp_f32 v165, v165\n"
      "v_mfma_f32_16x16x32_bf16 v[64:67], v[224:227], v[172:175], v[64:67]\n"
      "ds_read_b64_tr_b16 v[222:223], v248 offset:12288\n"
      "v_add_f32 v238, v156, v238\n"
      "v_add_f32 v239, v160, v239\n"
      "v_add_f32 v240, v164, v240\n"
      "v_add_f32 v238, v157, v238\n"
      "v_mfma_f32_16x16x32_bf16 v[68:71], v[224:227], v[176:179], v[68:71]\n"
      "v_add_f32 v239, v161, v239\n"
      "v_add_f32 v240, v165, v240\n"
      "v_cvt_pk_bf16_f32 v156, v156, v157\n"
      "v_cvt_pk_bf16_f32 v160, v160, v161\n"
      "v_cvt_pk_bf16_f32 v164, v164, v165\n"
      "v_fmamk_f32 v158, v158, 0x3e0293ee, v244\n"
      "s_waitcnt lgkmcnt(4)\n"
      "v_mfma_f32_16x16x32_bf16 v[60:63], v[228:231], v[180:183], v[60:63]\n"
      "v_xor_b32 v249, 224, v247\n"
      "v_fmamk_f32 v162, v162, 0x3e0293ee, v245\n"
      "v_fmamk_f32 v166, v166, 0x3e0293ee, v246\n"
      "v_fmamk_f32 v159, v159, 0x3e0293ee, v244\n"
      "v_fmamk_f32 v163, v163, 0x3e0293ee, v245\n"
      "v_fmamk_f32 v167, v167, 0x3e0293ee, v246\n"
      "v_mfma_f32_16x16x32_bf16 v[64:67], v[228:231], v[184:187], v[64:67]\n"
      "ds_read_b64_tr_b16 v[224:225], v249 offset:0\n"
      "v_exp_f32 v158, v158\n"
      "v_exp_f32 v162, v162\n"
      "v_exp_f32 v166, v166\n"
      "v_mfma_f32_16x16x32_bf16 v[68:71], v[228:231], v[188:191], v[68:71]\n"
      "ds_read_b64_tr_b16 v[226:227], v249 offset:4096\n"
      "v_exp_f32 v159, v159\n"
      "v_exp_f32 v163, v163\n"
      "v_exp_f32 v167, v167\n"
      "s_waitcnt lgkmcnt(4)\n"
      "v_mfma_f32_16x16x32_bf16 v[72:75], v[216:219], v[168:171], v[72:75]\n"
      "ds_read_b64_tr_b16 v[228:229], v249 offset:8192\n"
      "ds_read_b64_tr_b16 v[230:231], v249 offset:12288\n"
      "v_readfirstlane_b32 %[t1], v253\n"
      "v_add_f32 v238, v158, v238\n"
      "v_add_f32 v239, v162, v239\n"
      "v_add_f32 v240, v166, v240\n"
      "v_add_f32 v238, v159, v238\n"
      "v_mfma_f32_16x16x32_bf16 v[76:79], v[216:219], v[172:175], v[76:79]\n"
      "s_add_u32 %[t0], %[si], 4\n"
      "v_mov_b32 v253, %[t0]\n"
      "v_mov_b32 v254, %[sc]\n"
      "v_add_f32 v239, v163, v239\n"
      "v_add_f32 v240, v167, v240\n"
      "v_cvt_pk_bf16_f32 v157, v158, v159\n"
      "v_cvt_pk_bf16_f32 v161, v162, v163\n"
      "v_cvt_pk_bf16_f32 v165, v166, v167\n"
      "v_fmamk_f32 v204, v204, 0x3e0293ee, v244\n"
      "v_mfma_f32_16x16x32_bf16 v[80:83], v[216:219], v[176:179], v[80:83]\n"
      "ds_write_b32 v254, v253 offset:16\n"
      "s_cmp_eq_u32 %[t1], 0x01010101\n"
      "s_cbranch_scc1 .Latt6_d\n"
      "s_mov_b32 %[t0], 0x100000\n"
      "v_mov_b32 v254, %[sf]\n"
      ".Latt6_l:\n"
      "s_sleep 1\n"
      "ds_read_b32 v253, v254 offset:40\n"
      "s_waitcnt lgkmcnt(0)\n"
      "v_readfirstlane_b32 %[t1], v253\n"
      "s_cmp_eq_u32 %[t1], 0x01010101\n"
      "s_cbranch_scc1 .Latt6_d\n"
      "s_sub_u32 %[t0], %[t0], 1\n"
      "s_cbranch_scc0 .Latt6_l\n"
      ".Latt6_d:\n"
      "v_fmamk_f32 v208, v208, 0x3e0293ee, v245\n"
      "v_fmamk_f32 v212, v212, 0x3e0293ee, v246\n"
      "v_fmamk_f32 v205, v205, 0x3e0293ee, v244\n"
      "v_fmamk_f32 v209, v209, 0x3e0293ee, v245\n"
      "v_fmamk_f32 v213, v213, 0x3e0293ee, v246\n"
      "s_waitcnt lgkmcnt(5)\n"
      "v_mfma_f32_16x16x32_bf16 v[72:75], v[220:223], v[180:183], v[72:75]\n"
      "v_add_u32 v247, %[sk], v251\n"
      "v_xor_b32 v248, 64, v247\n"
      "v_exp_f32 v204, v204\n"
      "v_exp_f32 v208, v208\n"
      "v_exp_f32 v212, v212\n"
      "v_mfma_f32_16x16x32_bf16 v[76:79], v[220:223], v[184:187], v[76:79]\n"
      "v_xor_b32 v249, 128, v247\n"
      "v_xor_b32 v250, 192, v247\n"
      "v_exp_f32 v205, v205\n"
      "v_exp_f32 v209, v209\n"
      "v_exp_f32 v213, v213\n"
      "v_mfma_f32_16x16x32_bf16 v[80:83], v[220:223], v[188:191], v[80:83]\n"
      "ds_read_b128 v[216:219], v247 offset:0\n"
      "v_add_f32 v238, v204, v238\n"
      "v_add_f32 v239, v208, v239\n"
      "v_add_f32 v240, v212, v240\n"
      "v_add_f32 v238, v205, v238\n"
      "s_waitcnt lgkmcnt(4)\n"
      "v_mfma_f32_16x16x32_bf16 v[84:87], v[224:227], v[168:171], v[84:87]\n"
      "ds_read_b128 v[220:223], v248 offset:0\n"
      "s_add_u32 %[sv], %[sv], 0x10000\n"
      "v_add_f32 v239, v209, v239\n"
      "v_add_f32 v240, v213, v240\n"
      "v_cvt_pk_bf16_f32 v158, v204, v205\n"
      "v_cvt_pk_bf16_f32 v162, v208, v209\n"
      "v_cvt_pk_bf16_f32 v166, v212, v213\n"
      "v_fmamk_f32 v206, v206, 0x3e0293ee, v244\n"
      "v_mfma_f32_16x16x32_bf16 v[88:91], v[224:227], v[172:175], v[88:91]\n"
      "s_cmp_ge_u32 %[sv], 0x24000\n"
      "s_cselect_b32 %[t0], 0x24000, 0\n"
      "v_fmamk_f32 v210, v210, 0x3e0293ee, v245\n"
      "v_fmamk_f32 v214, v214, 0x3e0293ee, v246\n"
      "v_fmamk_f32 v207, v207, 0x3e0293ee, v244\n"
      "v_fmamk_f32 v211, v211, 0x3e0293ee, v245\n"
      "v_fmamk_f32 v215, v215, 0x3e0293ee, v246\n"
      "v_mfma_f32_16x16x32_bf16 v[92:95], v[224:227], v[176:179], v[92:95]\n"
      "s_sub_u32 %[sv], %[sv], %[t0]\n"
      "v_exp_f32 v206, v206\n"
      "v_exp_f32 v210, v210\n"
      "v_exp_f32 v214, v214\n"
      "s_waitcnt lgkmcnt(3)\n"
      "v_mfma_f32_16x16x32_bf16 v[84:87], v[228:231], v[180:183], v[84:87]\n"
      "ds_read_b128 v[224:227], v249 offset:0\n"
      "v_exp_f32 v207, v207\n"
      "v_exp_f32 v211, v211\n"
      "v_exp_f32 v215, v215\n"
      "v_mfma_f32_16x16x32_bf16 v[88:91], v[228:231], v[184:187], v[88:91]\n"
      "v_add_f32 v238, v206, v238\n"
      "v_add_f32 v239, v210, v239\n"
      "v_add_f32 v240, v214, v240\n"
      "v_add_f32 v238, v207, v238\n"
      "v_mfma_f32_16x16x32_bf16 v[92:95], v[228:231], v[188:191], v[92:95]\n"
      "v_add_f32 v239, v211, v239\n"
      "v_add_f32 v240, v215, v240\n"
      "v_cvt_pk_bf16_f32 v159, v206, v207\n"
      "v_cvt_pk_bf16_f32 v163, v210, v211\n"
      "v_cvt_pk_bf16_f32 v167, v214, v215\n"
      "s_add_u32 %[sf], %[sf], 32\n"
      "s_add_u32 %[si], %[si], 8\n"
      "s_sub_u32 %[cnt], %[cnt], 1\n"
      "s_branch .Latt_loop\n"
      ".Latt_exit:\n"
      "s_waitcnt lgkmcnt(4)\n"
      "v_mfma_f32_16x16x32_bf16 v[24:27], v[220:223], v[156:159], v[24:27]\n"
      "v_xor_b32 v249, 128, v247\n"
      "ds_read_b64_tr_b16 v[216:217], v249 offset:0\n"
      "v_exp_f32 v171, v171\n"
      "v_exp_f32 v175, v175\n"
      "v_exp_f32 v179, v179\n"
      "v_mfma_f32_16x16x32_bf16 v[28:31], v[220:223], v[160:163], v[28:31]\n"
      "ds_read_b64_tr_b16 v[218:219], v249 offset:4096\n"
      "v_mov_b32 v254, %[sf]\n"
      "v_add_f32 v238, v170, v238\n"
      "v_add_f32 v239, v174, v239\n"
      "v_add_f32 v240, v178, v240\n"
      "v_add_f32 v238, v171, v238\n"
      "v_add_f32 v239, v175, v239\n"
      "v_mfma_f32_16x16x32_bf16 v[32:35], v[220:223], v[164:167], v[32:35]\n"
      "ds_read_b32 v253, v254 offset:16\n"
      "v_add_f32 v240, v179, v240\n"
      "v_cvt_pk_bf16_f32 v169, v170, v171\n"
      "v_cvt_pk_bf16_f32 v173, v174, v175\n"
      "v_cvt_pk_bf16_f32 v177, v178, v179\n"
      "v_fmamk_f32 v192, v192, 0x3e0293ee, v244\n"
      "s_waitcnt lgkmcnt(5)\n"
      "v_mfma_f32_16x16x32_bf16 v[36:39], v[224:227], v[144:147], v[36:39]\n"
      "ds_read_b64_tr_b16 v[220:221], v249 offset:8192\n"
      "v_fmamk_f32 v196, v196, 0x3e0293ee, v245\n"
      "v_fmamk_f32 v200, v200, 0x3e0293ee, v246\n"
      "v_fmamk_f32 v193, v193, 0x3e0293ee, v244\n"
      "v_fmamk_f32 v197, v197, 0x3e0293ee, v245\n"
      "v_fmamk_f32 v201, v201, 0x3e0293ee, v246\n"
      "v_exp_f32 v192, v192\n"
      "v_mfma_f32_16x16x32_bf16 v[40:43], v[224:227], v[148:151], v[40:43]\n"
      "ds_read_b64_tr_b16 v[222:223], v249 offset:12288\n"
      "v_exp_f32 v196, v196\n"
      "v_exp_f32 v200, v200\n"
      "v_mfma_f32_16x16x32_bf16 v[44:47], v[224:227], v[152:155], v[44:47]\n"
      "v_exp_f32 v193, v193\n"
      "v_exp_f32 v197, v197\n"
      "v_exp_f32 v201, v201\n"
      "s_waitcnt lgkmcnt(5)\n"
      "v_mfma_f32_16x16x32_bf16 v[36:39], v[228:231], v[156:159], v[36:39]\n"
      "v_xor_b32 v250, 160, v247\n"
      "v_add_f32 v238, v192, v238\n"
      "v_add_f32 v239, v196, v239\n"
      "v_add_f32 v240, v200, v240\n"
      "v_add_f32 v238, v193, v238\n"
      "v_add_f32 v239, v197, v239\n"
      "v_mfma_f32_16x16x32_bf16 v[40:43], v[228:231], v[160:163], v[40:43]\n"
      "ds_read_b64_tr_b16 v[224:225], v250 offset:0\n"
      "v_add_f32 v240, v201, v240\n"
      "v_cvt_pk_bf16_f32 v170, v192, v193\n"
      "v_cvt_pk_bf16_f32 v174, v196, v197\n"
      "v_cvt_pk_bf16_f32 v178, v200, v201\n"
      "v_fmamk_f32 v194, v194, 0x3e0293ee, v244\n"
      "v_mfma_f32_16x16x32_bf16 v[44:47], v[228:231], v[164:167], v[44:47]\n"
      "ds_read_b64_tr_b16 v[226:227], v250 offset:4096\n"
      "v_fmamk_f32 v198, v198, 0x3e0293ee, v245\n"
      "v_fmamk_f32 v202, v202, 0x3e0293ee, v246\n"
      "v_fmamk_f32 v195, v195, 0x3e0293ee, v244\n"
      "v_fmamk_f32 v199, v199, 0x3e0293ee, v245\n"
      "v_fmamk_f32 v203, v203, 0x3e0293ee, v246\n"
      "s_waitcnt lgkmcnt(5)\n"
      "v_mfma_f32_16x16x32_bf16 v[48:51], v[216:219], v[144:147], v[48:51]\n"
      "ds_read_b64_tr_b16 v[228:229], v250 offset:8192\n"
      "v_exp_f32 v194, v194\n"
      "v_exp_f32 v198, v198\n"
      "v_exp_f32 v202, v202\n"
      "v_mfma_f32_16x16x32_bf16 v[52:55], v[216:219], v[148:151], v[52:55]\n"
      "ds_read_b64_tr_b16 v[230:231], v250 offset:12288\n"
      "v_exp_f32 v195, v195\n"
      "v_exp_f32 v199, v199\n"
      "v_exp_f32 v203, v203\n"
      "v_mfma_f32_16x16x32_bf16 v[56:59], v[216:219], v[152:155], v[56:59]\n"
      "v_add_f32 v238, v194, v238\n"
      "v_add_f32 v239, v198, v239\n"
      "v_add_f32 v240, v202, v240\n"
      "v_add_f32 v238, v195, v238\n"
      "v_add_f32 v239, v199, v239\n"
      "s_waitcnt lgkmcnt(4)\n"
      "v_mfma_f32_16x16x32_bf16 v[48:51], v[220:223], v[156:159], v[48:51]\n"
      "v_xor_b32 v248, 192, v247\n"
      "v_add_f32 v240, v203, v240\n"
      "v_cvt_pk_bf16_f32 v171, v194, v195\n"
      "v_cvt_pk_bf16_f32 v175, v198, v199\n"
      "v_cvt_pk_bf16_f32 v179, v202, v203\n"
      "v_fmamk_f32 v180, v180, 0x3e0293ee, v244\n"
      "v_mfma_f32_16x16x32_bf16 v[52:55], v[220:223], v[160:163], v[52:55]\n"
      "ds_read_b64_tr_b16 v[216:217], v248 offset:0\n"
      "v_fmamk_f32 v184, v184, 0x3e0293ee, v245\n"
      "v_fmamk_f32 v188, v188, 0x3e0293ee, v246\n"
      "v_fmamk_f32 v181, v181, 0x3e0293ee, v244\n"
      "v_fmamk_f32 v185, v185, 0x3e0293ee, v245\n"
      "v_fmamk_f32 v189, v189, 0x3e0293ee, v246\n"
      "v_mfma_f32_16x16x32_bf16 v[56:59], v[220:223], v[164:167], v[56:59]\n"
      "ds_read_b64_tr_b16 v[218:219], v248 offset:4096\n"
      "v_exp_f32 v180, v180\n"
      "v_exp_f32 v184, v184\n"
      "v_exp_f32 v188, v188\n"
      "s_waitcnt lgkmcnt(4)\n"
      "v_mfma_f32_16x16x32_bf16 v[60:63], v[224:227], v[144:147], v[60:63]\n"
      "ds_read_b64_tr_b16 v[220:221], v248 offset:8192\n"
      "v_exp_f32 v181, v181\n"
      "v_exp_f32 v185, v185\n"
      "v_exp_f32 v189, v189\n"
      "v_mfma_f32_16x16x32_bf16 v[64:67], v[224:227], v[148:151], v[64:67]\n"
      "ds_read_b64_tr_b16 v[222:223], v248 offset:12288\n"
      "v_add_f32 v238, v180, v238\n"
      "v_add_f32 v239, v184, v239\n"
      "v_add_f32 v240, v188, v240\n"
      "v_add_f32 v238, v181, v238\n"
      "v_mfma_f32_16x16x32_bf16 v[68:71], v[224:227], v[152:155], v[68:71]\n"
      "v_add_f32 v239, v185, v239\n"
      "v_add_f32 v240, v189, v240\n"
      "v_cvt_pk_bf16_f32 v180, v180, v181\n"
      "v_cvt_pk_bf16_f32 v184, v184, v185\n"
      "v_cvt_pk_bf16_f32 v188, v188, v189\n"
      "v_fmamk_f32 v182, v182, 0x3e0293ee, v244\n"
      "s_waitcnt lgkmcnt(4)\n"
      "v_mfma_f32_16x16x32_bf16 v[60:63], v[228:231], v[156:159], v[60:63]\n"
      "v_xor_b32 v249, 224, v247\n"
      "v_fmamk_f32 v186, v186, 0x3e0293ee, v245\n"
      "v_fmamk_f32 v190, v190, 0x3e0293ee, v246\n"
      "v_fmamk_f32 v183, v183, 0x3e0293ee, v244\n"
      "v_fmamk_f32 v187, v187, 0x3e0293ee, v245\n"
      "v_fmamk_f32 v191, v191, 0x3e0293ee, v246\n"
      "v_mfma_f32_16x16x32_bf16 v[64:67], v[228:231], v[160:163], v[64:67]\n"
      "ds_read_b64_tr_b16 v[224:225], v249 offset:0\n"
      "v_exp_f32 v182, v182\n"
      "v_exp_f32 v186, v186\n"
      "v_exp_f32 v190, v190\n"
      "v_mfma_f32_16x16x32_bf16 v[68:71], v[228:231], v[164:167], v[68:71]\n"
      "ds_read_b64_tr_b16 v[226:227], v249 offset:4096\n"
      "v_exp_f32 v183, v183\n"
      "v_exp_f32 v187, v187\n"
      "v_exp_f32 v191, v191\n"
      "s_waitcnt lgkmcnt(4)\n"
      "v_mfma_f32_16x16x32_bf16 v[72:75], v[216:219], v[144:147], v[72:75]\n"
      "ds_read_b64_tr_b16 v[228:229], v249 offset:8192\n"
      "ds_read_b64_tr_b16 v[230:231], v249 offset:12288\n"
      "v_readfirstlane_b32 %[t1], v253\n"
      "v_add_f32 v238, v182, v238\n"
      "v_add_f32 v239, v186, v239\n"
      "v_add_f32 v240, v190, v240\n"
      "v_add_f32 v238, v183, v238\n"
      "v_mfma_f32_16x16x32_bf16 v[76:79], v[216:219], v[148:151], v[76:79]\n"
      "s_add_u32 %[t0], %[si], 0\n"
      "v_mov_b32 v253, %[t0]\n"
      "v_mov_b32 v254, %[sc]\n"
      "v_add_f32 v239, v187, v239\n"
      "v_add_f32 v240, v191, v240\n"
      "v_cvt_pk_bf16_f32 v181, v182, v183\n"
      "v_cvt_pk_bf16_f32 v185, v186, v187\n"
      "v_cvt_pk_bf16_f32 v189, v190, v191\n"
      "v_fmamk_f32 v204, v204, 0x3e0293ee, v244\n"
      "v_mfma_f32_16x16x32_bf16 v[80:83], v[216:219], v[152:155], v[80:83]\n"
      "ds_write_b32 v254, v253 offset:16\n"
      "s_cmp_eq_u32 %[t1], 0x01010101\n"
      "s_cbranch_scc1 .Latt4_d\n"
      "s_mov_b32 %[t0], 0x100000\n"
      "v_mov_b32 v254, %[sf]\n"
      ".Latt4_l:\n"
      "s_sleep 1\n"
      "ds_read_b32 v253, v254 offset:16\n"
      "s_waitcnt lgkmcnt(0)\n"
      "v_readfirstlane_b32 %[t1], v253\n"
      "s_cmp_eq_u32 %[t1], 0x01010101\n"
      "s_cbranch_scc1 .Latt4_d\n"
      "s_sub_u32 %[t0], %[t0], 1\n"
      "s_cbranch_scc0 .Latt4_l\n"
      ".Latt4_d:\n"
      "v_fmamk_f32 v208, v208, 0x3e0293ee, v245\n"
      "v_fmamk_f32 v212, v212, 0x3e0293ee, v246\n"
      "v_fmamk_f32 v205, v205, 0x3e0293ee, v244\n"
      "v_fmamk_f32 v209, v209, 0x3e0293ee, v245\n"
      "v_fmamk_f32 v213, v213, 0x3e0293ee, v246\n"
      "s_waitcnt lgkmcnt(5)\n"
      "v_mfma_f32_16x16x32_bf16 v[72:75], v[220:223], v[156:159], v[72:75]\n"
      "s_add_u32 %[sv], %[sv], 0x10000\n"
      "s_cmp_ge_u32 %[sv], 0x24000\n"
      "s_cselect_b32 %[t0], 0x24000, 0\n"
      "v_exp_f32 v204, v204\n"
      "v_exp_f32 v208, v208\n"
      "v_exp_f32 v212, v212\n"
      "v_mfma_f32_16x16x32_bf16 v[76:79], v[220:223], v[160:163], v[76:79]\n"
      "s_sub_u32 %[sv], %[sv], %[t0]\n"
      "v_add_u32 v247, %[sv], v252\n"
      "v_exp_f32 v205, v205\n"
      "v_exp_f32 v209, v209\n"
      "v_exp_f32 v213, v213\n"
      "v_mfma_f32_16x16x32_bf16 v[80:83], v[220:223], v[164:167], v[80:83]\n"
      "ds_read_b64_tr_b16 v[216:217], v247 offset:0\n"
      "ds_read_b64_tr_b16 v[218:219], v247 offset:4096\n"
      "v_add_f32 v238, v204, v238\n"
      "v_add_f32 v239, v208, v239\n"
      "v_add_f32 v240, v212, v240\n"
      "v_add_f32 v238, v205, v238\n"
      "s_waitcnt lgkmcnt(5)\n"
      "v_mfma_f32_16x16x32_bf16 v[84:87], v[224:227], v[144:147], v[84:87]\n"
      "ds_read_b64_tr_b16 v[220:221], v247 offset:8192\n"
      "v_add_f32 v239, v209, v239\n"
      "v_add_f32 v240, v213, v240\n"
      "v_cvt_pk_bf16_f32 v182, v204, v205\n"
      "v_cvt_pk_bf16_f32 v186, v208, v209\n"
      "v_cvt_pk_bf16_f32 v190, v212, v213\n"
      "v_fmamk_f32 v206, v206, 0x3e0293ee, v244\n"
      "v_mfma_f32_16x16x32_bf16 v[88:91], v[224:227], v[148:151], v[88:91]\n"
      "ds_read_b64_tr_b16 v[222:223], v247 offset:12288\n"
      "v_fmamk_f32 v210, v210, 0x3e0293ee, v245\n"
      "v_fmamk_f32 v214, v214, 0x3e0293ee, v246\n"
      "v_fmamk_f32 v207, v207, 0x3e0293ee, v244\n"
      "v_fmamk_f32 v211, v211, 0x3e0293ee, v245\n"
      "v_fmamk_f32 v215, v215, 0x3e0293ee, v246\n"
      "v_mfma_f32_16x16x32_bf16 v[92:95], v[224:227], v[152:155], v[92:95]\n"
      "v_exp_f32 v206, v206\n"
      "v_exp_f32 v210, v210\n"
      "v_exp_f32 v214, v214\n"
      "s_waitcnt lgkmcnt(5)\n"
      "v_mfma_f32_16x16x32_bf16 v[84:87], v[228:231], v[156:159], v[84:87]\n"
      "v_xor_b32 v249, 32, v247\n"
      "v_exp_f32 v207, v207\n"
      "v_exp_f32 v211, v211\n"
      "v_exp_f32 v215, v215\n"
      "v_mfma_f32_16x16x32_bf16 v[88:91], v[228:231], v[160:163], v[88:91]\n"
      "ds_read_b64_tr_b16 v[224:225], v249 offset:0\n"
      "v_add_f32 v238, v206, v238\n"
      "v_add_f32 v239, v210, v239\n"
      "v_add_f32 v240, v214, v240\n"
      "v_add_f32 v238, v207, v238\n"
      "v_mfma_f32_16x16x32_bf16 v[92:95], v[228:231], v[164:167], v[92:95]\n"
      "ds_read_b64_tr_b16 v[226:227], v249 offset:4096\n"
      "v_add_f32 v239, v211, v239\n"
      "v_add_f32 v240, v215, v240\n"
      "v_cvt_pk_bf16_f32 v183, v206, v207\n"
      "v_cvt_pk_bf16_f32 v187, v210, v211\n"
      "v_cvt_pk_bf16_f32 v191, v214, v215\n"
      ATS
      "v_mul_f32 v0, v241, v0\n"
      "v_mul_f32 v4, v242, v4\n"
      "v_mul_f32 v8, v243, v8\n"
      "v_mul_f32 v1, v241, v1\n"
      "v_mul_f32 v5, v242, v5\n"
      "v_mul_f32 v9, v243, v9\n"
      "v_mul_f32 v2, v241, v2\n"
      "v_mul_f32 v6, v242, v6\n"
      "v_mul_f32 v10, v243, v10\n"
      "v_mul_f32 v3, v241, v3\n"
      "v_mul_f32 v7, v242, v7\n"
      "v_mul_f32 v11, v243, v11\n"
      "s_waitcnt lgkmcnt(4)\n"
      "v_mfma_f32_16x16x32_bf16 v[0:3], v[216:219], v[168:171], v[0:3]\n"
      "ds_read_b64_tr_b16 v[228:229], v249 offset:8192\n"
      "v_mul_f32 v12, v241, v12\n"
      "v_mul_f32 v16, v242, v16\n"
      "v_mfma_f32_16x16x32_bf16 v[4:7], v[216:219], v[172:175], v[4:7]\n"
      "ds_read_b64_tr_b16 v[230:231], v249 offset:12288\n"
      "v_mul_f32 v20, v243, v20\n"
      "v_mul_f32 v13, v241, v13\n"
      "v_mfma_f32_16x16x32_bf16 v[8:11], v[216:219], v[176:179], v[8:11]\n"
      "v_mul_f32 v17, v242, v17\n"
      "v_mul_f32 v21, v243, v21\n"
      "s_waitcnt lgkmcnt(4)\n"
      "v_mfma_f32_16x16x32_bf16 v[0:3], v[220:223], v[180:183], v[0:3]\n"
      "v_xor_b32 v250, 64, v247\n"
      "v_mul_f32 v14, v241, v14\n"
      "v_mul_f32 v18, v242, v18\n"
      "v_mfma_f32_16x16x32_bf16 v[4:7], v[220:223], v[184:187], v[4:7]\n"
      "ds_read_b64_tr_b16 v[216:217], v250 offset:0\n"
      "v_mul_f32 v22, v243, v22\n"
      "v_mul_f32 v15, v241, v15\n"
      "v_mfma_f32_16x16x32_bf16 v[8:11], v[220:223], v[188:191], v[8:11]\n"
      "ds_read_b64_tr_b16 v[218:219], v250 offset:4096\n"
      "v_mul_f32 v19, v242, v19\n"
      "v_mul_f32 v23, v243, v23\n"
      "s_waitcnt lgkmcnt(4)\n"
      "v_mfma_f32_16x16x32_bf16 v[12:15], v[224:227], v[168:171], v[12:15]\n"
      "ds_read_b64_tr_b16 v[220:221], v250 offset:8192\n"
      "v_mul_f32 v24, v241, v24\n"
      "v_mul_f32 v28, v242, v28\n"
      "v_mfma_f32_16x16x32_bf16 v[16:19], v[224:227], v[172:175], v[16:19]\n"
      "ds_read_b64_tr_b16 v[222:223], v250 offset:12288\n"
      "v_mul_f32 v32, v243, v32\n"
      "v_mul_f32 v25, v241, v25\n"
      "v_mfma_f32_16x16x32_bf16 v[20:23], v[224:227], v[176:179], v[20:23]\n"
      "v_mul_f32 v29, v242, v29\n"
      "v_mul_f32 v33, v243, v33\n"
      "s_waitcnt lgkmcnt(4)\n"
      "v_mfma_f32_16x16x32_bf16 v[12:15], v[228:231], v[180:183], v[12:15]\n"
      "v_xor_b32 v248, 96, v247\n"
      "v_mul_f32 v26, v241, v26\n"
      "v_mul_f32 v30, v242, v30\n"
      "v_mfma_f32_16x16x32_bf16 v[16:19], v[228:231], v[184:187], v[16:19]\n"
      "ds_read_b64_tr_b16 v[224:225], v248 offset:0\n"
      "v_mul_f32 v34, v243, v34\n"
      "v_mul_f32 v27, v241, v27\n"
      "v_mfma_f32_16x16x32_bf16 v[20:23], v[228:231], v[188:191], v[20:23]\n"
      "ds_read_b64_tr_b16 v[226:227], v248 offset:4096\n"
      "v_mul_f32 v31, v242, v31\n"
      "v_mul_f32 v35, v243, v35\n"
      "s_waitcnt lgkmcnt(4)\n"
      "v_mfma_f32_16x16x32_bf16 v[24:27], v[216:219], v[168:171], v[24:27]\n"
      "ds_read_b64_tr_b16 v[228:229], v248 offset:8192\n"
      "v_mul_f32 v36, v241, v36\n"
      "v_mul_f32 v40, v242, v40\n"
      "v_mfma_f32_16x16x32_bf16 v[28:31], v[216:219], v[172:175], v[28:31]\n"
      "ds_read_b64_tr_b16 v[230:231], v248 offset:12288\n"
      "v_mul_f32 v44, v243, v44\n"
      "v_mul_f32 v37, v241, v37\n"
      "v_mfma_f32_16x16x32_bf16 v[32:35], v[216:219], v[176:179], v[32:35]\n"
      "v_mul_f32 v41, v242, v41\n"
      "v_mul_f32 v45, v243, v45\n"
      "s_waitcnt lgkmcnt(4)\n"
      "v_mfma_f32_16x16x32_bf16 v[24:27], v[220:223], v[180:183], v[24:27]\n"
      "v_xor_b32 v249, 128, v247\n"
      "v_mul_f32 v38, v241, v38\n"
      "v_mul_f32 v42, v242, v42\n"
      "v_mfma_f32_16x16x32_bf16 v[28:31], v[220:223], v[184:187], v[28:31]\n"
      "ds_read_b64_tr_b16 v[216:217], v249 offset:0\n"
      "v_mul_f32 v46, v243, v46\n"
      "v_mul_f32 v39, v241, v39\n"
      "v_mfma_f32_16x16x32_bf16 v[32:35], v[220:223], v[188:191], v[32:35]\n"
      "ds_read_b64_tr_b16 v[218:219], v249 offset:4096\n"
      "v_mul_f32 v43, v242, v43\n"
      "v_mul_f32 v47, v243, v47\n"
      "s_waitcnt lgkmcnt(4)\n"
      "v_mfma_f32_16x16x32_bf16 v[36:39], v[224:227], v[168:171], v[36:39]\n"
      "ds_read_b64_tr_b16 v[220:221], v249 offset:8192\n"
      "v_mul_f32 v48, v241, v48\n"
      "v_mul_f32 v52, v242, v52\n"
      "v_mfma_f32_16x16x32_bf16 v[40:43], v[224:227], v[172:175], v[40:43]\n"
      "ds_read_b64_tr_b16 v[222:223], v249 offset:12288\n"
      "v_mul_f32 v56, v243, v56\n"
      "v_mul_f32 v49, v241, v49\n"
      "v_mfma_f32_16x16x32_bf16 v[44:47], v[224:227], v[176:179], v[44:47]\n"
      "v_mul_f32 v53, v242, v53\n"
      "v_mul_f32 v57, v243, v57\n"
      "s_waitcnt lgkmcnt(4)\n"
      "v_mfma_f32_16x16x32_bf16 v[36:39], v[228:231], v[180:183], v[36:39]\n"
      "v_xor_b32 v250, 160, v247\n"
      "v_mul_f32 v50, v241, v50\n"
      "v_mul_f32 v54, v242, v54\n"
      "v_mfma_f32_16x16x32_bf16 v[40:43], v[228:231], v[184:187], v[40:43]\n"
      "ds_read_b64_tr_b16 v[224:225], v250 offset:0\n"
      "v_mul_f32 v58, v243, v58\n"
      "v_mul_f32 v51, v241, v51\n"
      "v_mfma_f32_16x16x32_bf16 v[44:47], v[228:231], v[188:191], v[44:47]\n"
      "ds_read_b64_tr_b16 v[226:227], v250 offset:4096\n"
      "v_mul_f32 v55, v242, v55\n"
      "v_mul_f32 v59, v243, v59\n"
      "s_waitcnt lgkmcnt(4)\n"
      "v_mfma_f32_16x16x32_bf16 v[48:51], v[216:219], v[168:171], v[48:51]\n"
      "ds_read_b64_tr_b16 v[228:229], v250 offset:8192\n"
      "v_mul_f32 v60, v241, v60\n"
      "v_mul_f32 v64, v242, v64\n"
      "v_mfma_f32_16x16x32_bf16 v[52:55], v[216:219], v[172:175], v[52:55]\n"
      "ds_read_b64_tr_b16 v[230:231], v250 offset:12288\n"
      "v_mul_f32 v68, v243, v68\n"
      "v_mul_f32 v61, v241, v61\n"
      "v_mfma_f32_16x16x32_bf16 v[56:59], v[216:219], v[176:179], v[56:59]\n"
      "v_mul_f32 v65, v242, v65\n"
      "v_mul_f32 v69, v243, v69\n"
      "s_waitcnt lgkmcnt(4)\n"
      "v_mfma_f32_16x16x32_bf16 v[48:51], v[220:223], v[180:183], v[48:51]\n"
      "v_xor_b32 v248, 192, v247\n"
      "v_mul_f32 v62, v241, v62\n"
      "v_mul_f32 v66, v242, v66\n"
      "v_mfma_f32_16x16x32_bf16 v[52:55], v[220:223], v[184:187], v[52:55]\n"
      "ds_read_b64_tr_b16 v[216:217], v248 offset:0\n"
      "v_mul_f32 v70, v243, v70\n"
      "v_mul_f32 v63, v241, v63\n"
      "v_mfma_f32_16x16x32_bf16 v[56:59], v[220:223], v[188:191], v[56:59]\n"
      "ds_read_b64_tr_b16 v[218:219], v248 offset:4096\n"
      "v_mul_f32 v67, v242, v67\n"
      "v_mul_f32 v71, v243, v71\n"
      "s_waitcnt lgkmcnt(4)\n"
      "v_mfma_f32_16x16x32_bf16 v[60:63], v[224:227], v[168:171], v[60:63]\n"
      "ds_read_b64_tr_b16 v[220:221], v248 offset:8192\n"
      "v_mul_f32 v72, v241, v72\n"
      "v_mul_f32 v76, v242, v76\n"
      "v_mfma_f32_16x16x32_bf16 v[64:67], v[224:227], v[172:175], v[64:67]\n"
      "ds_read_b64_tr_b16 v[222:223], v248 offset:12288\n"
      "v_mul_f32 v80, v243, v80\n"
      "v_mul_f32 v73, v241, v73\n"
      "v_mfma_f32_16x16x32_bf16 v[68:71], v[224:227], v[176:179], v[68:71]\n"
      "v_mul_f32 v77, v242, v77\n"
      "v_mul_f32 v81, v243, v81\n"
      "s_waitcnt lgkmcnt(4)\n"
      "v_mfma_f32_16x16x32_bf16 v[60:63], v[228:231], v[180:183], v[60:63]\n"
      "v_xor_b32 v249, 224, v247\n"
      "v_mul_f32 v74, v241, v74\n"
      "v_mul_f32 v78, v242, v78\n"
      "v_mfma_f32_16x16x32_bf16 v[64:67], v[228:231], v[184:187], v[64:67]\n"
      "ds_read_b64_tr_b16 v[224:225], v249 offset:0\n"
      "v_mul_f32 v82, v243, v82\n"
      "v_mul_f32 v75, v241, v75\n"
      "v_mfma_f32_16x16x32_bf16 v[68:71], v[228:231], v[188:191], v[68:71]\n"
      "ds_read_b64_tr_b16 v[226:227], v249 offset:4096\n"
      "v_mul_f32 v79, v242, v79\n"
      "v_mul_f32 v83, v243, v83\n"
      "s_waitcnt lgkmcnt(4)\n"
      "v_mfma_f32_16x16x32_bf16 v[72:75], v[216:219], v[168:171], v[72:75]\n"
      "ds_read_b64_tr_b16 v[228:229], v249 offset:8192\n"
      "ds_read_b64_tr_b16 v[230:231], v249 offset:12288\n"
      "v_mul_f32 v84, v241, v84\n"
      "v_mul_f32 v88, v242, v88\n"
      "v_mfma_f32_16x16x32_bf16 v[76:79], v[216:219], v[172:175], v[76:79]\n"
      "s_add_u32 %[t0], %[si], 4\n"
      "v_mov_b32 v253, %[t0]\n"
      "v_mul_f32 v92, v243, v92\n"
      "v_mul_f32 v85, v241, v85\n"
      "v_mfma_f32_16x16x32_bf16 v[80:83], v[216:219], v[176:179], v[80:83]\n"
      "v_mov_b32 v254, %[sc]\n"
      "ds_write_b32 v254, v253 offset:16\n"
      "v_mul_f32 v89, v242, v89\n"
      "v_mul_f32 v93, v243, v93\n"
      "s_waitcnt lgkmcnt(5)\n"
      "v_mfma_f32_16x16x32_bf16 v[72:75], v[220:223], v[180:183], v[72:75]\n"
      "v_mul_f32 v86, v241, v86\n"
      "v_mul_f32 v90, v242, v90\n"
      "v_mfma_f32_16x16x32_bf16 v[76:79], v[220:223], v[184:187], v[76:79]\n"
      "v_mul_f32 v94, v243, v94\n"
      "v_mul_f32 v87, v241, v87\n"
      "v_mfma_f32_16x16x32_bf16 v[80:83], v[220:223], v[188:191], v[80:83]\n"
      "v_mul_f32 v91, v242, v91\n"
      "v_mul_f32 v95, v243, v95\n"
      "s_waitcnt lgkmcnt(3)\n"
      "v_mfma_f32_16x16x32_bf16 v[84:87], v[224:227], v[168:171], v[84:87]\n"
      "v_mfma_f32_16x16x32_bf16 v[88:91], v[224:227], v[172:175], v[88:91]\n"
      "v_mfma_f32_16x16x32_bf16 v[92:95], v[224:227], v[176:179], v[92:95]\n"
      "s_waitcnt lgkmcnt(1)\n"
      "v_mfma_f32_16x16x32_bf16 v[84:87], v[228:231], v[180:183], v[84:87]\n"
      "v_mfma_f32_16x16x32_bf16 v[88:91], v[228:231], v[184:187], v[88:91]\n"
      "v_mfma_f32_16x16x32_bf16 v[92:95], v[228:231], v[188:191], v[92:95]\n"
      "s_nop 7\n"
      "s_nop 1\n"
      : "={v[0:31]}"(a0), "={v[32:63]}"(a1), "={v[64:95]}"(a2), "={v[238:240]}"(lo), [sk] "+s"(sk), [sv] "+s"(sv), [sf] "+s"(sf), [si] "+s"(si),
        [cnt] "=&s"(cnt), [t0] "=&s"(t0), [t1] "=&s"(t1) ATS_O
      : "{v[96:127]}"(qa), "{v[128:143]}"(qc), "{v251}"(kb0), "{v252}"(vb0), [sc] "s"(sc) ATS_I
      : "memory", "scc", "v144", "v145", "v146", "v147", "v148", "v149", "v150", "v151", "v152", "v153", "v154", "v155", "v156", "v157", "v158", "v159", "v160", "v161", "v162", "v163", "v164", "v165", "v166", "v167", "v168", "v169", "v170", "v171", "v172", "v173", "v174", "v175", "v176", "v177", "v178", "v179", "v180", "v181", "v182", "v183", "v184", "v185", "v186", "v187", "v188", "v189", "v190", "v191", "v192", "v193", "v194", "v195", "v196", "v197", "v198", "v199", "v200", "v201", "v202", "v203", "v204", "v205", "v206", "v207", "v208", "v209", "v210", "v211", "v212", "v213", "v214", "v215", "v216", "v217", "v218", "v219", "v220", "v221", "v222", "v223", "v224", "v225", "v226", "v227", "v228", "v229", "v230", "v231", "v232", "v233", "v234", "v235", "v236", "v237", "v241", "v242", "v243", "v244", "v245", "v246", "v247", "v248", "v249", "v250", "v253", "v254" ATS_C);
#pragma unroll
    for (int i = 0; i < 24; ++i)
#pragma unroll
      for (int e = 0; e < 4; ++e) acc[i / 3][i % 3][e] = i < 8 ? a0[4 * i + e] : i < 16 ? a1[4 * (i - 8) + e] : a2[4 * (i - 16) + e];
    lrun[0] = lo[0]; lrun[1] = lo[1]; lrun[2] = lo[2];
  }
  TS();
  const int lq = fresh(lane_id()) & 15, lg = fresh(lane_id()) >> 4;
  const int vb0 = lbase + (4 * lg + (lq >> 2)) * 256 + ((4 * (lg & 1) + (lq >> 2)) << 5) + ((lq & 3) << 3);
  typedef __attribute__((ext_vector_type(32))) unsigned u32x32;
  typedef __attribute__((ext_vector_type(16))) unsigned u32x16;
  u32x32 oa; u32x16 oc;
  const int l16 = fresh(lane_id()) * 16;
  {
    const rsrc_t r_os = make_rsrc(ws + WS_O + (size_t)((pb * 4 + wid) * NH + h) * 12288, 12288);
#pragma unroll
    for (int qb = 0; qb < 3; ++qb) {
      const float inv = 1.f / xsum4(lrun[qb]);
#pragma unroll
      for (int ks = 0; ks < 4; ++ks) {
        const f32x4 a = acc[2 * ks][qb] * inv, c = acc[2 * ks + 1][qb] * inv;
        const u32x4 w = {cvtpk(a[0], a[1]), cvtpk(a[2], a[3]), cvtpk(c[0], c[1]), cvtpk(c[2], c[3])};
        st16(w, r_os, l16 + (ks * 3 + qb) * 1024, 0);
        const int i = ks * 3 + qb;
#pragma unroll
        for (int e = 0; e < 4; ++e) { if (i < 8) oa[4 * i + e] = w[e]; else oc[4 * (i - 8) + e] = w[e]; }
      }
    }
  }
  {
    const u32x4 r_ol = raw_rsrc(ws + WS_O + (size_t)((pb * 4 + wid) * NH + 4 * xg) * 12288, 4 * 12288);
    const u32x4 r_s = raw_rsrc(ws + WS_SLAB + (size_t)(pb * 4 + jn) * 8 * 49152 + xg * 49152 + wid * 12288, 12288);
    const u32x4 r_f = raw_rsrc(ws + WS_CNT, 131072);
    const int fo1 = (((pb * 8 + xg) * 4 + wid) * 16 + jn) * 8, fo2 = 65536 + (((pb * 4 + jn) * 4 + wid) * 16 + xg) * 8;
    const int vfo = (((pb * 8 + xg) * 4 + wid) * 16 + ((jn + fresh(lane_id())) & 3)) * 8, l8 = l16 >> 1;
    int sn, hv, h2, t0, t1;
    u32x4 r_3 = r_ol;
#define KTS(k)
#define KTE
    asm volatile(
      "v_mov_b32 v240, %[lc]\n"
      "ds_read_b64 v[252:253], v240 offset:224\n"
      "v_add_u32 v216, 65536, v232\n"
      "v_xor_b32 v217, 32, v216\n"
      "v_xor_b32 v218, 64, v216\n"
      "v_xor_b32 v219, 96, v216\n"
      "v_xor_b32 v220, 128, v216\n"
      "v_xor_b32 v221, 160, v216\n"
      "v_xor_b32 v222, 192, v216\n"
      "v_xor_b32 v223, 224, v216\n"
      "s_waitcnt lgkmcnt(0)\n"
      "v_and_b32 v252, v252, v253\n"
      "s_nop 0\n"
      "v_readfirstlane_b32 %[t1], v252\n"
      "s_cmp_eq_u32 %[t1], 0x01010101\n"
      "s_cbranch_scc1 .Lkt0_d\n"
      "s_mov_b32 %[t1], 0x100000\n"
      ".Lkt0_l:\n"
      "s_sleep 1\n"
      "ds_read_b64 v[252:253], v240 offset:224\n"
      "s_waitcnt lgkmcnt(0)\n"
      "v_and_b32 v252, v252, v253\n"
      "s_nop 0\n"
      "v_readfirstlane_b32 %[hv], v252\n"
      "s_cmp_eq_u32 %[hv], 0x01010101\n"
      "s_cbranch_scc1 .Lkt0_d\n"
      "s_sub_u32 %[t1], %[t1], 1\n"
      "s_cbranch_scc0 .Lkt0_l\n"
      ".Lkt0_d:\n"
      "ds_read_b64_tr_b16 v[192:193], v216 offset:0\n"
      "ds_read_b64_tr_b16 v[194:195], v216 offset:4096\n"
      "ds_read_b64_tr_b16 v[196:197], v217 offset:0\n"
      "ds_read_b64_tr_b16 v[198:199], v217 offset:4096\n"
      "ds_read_b64_tr_b16 v[200:201], v218 offset:0\n"
      "ds_read_b64_tr_b16 v[202:203], v218 offset:4096\n"
      "ds_read_b64_tr_b16 v[204:205], v219 offset:0\n"
      "ds_read_b64_tr_b16 v[206:207], v219 offset:4096\n"
      "ds_read_b64_tr_b16 v[208:209], v220 offset:0\n"
      "ds_read_b64_tr_b16 v[210:211], v220 offset:4096\n"
      "s_waitcnt lgkmcnt(8)\n"
      "v_mfma_f32_16x16x32_bf16 v[0:3], v[192:195], v[96:99], 0\n"
      "ds_read_b64_tr_b16 v[212:213], v221 offset:0\n"
      "v_mfma_f32_16x16x32_bf16 v[4:7], v[192:195], v[100:103], 0\n"
      "ds_read_b64_tr_b16 v[214:215], v221 offset:4096\n"
      "v_mfma_f32_16x16x32_bf16 v[8:11], v[192:195], v[104:107], 0\n"
      "s_waitcnt lgkmcnt(8)\n"
      "v_mfma_f32_16x16x32_bf16 v[12:15], v[196:199], v[96:99], 0\n"
      "ds_read_b64_tr_b16 v[192:193], v222 offset:0\n"
      "v_mfma_f32_16x16x32_bf16 v[16:19], v[196:199], v[100:103], 0\n"
      "ds_read_b64_tr_b16 v[194:195], v222 offset:4096\n"
      "v_mfma_f32_16x16x32_bf16 v[20:23], v[196:199], v[104:107], 0\n"
      "s_waitcnt lgkmcnt(8)\n"
      "v_mfma_f32_16x16x32_bf16 v[24:27], v[200:203], v[96:99], 0\n"
      "ds_read_b64_tr_b16 v[196:197], v223 offset:0\n"
      "v_mfma_f32_16x16x32_bf16 v[28:31], v[200:203], v[100:103], 0\n"
      "ds_read_b64_tr_b16 v[198:199], v223 offset:4096\n"
      "v_mfma_f32_16x16x32_bf16 v[32:35], v[200:203], v[104:107], 0\n"
      "s_waitcnt lgkmcnt(8)\n"
      "v_mfma_f32_16x16x32_bf16 v[36:39], v[204:207], v[96:99], 0\n"
      "ds_read_b64_tr_b16 v[200:201], v216 offset:8192\n"
      "v_mfma_f32_16x16x32_bf16 v[40:43], v[204:207], v[100:103], 0\n"
      "ds_read_b64_tr_b16 v[202:203], v216 offset:12288\n"
      "v_mfma_f32_16x16x32_bf16 v[44:47], v[204:207], v[104:107], 0\n"
      "s_waitcnt lgkmcnt(8)\n"
      "v_mfma_f32_16x16x32_bf16 v[48:51], v[208:211], v[96:99], 0\n"
      "ds_read_b64_tr_b16 v[204:205], v217 offset:8192\n"
      "v_mfma_f32_16x16x32_bf16 v[52:55], v[208:211], v[100:103], 0\n"
      "ds_read_b64_tr_b16 v[206:207], v217 offset:12288\n"
      "v_mfma_f32_16x16x32_bf16 v[56:59], v[208:211], v[104:107], 0\n"
      "s_waitcnt lgkmcnt(8)\n"
      "v_mfma_f32_16x16x32_bf16 v[60:63], v[212:215], v[96:99], 0\n"
      "ds_read_b64_tr_b16 v[208:209], v218 offset:8192\n"
      "v_mfma_f32_16x16x32_bf16 v[64:67], v[212:215], v[100:103], 0\n"
      "ds_read_b64_tr_b16 v[210:211], v218 offset:12288\n"
      "v_mfma_f32_16x16x32_bf16 v[68:71], v[212:215], v[104:107], 0\n"
      "s_waitcnt lgkmcnt(8)\n"
      "v_mfma_f32_16x16x32_bf16 v[72:75], v[192:195], v[96:99], 0\n"
      "ds_read_b64_tr_b16 v[212:213], v219 offset:8192\n"
      "v_mfma_f32_16x16x32_bf16 v[76:79], v[192:195], v[100:103], 0\n"
      "ds_read_b64_tr_b16 v[214:215], v219 offset:12288\n"
      "v_mfma_f32_16x16x32_bf16 v[80:83], v[192:195], v[104:107], 0\n"
      "s_waitcnt lgkmcnt(8)\n"
      "v_mfma_f32_16x16x32_bf16 v[84:87], v[196:199], v[96:99], 0\n"
      "ds_read_b64_tr_b16 v[192:193], v220 offset:8192\n"
      "v_mfma_f32_16x16x32_bf16 v[88:91], v[196:199], v[100:103], 0\n"
      "ds_read_b64_tr_b16 v[194:195], v220 offset:12288\n"
      "v_mfma_f32_16x16x32_bf16 v[92:95], v[196:199], v[104:107], 0\n"
      "s_waitcnt lgkmcnt(8)\n"
      "v_mfma_f32_16x16x32_bf16 v[0:3], v[200:203], v[108:111], v[0:3]\n"
      "ds_read_b64_tr_b16 v[196:197], v221 offset:8192\n"
      "v_mfma_f32_16x16x32_bf16 v[4:7], v[200:203], v[112:115], v[4:7]\n"
      "ds_read_b64_tr_b16 v[198:199], v221 offset:12288\n"
      "v_mfma_f32_16x16x32_bf16 v[8:11], v[200:203], v[116:119], v[8:11]\n"
      "v_add_u32 v224, 131072, v232\n"
      "v_xor_b32 v225, 32, v224\n"
      "v_xor_b32 v226, 64, v224\n"
      "v_xor_b32 v227, 96, v224\n"
      "s_waitcnt lgkmcnt(8)\n"
      "v_mfma_f32_16x16x32_bf16 v[12:15], v[204:207], v[108:111], v[12:15]\n"
      "ds_read_b64_tr_b16 v[200:201], v222 offset:8192\n"
      "v_mfma_f32_16x16x32_bf16 v[16:19], v[204:207], v[112:115], v[16:19]\n"
      "ds_read_b64_tr_b16 v[202:203], v222 offset:12288\n"
      "v_mfma_f32_16x16x32_bf16 v[20:23], v[204:207], v[116:119], v[20:23]\n"
      "v_xor_b32 v228, 128, v224\n"
      "v_xor_b32 v229, 160, v224\n"
      "v_xor_b32 v230, 192, v224\n"
      "v_xor_b32 v231, 224, v224\n"
      "s_waitcnt lgkmcnt(8)\n"
      "v_mfma_f32_16x16x32_bf16 v[24:27], v[208:211], v[108:111], v[24:27]\n"
      "ds_read_b64_tr_b16 v[204:205], v223 offset:8192\n"
      "v_mfma_f32_16x16x32_bf16 v[28:31], v[208:211], v[112:115], v[28:31]\n"
      "ds_read_b64_tr_b16 v[206:207], v223 offset:12288\n"
      "v_mfma_f32_16x16x32_bf16 v[32:35], v[208:211], v[116:119], v[32:35]\n"
      "s_waitcnt lgkmcnt(8)\n"
      "v_mfma_f32_16x16x32_bf16 v[36:39], v[212:215], v[108:111], v[36:39]\n"
      "ds_read_b64_tr_b16 v[208:209], v216 offset:16384\n"
      "v_mfma_f32_16x16x32_bf16 v[40:43], v[212:215], v[112:115], v[40:43]\n"
      "ds_read_b64_tr_b16 v[210:211], v216 offset:20480\n"
      "v_mfma_f32_16x16x32_bf16 v[44:47], v[212:215], v[116:119], v[44:47]\n"
      "s_waitcnt lgkmcnt(8)\n"
      "v_mfma_f32_16x16x32_bf16 v[48:51], v[192:195], v[108:111], v[48:51]\n"
      "ds_read_b64_tr_b16 v[212:213], v217 offset:16384\n"
      "v_mfma_f32_16x16x32_bf16 v[52:55], v[192:195], v[112:115], v[52:55]\n"
      "ds_read_b64_tr_b16 v[214:215], v217 offset:20480\n"
      "v_mfma_f32_16x16x32_bf16 v[56:59], v[192:195], v[116:119], v[56:59]\n"
      "s_waitcnt lgkmcnt(8)\n"
      "v_mfma_f32_16x16x32_bf16 v[60:63], v[196:199], v[108:111], v[60:63]\n"
      "ds_read_b64_tr_b16 v[192:193], v218 offset:16384\n"
      "v_mfma_f32_16x16x32_bf16 v[64:67], v[196:199], v[112:115], v[64:67]\n"
      "ds_read_b64_tr_b16 v[194:195], v218 offset:20480\n"
      "v_mfma_f32_16x16x32_bf16 v[68:71], v[196:199], v[116:119], v[68:71]\n"
      "s_waitcnt lgkmcnt(8)\n"
      "v_mfma_f32_16x16x32_bf16 v[72:75], v[200:203], v[108:111], v[72:75]\n"
      "ds_read_b64_tr_b16 v[196:197], v219 offset:16384\n"
      "v_mfma_f32_16x16x32_bf16 v[76:79], v[200:203], v[112:115], v[76:79]\n"
      "ds_read_b64_tr_b16 v[198:199], v219 offset:20480\n"
      "v_mfma_f32_16x16x32_bf16 v[80:83], v[200:203], v[116:119], v[80:83]\n"
      "buffer_load_dwordx2 v[250:251], v242, %[rf], 0 offen sc1\n"
      "s_waitcnt lgkmcnt(8)\n"
      "v_mfma_f32_16x16x32_bf16 v[84:87], v[204:207], v[108:111], v[84:87]\n"
      "ds_read_b64_tr_b16 v[200:201], v220 offset:16384\n"
      "v_mfma_f32_16x16x32_bf16 v[88:91], v[204:207], v[112:115], v[88:91]\n"
      "ds_read_b64_tr_b16 v[202:203], v220 offset:20480\n"
      "v_mfma_f32_16x16x32_bf16 v[92:95], v[204:207], v[116:119], v[92:95]\n"
      "s_waitcnt lgkmcnt(8)\n"
      "v_mfma_f32_16x16x32_bf16 v[0:3], v[208:211], v[120:123], v[0:3]\n"
      "ds_read_b64_tr_b16 v[204:205], v221 offset:16384\n"
      "v_mfma_f32_16x16x32_bf16 v[4:7], v[208:211], v[124:127], v[4:7]\n"
      "ds_read_b64_tr_b16 v[206:207], v221 offset:20480\n"
      "v_mfma_f32_16x16x32_bf16 v[8:11], v[208:211], v[128:131], v[8:11]\n"
      "s_waitcnt lgkmcnt(8)\n"
      "v_mfma_f32_16x16x32_bf16 v[12:15], v[212:215], v[120:123], v[12:15]\n"
      "ds_read_b64_tr_b16 v[208:209], v222 offset:16384\n"
      "v_mfma_f32_16x16x32_bf16 v[16:19], v[212:215], v[124:127], v[16:19]\n"
      "ds_read_b64_tr_b16 v[210:211], v222 offset:20480\n"
      "v_mfma_f32_16x16x32_bf16 v[20:23], v[212:215], v[128:131], v[20:23]\n"
      "s_waitcnt lgkmcnt(8)\n"
      "v_mfma_f32_16x16x32_bf16 v[24:27], v[192:195], v[120:123], v[24:27]\n"
      "ds_read_b64_tr_b16 v[212:213], v223 offset:16384\n"
      "v_mfma_f32_16x16x32_bf16 v[28:31], v[192:195], v[124:127], v[28:31]\n"
      "ds_read_b64_tr_b16 v[214:215], v223 offset:20480\n"
      "v_mfma_f32_16x16x32_bf16 v[32:35], v[192:195], v[128:131], v[32:35]\n"
      "s_waitcnt lgkmcnt(8)\n"
      "v_mfma_f32_16x16x32_bf16 v[36:39], v[196:199], v[120:123], v[36:39]\n"
      "ds_read_b64_tr_b16 v[192:193], v216 offset:24576\n"
      "v_mfma_f32_16x16x32_bf16 v[40:43], v[196:199], v[124:127], v[40:43]\n"
      "ds_read_b64_tr_b16 v[194:195], v216 offset:28672\n"
      "v_mfma_f32_16x16x32_bf16 v[44:47], v[196:199], v[128:131], v[44:47]\n"
      "s_waitcnt lgkmcnt(8)\n"
      "v_mfma_f32_16x16x32_bf16 v[48:51], v[200:203], v[120:123], v[48:51]\n"
      "ds_read_b64_tr_b16 v[196:197], v217 offset:24576\n"
      "v_mfma_f32_16x16x32_bf16 v[52:55], v[200:203], v[124:127], v[52:55]\n"
      "ds_read_b64_tr_b16 v[198:199], v217 offset:28672\n"
      "v_mfma_f32_16x16x32_bf16 v[56:59], v[200:203], v[128:131], v[56:59]\n"
      "ds_read_b64 v[252:253], v240 offset:232\n"
      "s_waitcnt lgkmcnt(9)\n"
      "v_mfma_f32_16x16x32_bf16 v[60:63], v[204:207], v[120:123], v[60:63]\n"
      "ds_read_b64_tr_b16 v[200:201], v218 offset:24576\n"
      "v_mfma_f32_16x16x32_bf16 v[64:67], v[204:207], v[124:127], v[64:67]\n"
      "ds_read_b64_tr_b16 v[202:203], v218 offset:28672\n"
      "v_mfma_f32_16x16x32_bf16 v[68:71], v[204:207], v[128:131], v[68:71]\n"
      "s_waitcnt lgkmcnt(9)\n"
      "v_mfma_f32_16x16x32_bf16 v[72:75], v[208:211], v[120:123], v[72:75]\n"
      "ds_read_b64_tr_b16 v[204:205], v219 offset:24576\n"
      "v_mfma_f32_16x16x32_bf16 v[76:79], v[208:211], v[124:127], v[76:79]\n"
      "ds_read_b64_tr_b16 v[206:207], v219 offset:28672\n"
      "v_mfma_f32_16x16x32_bf16 v[80:83], v[208:211], v[128:131], v[80:83]\n"
      "s_waitcnt lgkmcnt(9)\n"
      "v_mfma_f32_16x16x32_bf16 v[84:87], v[212:215], v[120:123], v[84:87]\n"
      "ds_read_b64_tr_b16 v[208:209], v220 offset:24576\n"
      "v_mfma_f32_16x16x32_bf16 v[88:91], v[212:215], v[124:127], v[88:91]\n"
      "ds_read_b64_tr_b16 v[210:211], v220 offset:28672\n"
      "v_mfma_f32_16x16x32_bf16 v[92:95], v[212:215], v[128:131], v[92:95]\n"
      "s_waitcnt lgkmcnt(9)\n"
      "v_mfma_f32_16x16x32_bf16 v[0:3], v[192:195], v[132:135], v[0:3]\n"
      "ds_read_b64_tr_b16 v[212:213], v221 offset:24576\n"
      "v_mfma_f32_16x16x32_bf16 v[4:7], v[192:195], v[136:139], v[4:7]\n"
      "ds_read_b64_tr_b16 v[214:215], v221 offset:28672\n"
      "v_mfma_f32_16x16x32_bf16 v[8:11], v[192:195], v[140:143], v[8:11]\n"
      "s_waitcnt lgkmcnt(9)\n"
      "v_mfma_f32_16x16x32_bf16 v[12:15], v[196:199], v[132:135], v[12:15]\n"
      "ds_read_b64_tr_b16 v[192:193], v222 offset:24576\n"
      "v_mfma_f32_16x16x32_bf16 v[16:19], v[196:199], v[136:139], v[16:19]\n"
      "ds_read_b64_tr_b16 v[194:195], v222 offset:28672\n"
      "v_mfma_f32_16x16x32_bf16 v[20:23], v[196:199], v[140:143], v[20:23]\n"
      "s_waitcnt lgkmcnt(10)\n"
      "v_and_b32 v252, v252, v253\n"
      "s_nop 0\n"
      "v_readfirstlane_b32 %[t1], v252\n"
      "s_cmp_eq_u32 %[t1], 0x01010101\n"
      "s_cbranch_scc1 .Lkt1_d\n"
      "s_mov_b32 %[t1], 0x100000\n"
      ".Lkt1_l:\n"
      "s_sleep 1\n"
      "ds_read_b64 v[252:253], v240 offset:232\n"
      "s_waitcnt lgkmcnt(0)\n"
      "v_and_b32 v252, v252, v253\n"
      "s_nop 0\n"
      "v_readfirstlane_b32 %[hv], v252\n"
      "s_cmp_eq_u32 %[hv], 0x01010101\n"
      "s_cbranch_scc1 .Lkt1_d\n"
      "s_sub_u32 %[t1], %[t1], 1\n"
      "s_cbranch_scc0 .Lkt1_l\n"
      ".Lkt1_d:\n"
      "s_waitcnt lgkmcnt(8)\n"
      "v_mfma_f32_16x16x32_bf16 v[24:27], v[200:203], v[132:135], v[24:27]\n"
      "ds_read_b64_tr_b16 v[196:197], v223 offset:24576\n"
      "v_mfma_f32_16x16x32_bf16 v[28:31], v[200:203], v[136:139], v[28:31]\n"
      "ds_read_b64_tr_b16 v[198:199], v223 offset:28672\n"
      "v_mfma_f32_16x16x32_bf16 v[32:35], v[200:203], v[140:143], v[32:35]\n"
      "s_waitcnt lgkmcnt(8)\n"
      "v_mfma_f32_16x16x32_bf16 v[36:39], v[204:207], v[132:135], v[36:39]\n"
      "ds_read_b64_tr_b16 v[200:201], v216 offset:32768\n"
      "v_mfma_f32_16x16x32_bf16 v[40:43], v[204:207], v[136:139], v[40:43]\n"
      "ds_read_b64_tr_b16 v[202:203], v216 offset:36864\n"
      "v_mfma_f32_16x16x32_bf16 v[44:47], v[204:207], v[140:143], v[44:47]\n"
      "s_waitcnt lgkmcnt(8)\n"
      "v_mfma_f32_16x16x32_bf16 v[48:51], v[208:211], v[132:135], v[48:51]\n"
      "ds_read_b64_tr_b16 v[204:205], v217 offset:32768\n"
      "v_mfma_f32_16x16x32_bf16 v[52:55], v[208:211], v[136:139], v[52:55]\n"
      "ds_read_b64_tr_b16 v[206:207], v217 offset:36864\n"
      "v_mfma_f32_16x16x32_bf16 v[56:59], v[208:211], v[140:143], v[56:59]\n"
      "s_waitcnt lgkmcnt(8)\n"
      "v_mfma_f32_16x16x32_bf16 v[60:63], v[212:215], v[132:135], v[60:63]\n"
      "ds_read_b64_tr_b16 v[208:209], v218 offset:32768\n"
      "v_mfma_f32_16x16x32_bf16 v[64:67], v[212:215], v[136:139], v[64:67]\n"
      "ds_read_b64_tr_b16 v[210:211], v218 offset:36864\n"
      "v_mfma_f32_16x16x32_bf16 v[68:71], v[212:215], v[140:143], v[68:71]\n"
      "s_waitcnt lgkmcnt(8)\n"
      "v_mfma_f32_16x16x32_bf16 v[72:75], v[192:195], v[132:135], v[72:75]\n"
      "ds_read_b64_tr_b16 v[212:213], v219 offset:32768\n"
      "v_mfma_f32_16x16x32_bf16 v[76:79], v[192:195], v[136:139], v[76:79]\n"
      "ds_read_b64_tr_b16 v[214:215], v219 offset:36864\n"
      "v_mfma_f32_16x16x32_bf16 v[80:83], v[192:195], v[140:143], v[80:83]\n"
      "s_waitcnt lgkmcnt(8)\n"
      "v_mfma_f32_16x16x32_bf16 v[84:87], v[196:199], v[132:135], v[84:87]\n"
      "ds_read_b64_tr_b16 v[192:193], v220 offset:32768\n"
      "v_mfma_f32_16x16x32_bf16 v[88:91], v[196:199], v[136:139], v[88:91]\n"
      "ds_read_b64_tr_b16 v[194:195], v220 offset:36864\n"
      "v_mfma_f32_16x16x32_bf16 v[92:95], v[196:199], v[140:143], v[92:95]\n"
      "s_waitcnt vmcnt(0)\n"
      "v_cmp_eq_u64 vcc, %[tg], v[250:251]\n"
      "s_mov_b32 %[sn], vcc_lo\n"
      "v_mov_b32 v250, %[tl]\n"
      "v_mov_b32 v251, %[th]\n"
      "s_mov_b64 exec, 1\n"
      "buffer_store_dwordx2 v[250:251], off, %[rf], %[fo1] sc1\n"
      "s_mov_b64 exec, -1\n"
      KTS(0)
      "s_bitcmp1_b32 %[sn], 1\n"
      "s_cbranch_scc1 .Lkb1_d\n"
      "s_mov_b32 %[t1], 0x100000\n"
      ".Lkb1_l:\n"
      "s_sleep 1\n"
      "buffer_load_dwordx2 v[250:251], v242, %[rf], 0 offen sc1\n"
      "s_waitcnt vmcnt(0)\n"
      "v_cmp_eq_u64 vcc, %[tg], v[250:251]\n"
      "s_mov_b32 %[sn], vcc_lo\n"
      "s_bitcmp1_b32 %[sn], 1\n"
      "s_cbranch_scc1 .Lkb1_d\n"
      "s_sub_u32 %[t1], %[t1], 1\n"
      "s_cbranch_scc0 .Lkb1_l\n"
      ".Lkb1_d:\n"
      "s_add_u32 %[t0], %[jn], 1\n"
      "s_and_b32 %[t0], %[t0], 3\n"
      "s_mul_i32 %[t0], %[t0], 12288\n"
      "buffer_load_dwordx4 v[144:147], v233, %[ro], %[t0] offen offset:0 sc1\n"
      "buffer_load_dwordx4 v[148:151], v233, %[ro], %[t0] offen offset:1024 sc1\n"
      "buffer_load_dwordx4 v[152:155], v233, %[ro], %[t0] offen offset:2048 sc1\n"
      "buffer_load_dwordx4 v[156:159], v233, %[ro], %[t0] offen offset:3072 sc1\n"
      "s_add_u32 %[t0], %[t0], 4096\n"
      "buffer_load_dwordx4 v[160:163], v233, %[ro], %[t0] offen offset:0 sc1\n"
      "buffer_load_dwordx4 v[164:167], v233, %[ro], %[t0] offen offset:1024 sc1\n"
      "buffer_load_dwordx4 v[168:171], v233, %[ro], %[t0] offen offset:2048 sc1\n"
      "buffer_load_dwordx4 v[172:175], v233, %[ro], %[t0] offen offset:3072 sc1\n"
      "s_add_u32 %[t0], %[t0], 4096\n"
      "buffer_load_dwordx4 v[176:179], v233, %[ro], %[t0] offen offset:0 sc1\n"
      "buffer_load_dwordx4 v[180:183], v233, %[ro], %[t0] offen offset:1024 sc1\n"
      "buffer_load_dwordx4 v[184:187], v233, %[ro], %[t0] offen offset:2048 sc1\n"
      "buffer_load_dwordx4 v[188:191], v233, %[ro], %[t0] offen offset:3072 sc1\n"
      "s_bitcmp1_b32 %[sn], 2\n"
      "s_cselect_b32 %[h2], 1, 0\n"
      "s_cbranch_scc0 .Lko2\n"
      "s_add_u32 %[t0], %[jn], 2\n"
      "s_and_b32 %[t0], %[t0], 3\n"
      "s_mul_i32 %[t0], %[t0], 12288\n"
      "buffer_load_dwordx4 v[96:99], v233, %[ro], %[t0] offen offset:0 sc1\n"
      "buffer_load_dwordx4 v[100:103], v233, %[ro], %[t0] offen offset:1024 sc1\n"
      "buffer_load_dwordx4 v[104:107], v233, %[ro], %[t0] offen offset:2048 sc1\n"
      "buffer_load_dwordx4 v[108:111], v233, %[ro], %[t0] offen offset:3072 sc1\n"
      "s_add_u32 %[t0], %[t0], 4096\n"
      "buffer_load_dwordx4 v[112:115], v233, %[ro], %[t0] offen offset:0 sc1\n"
      "buffer_load_dwordx4 v[116:119], v233, %[ro], %[t0] offen offset:1024 sc1\n"
      "buffer_load_dwordx4 v[120:123], v233, %[ro], %[t0] offen offset:2048 sc1\n"
      "buffer_load_dwordx4 v[124:127], v233, %[ro], %[t0] offen offset:3072 sc1\n"
      "s_add_u32 %[t0], %[t0], 4096\n"
      "buffer_load_dwordx4 v[128:131], v233, %[ro], %[t0] offen offset:0 sc1\n"
      "buffer_load_dwordx4 v[132:135], v233, %[ro], %[t0] offen offset:1024 sc1\n"
      "buffer_load_dwordx4 v[136:139], v233, %[ro], %[t0] offen offset:2048 sc1\n"
      "buffer_load_dwordx4 v[140:143], v233, %[ro], %[t0] offen offset:3072 sc1\n"
      KTS(1)
      "s_waitcnt vmcnt(12)\n"
      "s_branch .Lko3\n"
      ".Lko2:\n"
      KTS(1)
      "s_waitcnt vmcnt(0)\n"
      ".Lko3:\n"
      "s_waitcnt lgkmcnt(8)\n"
      "v_mfma_f32_16x16x32_bf16 v[0:3], v[200:203], v[144:147], v[0:3]\n"
      "ds_read_b64_tr_b16 v[196:197], v221 offset:32768\n"
      "v_mfma_f32_16x16x32_bf16 v[4:7], v[200:203], v[148:151], v[4:7]\n"
      "ds_read_b64_tr_b16 v[198:199], v221 offset:36864\n"
      "v_mfma_f32_16x16x32_bf16 v[8:11], v[200:203], v[152:155], v[8:11]\n"
      "s_waitcnt lgkmcnt(8)\n"
      "v_mfma_f32_16x16x32_bf16 v[12:15], v[204:207], v[144:147], v[12:15]\n"
      "ds_read_b64_tr_b16 v[200:201], v222 offset:32768\n"
      "v_mfma_f32_16x16x32_bf16 v[16:19], v[204:207], v[148:151], v[16:19]\n"
      "ds_read_b64_tr_b16 v[202:203], v222 offset:36864\n"
      "v_mfma_f32_16x16x32_bf16 v[20:23], v[204:207], v[152:155], v[20:23]\n"
      "s_waitcnt lgkmcnt(8)\n"
      "v_mfma_f32_16x16x32_bf16 v[24:27], v[208:211], v[144:147], v[24:27]\n"
      "ds_read_b64_tr_b16 v[204:205], v223 offset:32768\n"
      "v_mfma_f32_16x16x32_bf16 v[28:31], v[208:211], v[148:151], v[28:31]\n"
      "ds_read_b64_tr_b16 v[206:207], v223 offset:36864\n"
      "v_mfma_f32_16x16x32_bf16 v[32:35], v[208:211], v[152:155], v[32:35]\n"
      "s_waitcnt lgkmcnt(8)\n"
      "v_mfma_f32_16x16x32_bf16 v[36:39], v[212:215], v[144:147], v[36:39]\n"
      "ds_read_b64_tr_b16 v[208:209], v216 offset:40960\n"
      "v_mfma_f32_16x16x32_bf16 v[40:43], v[212:215], v[148:151], v[40:43]\n"
      "ds_read_b64_tr_b16 v[210:211], v216 offset:45056\n"
      "v_mfma_f32_16x16x32_bf16 v[44:47], v[212:215], v[152:155], v[44:47]\n"
      "s_waitcnt lgkmcnt(8)\n"
      "v_mfma_f32_16x16x32_bf16 v[48:51], v[192:195], v[144:147], v[48:51]\n"
      "ds_read_b64_tr_b16 v[212:213], v217 offset:40960\n"
      "v_mfma_f32_16x16x32_bf16 v[52:55], v[192:195], v[148:151], v[52:55]\n"
      "ds_read_b64_tr_b16 v[214:215], v217 offset:45056\n"
      "v_mfma_f32_16x16x32_bf16 v[56:59], v[192:195], v[152:155], v[56:59]\n"
      "s_waitcnt lgkmcnt(8)\n"
      "v_mfma_f32_16x16x32_bf16 v[60:63], v[196:199], v[144:147], v[60:63]\n"
      "ds_read_b64_tr_b16 v[192:193], v218 offset:40960\n"
      "v_mfma_f32_16x16x32_bf16 v[64:67], v[196:199], v[148:151], v[64:67]\n"
      "ds_read_b64_tr_b16 v[194:195], v218 offset:45056\n"
      "v_mfma_f32_16x16x32_bf16 v[68:71], v[196:199], v[152:155], v[68:71]\n"
      "s_waitcnt lgkmcnt(8)\n"
      "v_mfma_f32_16x16x32_bf16 v[72:75], v[200:203], v[144:147], v[72:75]\n"
      "ds_read_b64_tr_b16 v[196:197], v219 offset:40960\n"
      "v_mfma_f32_16x16x32_bf16 v[76:79], v[200:203], v[148:151], v[76:79]\n"
      "ds_read_b64_tr_b16 v[198:199], v219 offset:45056\n"
      "v_mfma_f32_16x16x32_bf16 v[80:83], v[200:203], v[152:155], v[80:83]\n"
      "s_waitcnt lgkmcnt(8)\n"
      "v_mfma_f32_16x16x32_bf16 v[84:87], v[204:207], v[144:147], v[84:87]\n"
      "ds_read_b64_tr_b16 v[200:201], v220 offset:40960\n"
      "v_mfma_f32_16x16x32_bf16 v[88:91], v[204:207], v[148:151], v[88:91]\n"
      "ds_read_b64_tr_b16 v[202:203], v220 offset:45056\n"
      "v_mfma_f32_16x16x32_bf16 v[92:95], v[204:207], v[152:155], v[92:95]\n"
      "s_waitcnt lgkmcnt(8)\n"
      "v_mfma_f32_16x16x32_bf16 v[0:3], v[208:211], v[156:159], v[0:3]\n"
      "ds_read_b64_tr_b16 v[204:205], v221 offset:40960\n"
      "v_mfma_f32_16x16x32_bf16 v[4:7], v[208:211], v[160:163], v[4:7]\n"
      "ds_read_b64_tr_b16 v[206:207], v221 offset:45056\n"
      "v_mfma_f32_16x16x32_bf16 v[8:11], v[208:211], v[164:167], v[8:11]\n"
      "s_waitcnt lgkmcnt(8)\n"
      "v_mfma_f32_16x16x32_bf16 v[12:15], v[212:215], v[156:159], v[12:15]\n"
      "ds_read_b64_tr_b16 v[208:209], v222 offset:40960\n"
      "v_mfma_f32_16x16x32_bf16 v[16:19], v[212:215], v[160:163], v[16:19]\n"
      "ds_read_b64_tr_b16 v[210:211], v222 offset:45056\n"
      "v_mfma_f32_16x16x32_bf16 v[20:23], v[212:215], v[164:167], v[20:23]\n"
      "s_waitcnt lgkmcnt(8)\n"
      "v_mfma_f32_16x16x32_bf16 v[24:27], v[192:195], v[156:159], v[24:27]\n"
      "ds_read_b64_tr_b16 v[212:213], v223 offset:40960\n"
      "v_mfma_f32_16x16x32_bf16 v[28:31], v[192:195], v[160:163], v[28:31]\n"
      "ds_read_b64_tr_b16 v[214:215], v223 offset:45056\n"
      "v_mfma_f32_16x16x32_bf16 v[32:35], v[192:195], v[164:167], v[32:35]\n"
      "s_waitcnt lgkmcnt(8)\n"
      "v_mfma_f32_16x16x32_bf16 v[36:39], v[196:199], v[156:159], v[36:39]\n"
      "ds_read_b64_tr_b16 v[192:193], v216 offset:49152\n"
      "v_mfma_f32_16x16x32_bf16 v[40:43], v[196:199], v[160:163], v[40:43]\n"
      "ds_read_b64_tr_b16 v[194:195], v216 offset:53248\n"
      "v_mfma_f32_16x16x32_bf16 v[44:47], v[196:199], v[164:167], v[44:47]\n"
      "s_waitcnt lgkmcnt(8)\n"
      "v_mfma_f32_16x16x32_bf16 v[48:51], v[200:203], v[156:159], v[48:51]\n"
      "ds_read_b64_tr_b16 v[196:197], v217 offset:49152\n"
      "v_mfma_f32_16x16x32_bf16 v[52:55], v[200:203], v[160:163], v[52:55]\n"
      "ds_read_b64_tr_b16 v[198:199], v217 offset:53248\n"
      "v_mfma_f32_16x16x32_bf16 v[56:59], v[200:203], v[164:167], v[56:59]\n"
      "s_waitcnt lgkmcnt(8)\n"
      "v_mfma_f32_16x16x32_bf16 v[60:63], v[204:207], v[156:159], v[60:63]\n"
      "ds_read_b64_tr_b16 v[200:201], v218 offset:49152\n"
      "v_mfma_f32_16x16x32_bf16 v[64:67], v[204:207], v[160:163], v[64:67]\n"
      "ds_read_b64_tr_b16 v[202:203], v218 offset:53248\n"
      "v_mfma_f32_16x16x32_bf16 v[68:71], v[204:207], v[164:167], v[68:71]\n"
      "s_waitcnt lgkmcnt(8)\n"
      "v_mfma_f32_16x16x32_bf16 v[72:75], v[208:211], v[156:159], v[72:75]\n"
      "ds_read_b64_tr_b16 v[204:205], v219 offset:49152\n"
      "v_mfma_f32_16x16x32_bf16 v[76:79], v[208:211], v[160:163], v[76:79]\n"
      "ds_read_b64_tr_b16 v[206:207], v219 offset:53248\n"
      "v_mfma_f32_16x16x32_bf16 v[80:83], v[208:211], v[164:167], v[80:83]\n"
      "buffer_load_dwordx2 v[250:251], v242, %[rf], 0 offen sc1\n"
      "s_waitcnt lgkmcnt(8)\n"
      "v_mfma_f32_16x16x32_bf16 v[84:87], v[212:215], v[156:159], v[84:87]\n"
      "ds_read_b64_tr_b16 v[208:209], v220 offset:49152\n"
      "v_mfma_f32_16x16x32_bf16 v[88:91], v[212:215], v[160:163], v[88:91]\n"
      "ds_read_b64_tr_b16 v[210:211], v220 offset:53248\n"
      "v_mfma_f32_16x16x32_bf16 v[92:95], v[212:215], v[164:167], v[92:95]\n"
      "s_waitcnt lgkmcnt(8)\n"
      "v_mfma_f32_16x16x32_bf16 v[0:3], v[192:195], v[168:171], v[0:3]\n"
      "ds_read_b64_tr_b16 v[212:213], v221 offset:49152\n"
      "v_mfma_f32_16x16x32_bf16 v[4:7], v[192:195], v[172:175], v[4:7]\n"
      "ds_read_b64_tr_b16 v[214:215], v221 offset:53248\n"
      "v_mfma_f32_16x16x32_bf16 v[8:11], v[192:195], v[176:179], v[8:11]\n"
      "s_waitcnt lgkmcnt(8)\n"
      "v_mfma_f32_16x16x32_bf16 v[12:15], v[196:199], v[168:171], v[12:15]\n"
      "ds_read_b64_tr_b16 v[192:193], v222 offset:49152\n"
      "v_mfma_f32_16x16x32_bf16 v[16:19], v[196:199], v[172:175], v[16:19]\n"
      "ds_read_b64_tr_b16 v[194:195], v222 offset:53248\n"
      "v_mfma_f32_16x16x32_bf16 v[20:23], v[196:199], v[176:179], v[20:23]\n"
      "s_waitcnt lgkmcnt(8)\n"
      "v_mfma_f32_16x16x32_bf16 v[24:27], v[200:203], v[168:171], v[24:27]\n"
      "ds_read_b64_tr_b16 v[196:197], v223 offset:49152\n"
      "v_mfma_f32_16x16x32_bf16 v[28:31], v[200:203], v[172:175], v[28:31]\n"
      "ds_read_b64_tr_b16 v[198:199], v223 offset:53248\n"
      "v_mfma_f32_16x16x32_bf16 v[32:35], v[200:203], v[176:179], v[32:35]\n"
      "s_waitcnt lgkmcnt(8)\n"
      "v_mfma_f32_16x16x32_bf16 v[36:39], v[204:207], v[168:171], v[36:39]\n"
      "ds_read_b64_tr_b16 v[200:201], v216 offset:57344\n"
      "v_mfma_f32_16x16x32_bf16 v[40:43], v[204:207], v[172:175], v[40:43]\n"
      "ds_read_b64_tr_b16 v[202:203], v216 offset:61440\n"
      "v_mfma_f32_16x16x32_bf16 v[44:47], v[204:207], v[176:179], v[44:47]\n"
      "s_waitcnt lgkmcnt(8)\n"
      "v_mfma_f32_16x16x32_bf16 v[48:51], v[208:211], v[168:171], v[48:51]\n"
      "ds_read_b64_tr_b16 v[204:205], v217 offset:57344\n"
      "v_mfma_f32_16x16x32_bf16 v[52:55], v[208:211], v[172:175], v[52:55]\n"
      "ds_read_b64_tr_b16 v[206:207], v217 offset:61440\n"
      "v_mfma_f32_16x16x32_bf16 v[56:59], v[208:211], v[176:179], v[56:59]\n"
      "ds_read_b64 v[252:253], v240 offset:240\n"
      "s_waitcnt lgkmcnt(9)\n"
      "v_mfma_f32_16x16x32_bf16 v[60:63], v[212:215], v[168:171], v[60:63]\n"
      "ds_read_b64_tr_b16 v[208:209], v218 offset:57344\n"
      "v_mfma_f32_16x16x32_bf16 v[64:67], v[212:215], v[172:175], v[64:67]\n"
      "ds_read_b64_tr_b16 v[210:211], v218 offset:61440\n"
      "v_mfma_f32_16x16x32_bf16 v[68:71], v[212:215], v[176:179], v[68:71]\n"
      "s_waitcnt lgkmcnt(9)\n"
      "v_mfma_f32_16x16x32_bf16 v[72:75], v[192:195], v[168:171], v[72:75]\n"
      "ds_read_b64_tr_b16 v[212:213], v219 offset:57344\n"
      "v_mfma_f32_16x16x32_bf16 v[76:79], v[192:195], v[172:175], v[76:79]\n"
      "ds_read_b64_tr_b16 v[214:215], v219 offset:61440\n"
      "v_mfma_f32_16x16x32_bf16 v[80:83], v[192:195], v[176:179], v[80:83]\n"
      "s_waitcnt lgkmcnt(9)\n"
      "v_mfma_f32_16x16x32_bf16 v[84:87], v[196:199], v[168:171], v[84:87]\n"
      "ds_read_b64_tr_b16 v[192:193], v220 offset:57344\n"
      "v_mfma_f32_16x16x32_bf16 v[88:91], v[196:199], v[172:175], v[88:91]\n"
      "ds_read_b64_tr_b16 v[194:195], v220 offset:61440\n"
      "v_mfma_f32_16x16x32_bf16 v[92:95], v[196:199], v[176:179], v[92:95]\n"
      "s_waitcnt lgkmcnt(9)\n"
      "v_mfma_f32_16x16x32_bf16 v[0:3], v[200:203], v[180:183], v[0:3]\n"
      "ds_read_b64_tr_b16 v[196:197], v221 offset:57344\n"
      "v_mfma_f32_16x16x32_bf16 v[4:7], v[200:203], v[184:187], v[4:7]\n"
      "ds_read_b64_tr_b16 v[198:199], v221 offset:61440\n"
      "v_mfma_f32_16x16x32_bf16 v[8:11], v[200:203], v[188:191], v[8:11]\n"
      "s_waitcnt lgkmcnt(9)\n"
      "v_mfma_f32_16x16x32_bf16 v[12:15], v[204:207], v[180:183], v[12:15]\n"
      "ds_read_b64_tr_b16 v[200:201], v222 offset:57344\n"
      "v_mfma_f32_16x16x32_bf16 v[16:19], v[204:207], v[184:187], v[16:19]\n"
      "ds_read_b64_tr_b16 v[202:203], v222 offset:61440\n"
      "v_mfma_f32_16x16x32_bf16 v[20:23], v[204:207], v[188:191], v[20:23]\n"
      "s_waitcnt lgkmcnt(10)\n"
      "v_and_b32 v252, v252, v253\n"
      "s_nop 0\n"
      "v_readfirstlane_b32 %[t1], v252\n"
      "s_cmp_eq_u32 %[t1], 0x01010101\n"
      "s_cbranch_scc1 .Lkt2_d\n"
      "s_mov_b32 %[t1], 0x100000\n"
      ".Lkt2_l:\n"
      "s_sleep 1\n"
      "ds_read_b64 v[252:253], v240 offset:240\n"
      "s_waitcnt lgkmcnt(0)\n"
      "v_and_b32 v252, v252, v253\n"
      "s_nop 0\n"
      "v_readfirstlane_b32 %[hv], v252\n"
      "s_cmp_eq_u32 %[hv], 0x01010101\n"
      "s_cbranch_scc1 .Lkt2_d\n"
      "s_sub_u32 %[t1], %[t1], 1\n"
      "s_cbranch_scc0 .Lkt2_l\n"
      ".Lkt2_d:\n"
      "s_waitcnt lgkmcnt(8)\n"
      "v_mfma_f32_16x16x32_bf16 v[24:27], v[208:211], v[180:183], v[24:27]\n"
      "ds_read_b64_tr_b16 v[204:205], v223 offset:57344\n"
      "v_mfma_f32_16x16x32_bf16 v[28:31], v[208:211], v[184:187], v[28:31]\n"
      "ds_read_b64_tr_b16 v[206:207], v223 offset:61440\n"
      "v_mfma_f32_16x16x32_bf16 v[32:35], v[208:211], v[188:191], v[32:35]\n"
      "s_waitcnt lgkmcnt(8)\n"
      "v_mfma_f32_16x16x32_bf16 v[36:39], v[212:215], v[180:183], v[36:39]\n"
      "ds_read_b64_tr_b16 v[208:209], v224 offset:0\n"
      "v_mfma_f32_16x16x32_bf16 v[40:43], v[212:215], v[184:187], v[40:43]\n"
      "ds_read_b64_tr_b16 v[210:211], v224 offset:4096\n"
      "v_mfma_f32_16x16x32_bf16 v[44:47], v[212:215], v[188:191], v[44:47]\n"
      "s_waitcnt lgkmcnt(8)\n"
      "v_mfma_f32_16x16x32_bf16 v[48:51], v[192:195], v[180:183], v[48:51]\n"
      "ds_read_b64_tr_b16 v[212:213], v225 offset:0\n"
      "v_mfma_f32_16x16x32_bf16 v[52:55], v[192:195], v[184:187], v[52:55]\n"
      "ds_read_b64_tr_b16 v[214:215], v225 offset:4096\n"
      "v_mfma_f32_16x16x32_bf16 v[56:59], v[192:195], v[188:191], v[56:59]\n"
      "s_waitcnt lgkmcnt(8)\n"
      "v_mfma_f32_16x16x32_bf16 v[60:63], v[196:199], v[180:183], v[60:63]\n"
      "ds_read_b64_tr_b16 v[192:193], v226 offset:0\n"
      "v_mfma_f32_16x16x32_bf16 v[64:67], v[196:199], v[184:187], v[64:67]\n"
      "ds_read_b64_tr_b16 v[194:195], v226 offset:4096\n"
      "v_mfma_f32_16x16x32_bf16 v[68:71], v[196:199], v[188:191], v[68:71]\n"
      "s_waitcnt lgkmcnt(8)\n"
      "v_mfma_f32_16x16x32_bf16 v[72:75], v[200:203], v[180:183], v[72:75]\n"
      "ds_read_b64_tr_b16 v[196:197], v227 offset:0\n"
      "v_mfma_f32_16x16x32_bf16 v[76:79], v[200:203], v[184:187], v[76:79]\n"
      "ds_read_b64_tr_b16 v[198:199], v227 offset:4096\n"
      "v_mfma_f32_16x16x32_bf16 v[80:83], v[200:203], v[188:191], v[80:83]\n"
      "s_waitcnt lgkmcnt(8)\n"
      "v_mfma_f32_16x16x32_bf16 v[84:87], v[204:207], v[180:183], v[84:87]\n"
      "ds_read_b64_tr_b16 v[200:201], v228 offset:0\n"
      "v_mfma_f32_16x16x32_bf16 v[88:91], v[204:207], v[184:187], v[88:91]\n"
      "ds_read_b64_tr_b16 v[202:203], v228 offset:4096\n"
      "v_mfma_f32_16x16x32_bf16 v[92:95], v[204:207], v[188:191], v[92:95]\n"
      KTS(2)
      "s_waitcnt vmcnt(0)\n"
      "v_cmp_eq_u64 vcc, %[tg], v[250:251]\n"
      "s_mov_b32 %[sn], vcc_lo\n"
      "s_cmp_eq_u32 %[h2], 0\n"
      "s_cbranch_scc0 .Lkl2\n"
      "s_bitcmp1_b32 %[sn], 2\n"
      "s_cbranch_scc1 .Lkb2_d\n"
      "s_mov_b32 %[t1], 0x100000\n"
      ".Lkb2_l:\n"
      "s_sleep 1\n"
      "buffer_load_dwordx2 v[250:251], v242, %[rf], 0 offen sc1\n"
      "s_waitcnt vmcnt(0)\n"
      "v_cmp_eq_u64 vcc, %[tg], v[250:251]\n"
      "s_mov_b32 %[sn], vcc_lo\n"
      "s_bitcmp1_b32 %[sn], 2\n"
      "s_cbranch_scc1 .Lkb2_d\n"
      "s_sub_u32 %[t1], %[t1], 1\n"
      "s_cbranch_scc0 .Lkb2_l\n"
      ".Lkb2_d:\n"
      "s_add_u32 %[t0], %[jn], 2\n"
      "s_and_b32 %[t0], %[t0], 3\n"
      "s_mul_i32 %[t0], %[t0], 12288\n"
      "buffer_load_dwordx4 v[96:99], v233, %[ro], %[t0] offen offset:0 sc1\n"
      "buffer_load_dwordx4 v[100:103], v233, %[ro], %[t0] offen offset:1024 sc1\n"
      "buffer_load_dwordx4 v[104:107], v233, %[ro], %[t0] offen offset:2048 sc1\n"
      "buffer_load_dwordx4 v[108:111], v233, %[ro], %[t0] offen offset:3072 sc1\n"
      "s_add_u32 %[t0], %[t0], 4096\n"
      "buffer_load_dwordx4 v[112:115], v233, %[ro], %[t0] offen offset:0 sc1\n"
      "buffer_load_dwordx4 v[116:119], v233, %[ro], %[t0] offen offset:1024 sc1\n"
      "buffer_load_dwordx4 v[120:123], v233, %[ro], %[t0] offen offset:2048 sc1\n"
      "buffer_load_dwordx4 v[124:127], v233, %[ro], %[t0] offen offset:3072 sc1\n"
      "s_add_u32 %[t0], %[t0], 4096\n"
      "buffer_load_dwordx4 v[128:131], v233, %[ro], %[t0] offen offset:0 sc1\n"
      "buffer_load_dwordx4 v[132:135], v233, %[ro], %[t0] offen offset:1024 sc1\n"
      "buffer_load_dwordx4 v[136:139], v233, %[ro], %[t0] offen offset:2048 sc1\n"
      "buffer_load_dwordx4 v[140:143], v233, %[ro], %[t0] offen offset:3072 sc1\n"
      "s_waitcnt vmcnt(0)\n"
      ".Lkl2:\n"
      "s_bitcmp1_b32 %[sn], 3\n"
      "s_cselect_b32 %[h2], 1, 0\n"
      "s_cselect_b32 s42, 49152, 0\n"
      "s_waitcnt lgkmcnt(8)\n"
      "v_mfma_f32_16x16x32_bf16 v[0:3], v[208:211], v[96:99], v[0:3]\n"
      "ds_read_b64_tr_b16 v[204:205], v229 offset:0\n"
      "v_mfma_f32_16x16x32_bf16 v[4:7], v[208:211], v[100:103], v[4:7]\n"
      "ds_read_b64_tr_b16 v[206:207], v229 offset:4096\n"
      "v_mfma_f32_16x16x32_bf16 v[8:11], v[208:211], v[104:107], v[8:11]\n"
      "v_add_u32 v216, 0, v232\n"
      "v_xor_b32 v217, 32, v216\n"
      "v_xor_b32 v218, 64, v216\n"
      "v_xor_b32 v219, 96, v216\n"
      "s_waitcnt lgkmcnt(8)\n"
      "v_mfma_f32_16x16x32_bf16 v[12:15], v[212:215], v[96:99], v[12:15]\n"
      "ds_read_b64_tr_b16 v[208:209], v230 offset:0\n"
      "v_mfma_f32_16x16x32_bf16 v[16:19], v[212:215], v[100:103], v[16:19]\n"
      "ds_read_b64_tr_b16 v[210:211], v230 offset:4096\n"
      "v_mfma_f32_16x16x32_bf16 v[20:23], v[212:215], v[104:107], v[20:23]\n"
      "v_xor_b32 v220, 128, v216\n"
      "v_xor_b32 v221, 160, v216\n"
      "v_xor_b32 v222, 192, v216\n"
      "v_xor_b32 v223, 224, v216\n"
      "s_waitcnt lgkmcnt(8)\n"
      "v_mfma_f32_16x16x32_bf16 v[24:27], v[192:195], v[96:99], v[24:27]\n"
      "ds_read_b64_tr_b16 v[212:213], v231 offset:0\n"
      "s_add_u32 %[t0], %[jn], 3\n"
      "s_and_b32 %[t0], %[t0], 3\n"
      "s_mul_i32 %[t0], %[t0], 12288\n"
      "buffer_load_dwordx4 v[144:147], v233, s[40:43], %[t0] offen offset:0 sc1\n"
      "v_mfma_f32_16x16x32_bf16 v[28:31], v[192:195], v[100:103], v[28:31]\n"
      "ds_read_b64_tr_b16 v[214:215], v231 offset:4096\n"
      "buffer_load_dwordx4 v[148:151], v233, s[40:43], %[t0] offen offset:1024 sc1\n"
      "v_mfma_f32_16x16x32_bf16 v[32:35], v[192:195], v[104:107], v[32:35]\n"
      "buffer_load_dwordx4 v[152:155], v233, s[40:43], %[t0] offen offset:2048 sc1\n"
      "s_waitcnt lgkmcnt(8)\n"
      "v_mfma_f32_16x16x32_bf16 v[36:39], v[196:199], v[96:99], v[36:39]\n"
      "ds_read_b64_tr_b16 v[192:193], v224 offset:8192\n"
      "buffer_load_dwordx4 v[156:159], v233, s[40:43], %[t0] offen offset:3072 sc1\n"
      "v_mfma_f32_16x16x32_bf16 v[40:43], v[196:199], v[100:103], v[40:43]\n"
      "ds_read_b64_tr_b16 v[194:195], v224 offset:12288\n"
      "s_add_u32 %[t0], %[t0], 4096\n"
      "buffer_load_dwordx4 v[160:163], v233, s[40:43], %[t0] offen offset:0 sc1\n"
      "v_mfma_f32_16x16x32_bf16 v[44:47], v[196:199], v[104:107], v[44:47]\n"
      "buffer_load_dwordx4 v[164:167], v233, s[40:43], %[t0] offen offset:1024 sc1\n"
      "s_waitcnt lgkmcnt(8)\n"
      "v_mfma_f32_16x16x32_bf16 v[48:51], v[200:203], v[96:99], v[48:51]\n"
      "ds_read_b64_tr_b16 v[196:197], v225 offset:8192\n"
      "buffer_load_dwordx4 v[168:171], v233, s[40:43], %[t0] offen offset:2048 sc1\n"
      "v_mfma_f32_16x16x32_bf16 v[52:55], v[200:203], v[100:103], v[52:55]\n"
      "ds_read_b64_tr_b16 v[198:199], v225 offset:12288\n"
      "buffer_load_dwordx4 v[172:175], v233, s[40:43], %[t0] offen offset:3072 sc1\n"
      "v_mfma_f32_16x16x32_bf16 v[56:59], v[200:203], v[104:107], v[56:59]\n"
      "s_add_u32 %[t0], %[t0], 4096\n"
      "buffer_load_dwordx4 v[176:179], v233, s[40:43], %[t0] offen offset:0 sc1\n"
      "s_waitcnt lgkmcnt(8)\n"
      "v_mfma_f32_16x16x32_bf16 v[60:63], v[204:207], v[96:99], v[60:63]\n"
      "ds_read_b64_tr_b16 v[200:201], v226 offset:8192\n"
      "buffer_load_dwordx4 v[180:183], v233, s[40:43], %[t0] offen offset:1024 sc1\n"
      "v_mfma_f32_16x16x32_bf16 v[64:67], v[204:207], v[100:103], v[64:67]\n"
      "ds_read_b64_tr_b16 v[202:203], v226 offset:12288\n"
      "buffer_load_dwordx4 v[184:187], v233, s[40:43], %[t0] offen offset:2048 sc1\n"
      "v_mfma_f32_16x16x32_bf16 v[68:71], v[204:207], v[104:107], v[68:71]\n"
      "buffer_load_dwordx4 v[188:191], v233, s[40:43], %[t0] offen offset:3072 sc1\n"
      "s_waitcnt lgkmcnt(8)\n"
      "v_mfma_f32_16x16x32_bf16 v[72:75], v[208:211], v[96:99], v[72:75]\n"
      "ds_read_b64_tr_b16 v[204:205], v227 offset:8192\n"
      "v_mfma_f32_16x16x32_bf16 v[76:79], v[208:211], v[100:103], v[76:79]\n"
      "ds_read_b64_tr_b16 v[206:207], v227 offset:12288\n"
      "v_mfma_f32_16x16x32_bf16 v[80:83], v[208:211], v[104:107], v[80:83]\n"
      "s_waitcnt lgkmcnt(8)\n"
      "v_mfma_f32_16x16x32_bf16 v[84:87], v[212:215], v[96:99], v[84:87]\n"
      "ds_read_b64_tr_b16 v[208:209], v228 offset:8192\n"
      "v_mfma_f32_16x16x32_bf16 v[88:91], v[212:215], v[100:103], v[88:91]\n"
      "ds_read_b64_tr_b16 v[210:211], v228 offset:12288\n"
      "v_mfma_f32_16x16x32_bf16 v[92:95], v[212:215], v[104:107], v[92:95]\n"
      "s_waitcnt lgkmcnt(8)\n"
      "v_mfma_f32_16x16x32_bf16 v[0:3], v[192:195], v[108:111], v[0:3]\n"
      "ds_read_b64_tr_b16 v[212:213], v229 offset:8192\n"
      "v_mfma_f32_16x16x32_bf16 v[4:7], v[192:195], v[112:115], v[4:7]\n"
      "ds_read_b64_tr_b16 v[214:215], v229 offset:12288\n"
      "v_mfma_f32_16x16x32_bf16 v[8:11], v[192:195], v[116:119], v[8:11]\n"
      "s_waitcnt lgkmcnt(8)\n"
      "v_mfma_f32_16x16x32_bf16 v[12:15], v[196:199], v[108:111], v[12:15]\n"
      "ds_read_b64_tr_b16 v[192:193], v230 offset:8192\n"
      "v_mfma_f32_16x16x32_bf16 v[16:19], v[196:199], v[112:115], v[16:19]\n"
      "ds_read_b64_tr_b16 v[194:195], v230 offset:12288\n"
      "v_mfma_f32_16x16x32_bf16 v[20:23], v[196:199], v[116:119], v[20:23]\n"
      "s_waitcnt lgkmcnt(8)\n"
      "v_mfma_f32_16x16x32_bf16 v[24:27], v[200:203], v[108:111], v[24:27]\n"
      "ds_read_b64_tr_b16 v[196:197], v231 offset:8192\n"
      "v_mfma_f32_16x16x32_bf16 v[28:31], v[200:203], v[112:115], v[28:31]\n"
      "ds_read_b64_tr_b16 v[198:199], v231 offset:12288\n"
      "v_mfma_f32_16x16x32_bf16 v[32:35], v[200:203], v[116:119], v[32:35]\n"
      "s_waitcnt lgkmcnt(8)\n"
      "v_mfma_f32_16x16x32_bf16 v[36:39], v[204:207], v[108:111], v[36:39]\n"
      "ds_read_b64_tr_b16 v[200:201], v216 offset:0\n"
      "v_mfma_f32_16x16x32_bf16 v[40:43], v[204:207], v[112:115], v[40:43]\n"
      "ds_read_b64_tr_b16 v[202:203], v216 offset:4096\n"
      "v_mfma_f32_16x16x32_bf16 v[44:47], v[204:207], v[116:119], v[44:47]\n"
      "s_waitcnt lgkmcnt(8)\n"
      "v_mfma_f32_16x16x32_bf16 v[48:51], v[208:211], v[108:111], v[48:51]\n"
      "ds_read_b64_tr_b16 v[204:205], v217 offset:0\n"
      "v_mfma_f32_16x16x32_bf16 v[52:55], v[208:211], v[112:115], v[52:55]\n"
      "ds_read_b64_tr_b16 v[206:207], v217 offset:4096\n"
      "v_mfma_f32_16x16x32_bf16 v[56:59], v[208:211], v[116:119], v[56:59]\n"
      "s_waitcnt lgkmcnt(8)\n"
      "v_mfma_f32_16x16x32_bf16 v[60:63], v[212:215], v[108:111], v[60:63]\n"
      "ds_read_b64_tr_b16 v[208:209], v218 offset:0\n"
      "v_mfma_f32_16x16x32_bf16 v[64:67], v[212:215], v[112:115], v[64:67]\n"
      "ds_read_b64_tr_b16 v[210:211], v218 offset:4096\n"
      "v_mfma_f32_16x16x32_bf16 v[68:71], v[212:215], v[116:119], v[68:71]\n"
      "s_waitcnt lgkmcnt(8)\n"
      "v_mfma_f32_16x16x32_bf16 v[72:75], v[192:195], v[108:111], v[72:75]\n"
      "ds_read_b64_tr_b16 v[212:213], v219 offset:0\n"
      "v_mfma_f32_16x16x32_bf16 v[76:79], v[192:195], v[112:115], v[76:79]\n"
      "ds_read_b64_tr_b16 v[214:215], v219 offset:4096\n"
      "v_mfma_f32_16x16x32_bf16 v[80:83], v[192:195], v[116:119], v[80:83]\n"
      "s_waitcnt lgkmcnt(8)\n"
      "v_mfma_f32_16x16x32_bf16 v[84:87], v[196:199], v[108:111], v[84:87]\n"
      "ds_read_b64_tr_b16 v[192:193], v220 offset:0\n"
      "v_mfma_f32_16x16x32_bf16 v[88:91], v[196:199], v[112:115], v[88:91]\n"
      "ds_read_b64_tr_b16 v[194:195], v220 offset:4096\n"
      "v_mfma_f32_16x16x32_bf16 v[92:95], v[196:199], v[116:119], v[92:95]\n"
      "s_waitcnt lgkmcnt(8)\n"
      "v_mfma_f32_16x16x32_bf16 v[0:3], v[200:203], v[120:123], v[0:3]\n"
      "ds_read_b64_tr_b16 v[196:197], v221 offset:0\n"
      "v_mfma_f32_16x16x32_bf16 v[4:7], v[200:203], v[124:127], v[4:7]\n"
      "ds_read_b64_tr_b16 v[198:199], v221 offset:4096\n"
      "v_mfma_f32_16x16x32_bf16 v[8:11], v[200:203], v[128:131], v[8:11]\n"
      "s_waitcnt lgkmcnt(8)\n"
      "v_mfma_f32_16x16x32_bf16 v[12:15], v[204:207], v[120:123], v[12:15]\n"
      "ds_read_b64_tr_b16 v[200:201], v222 offset:0\n"
      "v_mfma_f32_16x16x32_bf16 v[16:19], v[204:207], v[124:127], v[16:19]\n"
      "ds_read_b64_tr_b16 v[202:203], v222 offset:4096\n"
      "v_mfma_f32_16x16x32_bf16 v[20:23], v[204:207], v[128:131], v[20:23]\n"
      "s_waitcnt lgkmcnt(8)\n"
      "v_mfma_f32_16x16x32_bf16 v[24:27], v[208:211], v[120:123], v[24:27]\n"
      "ds_read_b64_tr_b16 v[204:205], v223 offset:0\n"
      "v_mfma_f32_16x16x32_bf16 v[28:31], v[208:211], v[124:127], v[28:31]\n"
      "ds_read_b64_tr_b16 v[206:207], v223 offset:4096\n"
      "v_mfma_f32_16x16x32_bf16 v[32:35], v[208:211], v[128:131], v[32:35]\n"
      "s_waitcnt lgkmcnt(8)\n"
      "v_mfma_f32_16x16x32_bf16 v[36:39], v[212:215], v[120:123], v[36:39]\n"
      "ds_read_b64_tr_b16 v[208:209], v216 offset:8192\n"
      "v_mfma_f32_16x16x32_bf16 v[40:43], v[212:215], v[124:127], v[40:43]\n"
      "ds_read_b64_tr_b16 v[210:211], v216 offset:12288\n"
      "v_mfma_f32_16x16x32_bf16 v[44:47], v[212:215], v[128:131], v[44:47]\n"
      "s_waitcnt lgkmcnt(8)\n"
      "v_mfma_f32_16x16x32_bf16 v[48:51], v[192:195], v[120:123], v[48:51]\n"
      "ds_read_b64_tr_b16 v[212:213], v217 offset:8192\n"
      "v_mfma_f32_16x16x32_bf16 v[52:55], v[192:195], v[124:127], v[52:55]\n"
      "ds_read_b64_tr_b16 v[214:215], v217 offset:12288\n"
      "v_mfma_f32_16x16x32_bf16 v[56:59], v[192:195], v[128:131], v[56:59]\n"
      "ds_read_b64 v[252:253], v240 offset:248\n"
      "s_waitcnt lgkmcnt(9)\n"
      "v_mfma_f32_16x16x32_bf16 v[60:63], v[196:199], v[120:123], v[60:63]\n"
      "ds_read_b64_tr_b16 v[192:193], v218 offset:8192\n"
      "v_mfma_f32_16x16x32_bf16 v[64:67], v[196:199], v[124:127], v[64:67]\n"
      "ds_read_b64_tr_b16 v[194:195], v218 offset:12288\n"
      "v_mfma_f32_16x16x32_bf16 v[68:71], v[196:199], v[128:131], v[68:71]\n"
      "s_waitcnt lgkmcnt(9)\n"
      "v_mfma_f32_16x16x32_bf16 v[72:75], v[200:203], v[120:123], v[72:75]\n"
      "ds_read_b64_tr_b16 v[196:197], v219 offset:8192\n"
      "v_mfma_f32_16x16x32_bf16 v[76:79], v[200:203], v[124:127], v[76:79]\n"
      "ds_read_b64_tr_b16 v[198:199], v219 offset:12288\n"
      "v_mfma_f32_16x16x32_bf16 v[80:83], v[200:203], v[128:131], v[80:83]\n"
      "s_waitcnt lgkmcnt(9)\n"
      "v_mfma_f32_16x16x32_bf16 v[84:87], v[204:207], v[120:123], v[84:87]\n"
      "ds_read_b64_tr_b16 v[200:201], v220 offset:8192\n"
      "v_mfma_f32_16x16x32_bf16 v[88:91], v[204:207], v[124:127], v[88:91]\n"
      "ds_read_b64_tr_b16 v[202:203], v220 offset:12288\n"
      "v_mfma_f32_16x16x32_bf16 v[92:95], v[204:207], v[128:131], v[92:95]\n"
      "s_waitcnt lgkmcnt(9)\n"
      "v_mfma_f32_16x16x32_bf16 v[0:3], v[208:211], v[132:135], v[0:3]\n"
      "ds_read_b64_tr_b16 v[204:205], v221 offset:8192\n"
      "v_mfma_f32_16x16x32_bf16 v[4:7], v[208:211], v[136:139], v[4:7]\n"
      "ds_read_b64_tr_b16 v[206:207], v221 offset:12288\n"
      "v_mfma_f32_16x16x32_bf16 v[8:11], v[208:211], v[140:143], v[8:11]\n"
      "s_waitcnt lgkmcnt(9)\n"
      "v_mfma_f32_16x16x32_bf16 v[12:15], v[212:215], v[132:135], v[12:15]\n"
      "ds_read_b64_tr_b16 v[208:209], v222 offset:8192\n"
      "v_mfma_f32_16x16x32_bf16 v[16:19], v[212:215], v[136:139], v[16:19]\n"
      "ds_read_b64_tr_b16 v[210:211], v222 offset:12288\n"
      "v_mfma_f32_16x16x32_bf16 v[20:23], v[212:215], v[140:143], v[20:23]\n"
      "s_waitcnt lgkmcnt(10)\n"
      "v_and_b32 v252, v252, v253\n"
      "s_nop 0\n"
      "v_readfirstlane_b32 %[t1], v252\n"
      "s_cmp_eq_u32 %[t1], 0x01010101\n"
      "s_cbranch_scc1 .Lkt3_d\n"
      "s_mov_b32 %[t1], 0x100000\n"
      ".Lkt3_l:\n"
      "s_sleep 1\n"
      "ds_read_b64 v[252:253], v240 offset:248\n"
      "s_waitcnt lgkmcnt(0)\n"
      "v_and_b32 v252, v252, v253\n"
      "s_nop 0\n"
      "v_readfirstlane_b32 %[hv], v252\n"
      "s_cmp_eq_u32 %[hv], 0x01010101\n"
      "s_cbranch_scc1 .Lkt3_d\n"
      "s_sub_u32 %[t1], %[t1], 1\n"
      "s_cbranch_scc0 .Lkt3_l\n"
      ".Lkt3_d:\n"
      "s_waitcnt lgkmcnt(8)\n"
      "v_mfma_f32_16x16x32_bf16 v[24:27], v[192:195], v[132:135], v[24:27]\n"
      "ds_read_b64_tr_b16 v[212:213], v223 offset:8192\n"
      "v_mfma_f32_16x16x32_bf16 v[28:31], v[192:195], v[136:139], v[28:31]\n"
      "ds_read_b64_tr_b16 v[214:215], v223 offset:12288\n"
      "v_mfma_f32_16x16x32_bf16 v[32:35], v[192:195], v[140:143], v[32:35]\n"
      "s_waitcnt lgkmcnt(8)\n"
      "v_mfma_f32_16x16x32_bf16 v[36:39], v[196:199], v[132:135], v[36:39]\n"
      "ds_read_b64_tr_b16 v[192:193], v216 offset:16384\n"
      "v_mfma_f32_16x16x32_bf16 v[40:43], v[196:199], v[136:139], v[40:43]\n"
      "ds_read_b64_tr_b16 v[194:195], v216 offset:20480\n"
      "v_mfma_f32_16x16x32_bf16 v[44:47], v[196:199], v[140:143], v[44:47]\n"
      "s_waitcnt lgkmcnt(8)\n"
      "v_mfma_f32_16x16x32_bf16 v[48:51], v[200:203], v[132:135], v[48:51]\n"
      "ds_read_b64_tr_b16 v[196:197], v216 offset:24576\n"
      "v_mfma_f32_16x16x32_bf16 v[52:55], v[200:203], v[136:139], v[52:55]\n"
      "ds_read_b64_tr_b16 v[198:199], v216 offset:28672\n"
      "v_mfma_f32_16x16x32_bf16 v[56:59], v[200:203], v[140:143], v[56:59]\n"
      "s_waitcnt lgkmcnt(8)\n"
      "v_mfma_f32_16x16x32_bf16 v[60:63], v[204:207], v[132:135], v[60:63]\n"
      "ds_read_b64_tr_b16 v[200:201], v216 offset:32768\n"
      "v_mfma_f32_16x16x32_bf16 v[64:67], v[204:207], v[136:139], v[64:67]\n"
      "ds_read_b64_tr_b16 v[202:203], v216 offset:36864\n"
      "v_mfma_f32_16x16x32_bf16 v[68:71], v[204:207], v[140:143], v[68:71]\n"
      "s_waitcnt lgkmcnt(8)\n"
      "v_mfma_f32_16x16x32_bf16 v[72:75], v[208:211], v[132:135], v[72:75]\n"
      "ds_read_b64_tr_b16 v[204:205], v216 offset:40960\n"
      "v_mfma_f32_16x16x32_bf16 v[76:79], v[208:211], v[136:139], v[76:79]\n"
      "ds_read_b64_tr_b16 v[206:207], v216 offset:45056\n"
      "v_mfma_f32_16x16x32_bf16 v[80:83], v[208:211], v[140:143], v[80:83]\n"
      "s_waitcnt lgkmcnt(8)\n"
      "v_mfma_f32_16x16x32_bf16 v[84:87], v[212:215], v[132:135], v[84:87]\n"
      "ds_read_b64_tr_b16 v[208:209], v217 offset:16384\n"
      "v_mfma_f32_16x16x32_bf16 v[88:91], v[212:215], v[136:139], v[88:91]\n"
      "ds_read_b64_tr_b16 v[210:211], v217 offset:20480\n"
      "v_mfma_f32_16x16x32_bf16 v[92:95], v[212:215], v[140:143], v[92:95]\n"
      KTS(3)
      "s_mov_b32 s42, 49152\n"
      "s_cmp_eq_u32 %[h2], 0\n"
      "s_cbranch_scc0 .Lkl3\n"
      "s_bitcmp1_b32 %[sn], 3\n"
      "s_cbranch_scc1 .Lkb3_d\n"
      "s_mov_b32 %[t1], 0x100000\n"
      ".Lkb3_l:\n"
      "s_sleep 1\n"
      "buffer_load_dwordx2 v[250:251], v242, %[rf], 0 offen sc1\n"
      "s_waitcnt vmcnt(0)\n"
      "v_cmp_eq_u64 vcc, %[tg], v[250:251]\n"
      "s_mov_b32 %[sn], vcc_lo\n"
      "s_bitcmp1_b32 %[sn], 3\n"
      "s_cbranch_scc1 .Lkb3_d\n"
      "s_sub_u32 %[t1], %[t1], 1\n"
      "s_cbranch_scc0 .Lkb3_l\n"
      ".Lkb3_d:\n"
      "s_add_u32 %[t0], %[jn], 3\n"
      "s_and_b32 %[t0], %[t0], 3\n"
      "s_mul_i32 %[t0], %[t0], 12288\n"
      "buffer_load_dwordx4 v[144:147], v233, %[ro], %[t0] offen offset:0 sc1\n"
      "buffer_load_dwordx4 v[148:151], v233, %[ro], %[t0] offen offset:1024 sc1\n"
      "buffer_load_dwordx4 v[152:155], v233, %[ro], %[t0] offen offset:2048 sc1\n"
      "buffer_load_dwordx4 v[156:159], v233, %[ro], %[t0] offen offset:3072 sc1\n"
      "s_add_u32 %[t0], %[t0], 4096\n"
      "buffer_load_dwordx4 v[160:163], v233, %[ro], %[t0] offen offset:0 sc1\n"
      "buffer_load_dwordx4 v[164:167], v233, %[ro], %[t0] offen offset:1024 sc1\n"
      "buffer_load_dwordx4 v[168:171], v233, %[ro], %[t0] offen offset:2048 sc1\n"
      "buffer_load_dwordx4 v[172:175], v233, %[ro], %[t0] offen offset:3072 sc1\n"
      "s_add_u32 %[t0], %[t0], 4096\n"
      "buffer_load_dwordx4 v[176:179], v233, %[ro], %[t0] offen offset:0 sc1\n"
      "buffer_load_dwordx4 v[180:183], v233, %[ro], %[t0] offen offset:1024 sc1\n"
      "buffer_load_dwordx4 v[184:187], v233, %[ro], %[t0] offen offset:2048 sc1\n"
      "buffer_load_dwordx4 v[188:191], v233, %[ro], %[t0] offen offset:3072 sc1\n"
      ".Lkl3:\n"
      "s_waitcnt vmcnt(0)\n"
      "s_waitcnt lgkmcnt(8)\n"
      "v_mfma_f32_16x16x32_bf16 v[0:3], v[192:195], v[144:147], v[0:3]\n"
      "ds_read_b64_tr_b16 v[212:213], v217 offset:24576\n"
      "v_mfma_f32_16x16x32_bf16 v[4:7], v[192:195], v[148:151], v[4:7]\n"
      "ds_read_b64_tr_b16 v[214:215], v217 offset:28672\n"
      "v_mfma_f32_16x16x32_bf16 v[8:11], v[192:195], v[152:155], v[8:11]\n"
      "v_mov_b32 v252, 1\n"
      "v_mov_b32 v253, %[lm]\n"
      "ds_write_b32 v253, v252\n"
      "s_waitcnt lgkmcnt(9)\n"
      "v_mfma_f32_16x16x32_bf16 v[0:3], v[196:199], v[156:159], v[0:3]\n"
      "ds_read_b64_tr_b16 v[192:193], v217 offset:32768\n"
      "v_mfma_f32_16x16x32_bf16 v[4:7], v[196:199], v[160:163], v[4:7]\n"
      "ds_read_b64_tr_b16 v[194:195], v217 offset:36864\n"
      "v_mfma_f32_16x16x32_bf16 v[8:11], v[196:199], v[164:167], v[8:11]\n"
      "s_waitcnt lgkmcnt(9)\n"
      "v_mfma_f32_16x16x32_bf16 v[0:3], v[200:203], v[168:171], v[0:3]\n"
      "ds_read_b64_tr_b16 v[196:197], v217 offset:40960\n"
      "v_mfma_f32_16x16x32_bf16 v[4:7], v[200:203], v[172:175], v[4:7]\n"
      "ds_read_b64_tr_b16 v[198:199], v217 offset:45056\n"
      "v_mfma_f32_16x16x32_bf16 v[8:11], v[200:203], v[176:179], v[8:11]\n"
      "s_waitcnt lgkmcnt(9)\n"
      "v_mfma_f32_16x16x32_bf16 v[0:3], v[204:207], v[180:183], v[0:3]\n"
      "ds_read_b64_tr_b16 v[200:201], v218 offset:16384\n"
      "v_mfma_f32_16x16x32_bf16 v[4:7], v[204:207], v[184:187], v[4:7]\n"
      "ds_read_b64_tr_b16 v[202:203], v218 offset:20480\n"
      "v_mfma_f32_16x16x32_bf16 v[8:11], v[204:207], v[188:191], v[8:11]\n"
      "s_waitcnt lgkmcnt(9)\n"
      "v_mfma_f32_16x16x32_bf16 v[12:15], v[208:211], v[144:147], v[12:15]\n"
      "ds_read_b64_tr_b16 v[204:205], v218 offset:24576\n"
      "v_mfma_f32_16x16x32_bf16 v[16:19], v[208:211], v[148:151], v[16:19]\n"
      "ds_read_b64_tr_b16 v[206:207], v218 offset:28672\n"
      "v_mfma_f32_16x16x32_bf16 v[20:23], v[208:211], v[152:155], v[20:23]\n"
      "s_waitcnt lgkmcnt(9)\n"
      "v_mfma_f32_16x16x32_bf16 v[12:15], v[212:215], v[156:159], v[12:15]\n"
      "ds_read_b64_tr_b16 v[208:209], v218 offset:32768\n"
      "v_cvt_pk_bf16_f32 v234, v0, v1\n"
      "v_cvt_pk_bf16_f32 v235, v2, v3\n"
      "v_mfma_f32_16x16x32_bf16 v[16:19], v[212:215], v[160:163], v[16:19]\n"
      "ds_read_b64_tr_b16 v[210:211], v218 offset:36864\n"
      "v_cvt_pk_bf16_f32 v236, v4, v5\n"
      "v_cvt_pk_bf16_f32 v237, v6, v7\n"
      "v_cvt_pk_bf16_f32 v238, v8, v9\n"
      "v_cvt_pk_bf16_f32 v239, v10, v11\n"
      "s_movk_i32 %[t0], 0\n"
      "v_mfma_f32_16x16x32_bf16 v[20:23], v[212:215], v[164:167], v[20:23]\n"
      "buffer_store_dwordx4 v[234:237], v233, %[rs], %[t0] offen sc1\n"
      "buffer_store_dwordx2 v[238:239], v241, %[rs], %[t0] offen offset:1024 sc1\n"
      "s_waitcnt lgkmcnt(8)\n"
      "v_mfma_f32_16x16x32_bf16 v[12:15], v[192:195], v[168:171], v[12:15]\n"
      "ds_read_b64_tr_b16 v[212:213], v218 offset:40960\n"
      "v_mfma_f32_16x16x32_bf16 v[16:19], v[192:195], v[172:175], v[16:19]\n"
      "ds_read_b64_tr_b16 v[214:215], v218 offset:45056\n"
      "v_mfma_f32_16x16x32_bf16 v[20:23], v[192:195], v[176:179], v[20:23]\n"
      "s_waitcnt lgkmcnt(8)\n"
      "v_mfma_f32_16x16x32_bf16 v[12:15], v[196:199], v[180:183], v[12:15]\n"
      "ds_read_b64_tr_b16 v[192:193], v219 offset:16384\n"
      "v_mfma_f32_16x16x32_bf16 v[16:19], v[196:199], v[184:187], v[16:19]\n"
      "ds_read_b64_tr_b16 v[194:195], v219 offset:20480\n"
      "v_mfma_f32_16x16x32_bf16 v[20:23], v[196:199], v[188:191], v[20:23]\n"
      "s_waitcnt lgkmcnt(8)\n"
      "v_mfma_f32_16x16x32_bf16 v[24:27], v[200:203], v[144:147], v[24:27]\n"
      "ds_read_b64_tr_b16 v[196:197], v219 offset:24576\n"
      "v_mfma_f32_16x16x32_bf16 v[28:31], v[200:203], v[148:151], v[28:31]\n"
      "ds_read_b64_tr_b16 v[198:199], v219 offset:28672\n"
      "v_mfma_f32_16x16x32_bf16 v[32:35], v[200:203], v[152:155], v[32:35]\n"
      "s_waitcnt lgkmcnt(8)\n"
      "v_mfma_f32_16x16x32_bf16 v[24:27], v[204:207], v[156:159], v[24:27]\n"
      "ds_read_b64_tr_b16 v[200:201], v219 offset:32768\n"
      "v_cvt_pk_bf16_f32 v244, v12, v13\n"
      "v_cvt_pk_bf16_f32 v245, v14, v15\n"
      "v_mfma_f32_16x16x32_bf16 v[28:31], v[204:207], v[160:163], v[28:31]\n"
      "ds_read_b64_tr_b16 v[202:203], v219 offset:36864\n"
      "v_cvt_pk_bf16_f32 v246, v16, v17\n"
      "v_cvt_pk_bf16_f32 v247, v18, v19\n"
      "v_cvt_pk_bf16_f32 v248, v20, v21\n"
      "v_cvt_pk_bf16_f32 v249, v22, v23\n"
      "s_movk_i32 %[t0], 1536\n"
      "v_mfma_f32_16x16x32_bf16 v[32:35], v[204:207], v[164:167], v[32:35]\n"
      "buffer_store_dwordx4 v[244:247], v233, %[rs], %[t0] offen sc1\n"
      "buffer_store_dwordx2 v[248:249], v241, %[rs], %[t0] offen offset:1024 sc1\n"
      "s_waitcnt lgkmcnt(8)\n"
      "v_mfma_f32_16x16x32_bf16 v[24:27], v[208:211], v[168:171], v[24:27]\n"
      "ds_read_b64_tr_b16 v[204:205], v219 offset:40960\n"
      "v_mfma_f32_16x16x32_bf16 v[28:31], v[208:211], v[172:175], v[28:31]\n"
      "ds_read_b64_tr_b16 v[206:207], v219 offset:45056\n"
      "v_mfma_f32_16x16x32_bf16 v[32:35], v[208:211], v[176:179], v[32:35]\n"
      "s_waitcnt lgkmcnt(8)\n"
      "v_mfma_f32_16x16x32_bf16 v[24:27], v[212:215], v[180:183], v[24:27]\n"
      "ds_read_b64_tr_b16 v[208:209], v220 offset:16384\n"
      "v_mfma_f32_16x16x32_bf16 v[28:31], v[212:215], v[184:187], v[28:31]\n"
      "ds_read_b64_tr_b16 v[210:211], v220 offset:20480\n"
      "v_mfma_f32_16x16x32_bf16 v[32:35], v[212:215], v[188:191], v[32:35]\n"
      "s_waitcnt lgkmcnt(8)\n"
      "v_mfma_f32_16x16x32_bf16 v[36:39], v[192:195], v[144:147], v[36:39]\n"
      "ds_read_b64_tr_b16 v[212:213], v220 offset:24576\n"
      "v_mfma_f32_16x16x32_bf16 v[40:43], v[192:195], v[148:151], v[40:43]\n"
      "ds_read_b64_tr_b16 v[214:215], v220 offset:28672\n"
      "v_mfma_f32_16x16x32_bf16 v[44:47], v[192:195], v[152:155], v[44:47]\n"
      "s_waitcnt lgkmcnt(8)\n"
      "v_mfma_f32_16x16x32_bf16 v[36:39], v[196:199], v[156:159], v[36:39]\n"
      "ds_read_b64_tr_b16 v[192:193], v220 offset:32768\n"
      "v_cvt_pk_bf16_f32 v234, v24, v25\n"
      "v_cvt_pk_bf16_f32 v235, v26, v27\n"
      "v_mfma_f32_16x16x32_bf16 v[40:43], v[196:199], v[160:163], v[40:43]\n"
      "ds_read_b64_tr_b16 v[194:195], v220 offset:36864\n"
      "v_cvt_pk_bf16_f32 v236, v28, v29\n"
      "v_cvt_pk_bf16_f32 v237, v30, v31\n"
      "v_cvt_pk_bf16_f32 v238, v32, v33\n"
      "v_cvt_pk_bf16_f32 v239, v34, v35\n"
      "s_movk_i32 %[t0], 3072\n"
      "v_mfma_f32_16x16x32_bf16 v[44:47], v[196:199], v[164:167], v[44:47]\n"
      "buffer_store_dwordx4 v[234:237], v233, %[rs], %[t0] offen sc1\n"
      "buffer_store_dwordx2 v[238:239], v241, %[rs], %[t0] offen offset:1024 sc1\n"
      "s_waitcnt lgkmcnt(8)\n"
      "v_mfma_f32_16x16x32_bf16 v[36:39], v[200:203], v[168:171], v[36:39]\n"
      "ds_read_b64_tr_b16 v[196:197], v220 offset:40960\n"
      "v_mfma_f32_16x16x32_bf16 v[40:43], v[200:203], v[172:175], v[40:43]\n"
      "ds_read_b64_tr_b16 v[198:199], v220 offset:45056\n"
      "v_mfma_f32_16x16x32_bf16 v[44:47], v[200:203], v[176:179], v[44:47]\n"
      "s_waitcnt lgkmcnt(8)\n"
      "v_mfma_f32_16x16x32_bf16 v[36:39], v[204:207], v[180:183], v[36:39]\n"
      "ds_read_b64_tr_b16 v[200:201], v221 offset:16384\n"
      "v_mfma_f32_16x16x32_bf16 v[40:43], v[204:207], v[184:187], v[40:43]\n"
      "ds_read_b64_tr_b16 v[202:203], v221 offset:20480\n"
      "v_mfma_f32_16x16x32_bf16 v[44:47], v[204:207], v[188:191], v[44:47]\n"
      "s_waitcnt lgkmcnt(8)\n"
      "v_mfma_f32_16x16x32_bf16 v[48:51], v[208:211], v[144:147], v[48:51]\n"
      "ds_read_b64_tr_b16 v[204:205], v221 offset:24576\n"
      "v_mfma_f32_16x16x32_bf16 v[52:55], v[208:211], v[148:151], v[52:55]\n"
      "ds_read_b64_tr_b16 v[206:207], v221 offset:28672\n"
      "v_mfma_f32_16x16x32_bf16 v[56:59], v[208:211], v[152:155], v[56:59]\n"
      "s_waitcnt lgkmcnt(8)\n"
      "v_mfma_f32_16x16x32_bf16 v[48:51], v[212:215], v[156:159], v[48:51]\n"
      "ds_read_b64_tr_b16 v[208:209], v221 offset:32768\n"
      "v_cvt_pk_bf16_f32 v244, v36, v37\n"
      "v_cvt_pk_bf16_f32 v245, v38, v39\n"
      "v_mfma_f32_16x16x32_bf16 v[52:55], v[212:215], v[160:163], v[52:55]\n"
      "ds_read_b64_tr_b16 v[210:211], v221 offset:36864\n"
      "v_cvt_pk_bf16_f32 v246, v40, v41\n"
      "v_cvt_pk_bf16_f32 v247, v42, v43\n"
      "v_cvt_pk_bf16_f32 v248, v44, v45\n"
      "v_cvt_pk_bf16_f32 v249, v46, v47\n"
      "s_movk_i32 %[t0], 4608\n"
      "v_mfma_f32_16x16x32_bf16 v[56:59], v[212:215], v[164:167], v[56:59]\n"
      "buffer_store_dwordx4 v[244:247], v233, %[rs], %[t0] offen sc1\n"
      "buffer_store_dwordx2 v[248:249], v241, %[rs], %[t0] offen offset:1024 sc1\n"
      "s_waitcnt lgkmcnt(8)\n"
      "v_mfma_f32_16x16x32_bf16 v[48:51], v[192:195], v[168:171], v[48:51]\n"
      "ds_read_b64_tr_b16 v[212:213], v221 offset:40960\n"
      "v_mfma_f32_16x16x32_bf16 v[52:55], v[192:195], v[172:175], v[52:55]\n"
      "ds_read_b64_tr_b16 v[214:215], v221 offset:45056\n"
      "v_mfma_f32_16x16x32_bf16 v[56:59], v[192:195], v[176:179], v[56:59]\n"
      "s_waitcnt lgkmcnt(8)\n"
      "v_mfma_f32_16x16x32_bf16 v[48:51], v[196:199], v[180:183], v[48:51]\n"
      "ds_read_b64_tr_b16 v[192:193], v222 offset:16384\n"
      "v_mfma_f32_16x16x32_bf16 v[52:55], v[196:199], v[184:187], v[52:55]\n"
      "ds_read_b64_tr_b16 v[194:195], v222 offset:20480\n"
      "v_mfma_f32_16x16x32_bf16 v[56:59], v[196:199], v[188:191], v[56:59]\n"
      "s_waitcnt lgkmcnt(8)\n"
      "v_mfma_f32_16x16x32_bf16 v[60:63], v[200:203], v[144:147], v[60:63]\n"
      "ds_read_b64_tr_b16 v[196:197], v222 offset:24576\n"
      "v_mfma_f32_16x16x32_bf16 v[64:67], v[200:203], v[148:151], v[64:67]\n"
      "ds_read_b64_tr_b16 v[198:199], v222 offset:28672\n"
      "v_mfma_f32_16x16x32_bf16 v[68:71], v[200:203], v[152:155], v[68:71]\n"
      "s_waitcnt lgkmcnt(8)\n"
      "v_mfma_f32_16x16x32_bf16 v[60:63], v[204:207], v[156:159], v[60:63]\n"
      "ds_read_b64_tr_b16 v[200:201], v222 offset:32768\n"
      "v_cvt_pk_bf16_f32 v234, v48, v49\n"
      "v_cvt_pk_bf16_f32 v235, v50, v51\n"
      "v_mfma_f32_16x16x32_bf16 v[64:67], v[204:207], v[160:163], v[64:67]\n"
      "ds_read_b64_tr_b16 v[202:203], v222 offset:36864\n"
      "v_cvt_pk_bf16_f32 v236, v52, v53\n"
      "v_cvt_pk_bf16_f32 v237, v54, v55\n"
      "v_cvt_pk_bf16_f32 v238, v56, v57\n"
      "v_cvt_pk_bf16_f32 v239, v58, v59\n"
      "s_movk_i32 %[t0], 6144\n"
      "v_mfma_f32_16x16x32_bf16 v[68:71], v[204:207], v[164:167], v[68:71]\n"
      "buffer_store_dwordx4 v[234:237], v233, %[rs], %[t0] offen sc1\n"
      "buffer_store_dwordx2 v[238:239], v241, %[rs], %[t0] offen offset:1024 sc1\n"
      "s_waitcnt lgkmcnt(8)\n"
      "v_mfma_f32_16x16x32_bf16 v[60:63], v[208:211], v[168:171], v[60:63]\n"
      "ds_read_b64_tr_b16 v[204:205], v222 offset:40960\n"
      "v_mfma_f32_16x16x32_bf16 v[64:67], v[208:211], v[172:175], v[64:67]\n"
      "ds_read_b64_tr_b16 v[206:207], v222 offset:45056\n"
      "v_mfma_f32_16x16x32_bf16 v[68:71], v[208:211], v[176:179], v[68:71]\n"
      "s_waitcnt lgkmcnt(8)\n"
      "v_mfma_f32_16x16x32_bf16 v[60:63], v[212:215], v[180:183], v[60:63]\n"
      "ds_read_b64_tr_b16 v[208:209], v223 offset:16384\n"
      "v_mfma_f32_16x16x32_bf16 v[64:67], v[212:215], v[184:187], v[64:67]\n"
      "ds_read_b64_tr_b16 v[210:211], v223 offset:20480\n"
      "v_mfma_f32_16x16x32_bf16 v[68:71], v[212:215], v[188:191], v[68:71]\n"
      "s_waitcnt lgkmcnt(8)\n"
      "v_mfma_f32_16x16x32_bf16 v[72:75], v[192:195], v[144:147], v[72:75]\n"
      "ds_read_b64_tr_b16 v[212:213], v223 offset:24576\n"
      "v_mfma_f32_16x16x32_bf16 v[76:79], v[192:195], v[148:151], v[76:79]\n"
      "ds_read_b64_tr_b16 v[214:215], v223 offset:28672\n"
      "v_mfma_f32_16x16x32_bf16 v[80:83], v[192:195], v[152:155], v[80:83]\n"
      "s_waitcnt lgkmcnt(8)\n"
      "v_mfma_f32_16x16x32_bf16 v[72:75], v[196:199], v[156:159], v[72:75]\n"
      "ds_read_b64_tr_b16 v[192:193], v223 offset:32768\n"
      "v_cvt_pk_bf16_f32 v244, v60, v61\n"
      "v_cvt_pk_bf16_f32 v245, v62, v63\n"
      "v_mfma_f32_16x16x32_bf16 v[76:79], v[196:199], v[160:163], v[76:79]\n"
      "ds_read_b64_tr_b16 v[194:195], v223 offset:36864\n"
      "v_cvt_pk_bf16_f32 v246, v64, v65\n"
      "v_cvt_pk_bf16_f32 v247, v66, v67\n"
      "v_cvt_pk_bf16_f32 v248, v68, v69\n"
      "v_cvt_pk_bf16_f32 v249, v70, v71\n"
      "s_movk_i32 %[t0], 7680\n"
      "v_mfma_f32_16x16x32_bf16 v[80:83], v[196:199], v[164:167], v[80:83]\n"
      "buffer_store_dwordx4 v[244:247], v233, %[rs], %[t0] offen sc1\n"
      "buffer_store_dwordx2 v[248:249], v241, %[rs], %[t0] offen offset:1024 sc1\n"
      "s_waitcnt lgkmcnt(8)\n"
      "v_mfma_f32_16x16x32_bf16 v[72:75], v[200:203], v[168:171], v[72:75]\n"
      "ds_read_b64_tr_b16 v[196:197], v223 offset:40960\n"
      "v_mfma_f32_16x16x32_bf16 v[76:79], v[200:203], v[172:175], v[76:79]\n"
      "ds_read_b64_tr_b16 v[198:199], v223 offset:45056\n"
      "v_mfma_f32_16x16x32_bf16 v[80:83], v[200:203], v[176:179], v[80:83]\n"
      "s_waitcnt lgkmcnt(8)\n"
      "v_mfma_f32_16x16x32_bf16 v[72:75], v[204:207], v[180:183], v[72:75]\n"
      "v_mfma_f32_16x16x32_bf16 v[76:79], v[204:207], v[184:187], v[76:79]\n"
      "v_mfma_f32_16x16x32_bf16 v[80:83], v[204:207], v[188:191], v[80:83]\n"
      "s_waitcnt lgkmcnt(6)\n"
      "v_mfma_f32_16x16x32_bf16 v[84:87], v[208:211], v[144:147], v[84:87]\n"
      "v_mfma_f32_16x16x32_bf16 v[88:91], v[208:211], v[148:151], v[88:91]\n"
      "v_mfma_f32_16x16x32_bf16 v[92:95], v[208:211], v[152:155], v[92:95]\n"
      "s_waitcnt lgkmcnt(4)\n"
      "v_mfma_f32_16x16x32_bf16 v[84:87], v[212:215], v[156:159], v[84:87]\n"
      "v_cvt_pk_bf16_f32 v234, v72, v73\n"
      "v_cvt_pk_bf16_f32 v235, v74, v75\n"
      "v_mfma_f32_16x16x32_bf16 v[88:91], v[212:215], v[160:163], v[88:91]\n"
      "v_cvt_pk_bf16_f32 v236, v76, v77\n"
      "v_cvt_pk_bf16_f32 v237, v78, v79\n"
      "v_cvt_pk_bf16_f32 v238, v80, v81\n"
      "v_cvt_pk_bf16_f32 v239, v82, v83\n"
      "s_movk_i32 %[t0], 9216\n"
      "v_mfma_f32_16x16x32_bf16 v[92:95], v[212:215], v[164:167], v[92:95]\n"
      "buffer_store_dwordx4 v[234:237], v233, %[rs], %[t0] offen sc1\n"
      "buffer_store_dwordx2 v[238:239], v241, %[rs], %[t0] offen offset:1024 sc1\n"
      "s_waitcnt lgkmcnt(2)\n"
      "v_mfma_f32_16x16x32_bf16 v[84:87], v[192:195], v[168:171], v[84:87]\n"
      "v_mfma_f32_16x16x32_bf16 v[88:91], v[192:195], v[172:175], v[88:91]\n"
      "v_mfma_f32_16x16x32_bf16 v[92:95], v[192:195], v[176:179], v[92:95]\n"
      "s_waitcnt lgkmcnt(0)\n"
      "v_mfma_f32_16x16x32_bf16 v[84:87], v[196:199], v[180:183], v[84:87]\n"
      "v_mfma_f32_16x16x32_bf16 v[88:91], v[196:199], v[184:187], v[88:91]\n"
      "v_mfma_f32_16x16x32_bf16 v[92:95], v[196:199], v[188:191], v[92:95]\n"
      "s_nop 15\n"
      "s_nop 7\n"
      "v_cvt_pk_bf16_f32 v244, v84, v85\n"
      "v_cvt_pk_bf16_f32 v245, v86, v87\n"
      "v_cvt_pk_bf16_f32 v246, v88, v89\n"
      "v_cvt_pk_bf16_f32 v247, v90, v91\n"
      "v_cvt_pk_bf16_f32 v248, v92, v93\n"
      "v_cvt_pk_bf16_f32 v249, v94, v95\n"
      "s_movk_i32 %[t0], 10752\n"
      "buffer_store_dwordx4 v[244:247], v233, %[rs], %[t0] offen sc1\n"
      "buffer_store_dwordx2 v[248:249], v241, %[rs], %[t0] offen offset:1024 sc1\n"
      KTS(4)
      "s_waitcnt vmcnt(0)\n"
      "v_mov_b32 v250, %[tl]\n"
      "v_mov_b32 v251, %[th]\n"
      "s_mov_b64 exec, 1\n"
      "buffer_store_dwordx2 v[250:251], off, %[rf], %[fo2] sc1\n"
      "s_mov_b64 exec, -1\n"
      KTS(5)
      KTE
      : "+{v[96:127]}"(oa), "+{v[128:143]}"(oc), "+{s[40:43]}"(r_3), [sn] "=&s"(sn), [hv] "=&s"(hv), [h2] "=&s"(h2), [t0] "=&s"(t0), [t1] "=&s"(t1) ATS_O
      : "{v232}"(vb0), "{v233}"(l16), "{v241}"(l8), "{v242}"(vfo), [jn] "s"(jn), [ro] "s"(r_ol), [rs] "s"(r_s), [rf] "s"(r_f), [fo1] "s"(fo1), [fo2] "s"(fo2),
        [tl] "s"((unsigned)tag), [th] "s"((unsigned)(tag >> 32)), [tg] "s"(tag), [lc] "s"(lbase + CTRL), [lm] "s"(lbase + CTRL + 48 + wid * 4) ATS_I
      : "memory", "scc", "vcc", "v0", "v1", "v2", "v3", "v4", "v5", "v6", "v7", "v8", "v9", "v10", "v11", "v12", "v13", "v14", "v15", "v16", "v17", "v18", "v19", "v20", "v21", "v22", "v23", "v24", "v25", "v26", "v27", "v28", "v29", "v30", "v31", "v32", "v33", "v34", "v35", "v36", "v37", "v38", "v39", "v40", "v41", "v42", "v43", "v44", "v45", "v46", "v47", "v48", "v49", "v50", "v51", "v52", "v53", "v54", "v55", "v56", "v57", "v58", "v59", "v60", "v61", "v62", "v63", "v64", "v65", "v66", "v67", "v68", "v69", "v70", "v71", "v72", "v73", "v74", "v75", "v76", "v77", "v78", "v79", "v80", "v81", "v82", "v83", "v84", "v85", "v86", "v87", "v88", "v89", "v90", "v91", "v92", "v93", "v94", "v95", "v144", "v145", "v146", "v147", "v148", "v149", "v150", "v151", "v152", "v153", "v154", "v155", "v156", "v157", "v158", "v159", "v160", "v161", "v162", "v163", "v164", "v165", "v166", "v167", "v168", "v169", "v170", "v171", "v172", "v173", "v174", "v175", "v176", "v177", "v178", "v179", "v180", "v181", "v182", "v183", "v184", "v185", "v186", "v187", "v188", "v189", "v190", "v191", "v192", "v193", "v194", "v195", "v196", "v197", "v198", "v199", "v200", "v201", "v202", "v203", "v204", "v205", "v206", "v207", "v208", "v209", "v210", "v211", "v212", "v213", "v214", "v215", "v216", "v217", "v218", "v219", "v220", "v221", "v222", "v223", "v224", "v225", "v226", "v227", "v228", "v229", "v230", "v231", "v234", "v235", "v236", "v237", "v238", "v239", "v240", "v243", "v244", "v245", "v246", "v247", "v248", "v249", "v250", "v251", "v252", "v253");
  }
  TS();
#undef TS
  asm volatile(".LendC:\n");
}
extern "C" void kernel_launch(void* const* d_in, const int* in_sizes, int n_in, void* d_out, int out_size, void* d_ws, size_t ws_size,
                              hipStream_t stream) {
  const bf16* xA = (const bf16*)d_in[0];
  const bf16* kA = (const bf16*)d_in[1];
  const bf16* vA = (const bf16*)d_in[2];
  const bf16* wqA = (const bf16*)d_in[3];
  const bf16* woA = (const bf16*)d_in[4];
  const bf16* xB = (const bf16*)d_in[5];
  const bf16* kB = (const bf16*)d_in[6];
  const bf16* vB = (const bf16*)d_in[7];
  const bf16* wqB = (const bf16*)d_in[8];
  const bf16* woB = (const bf16*)d_in[9];
  int dev = 0, cus = 0;
  (void)hipGetDevice(&dev);
  (void)hipDeviceGetAttribute(&cus, hipDeviceAttributeMultiprocessorCount, dev);
  if (cus >= 256) {
    (void)hipFuncSetAttribute((const void*)fused_kernel, hipFuncAttributeMaxDynamicSharedMemorySize, GLDS);
    hipLaunchKernelGGL(fused_kernel, dim3(256), dim3(512), GLDS, stream, xA, kA, vA, wqA, woA, xB, kB, vB, wqB, woB, (char*)d_ws,
                       (bf16*)d_out);
    return;
  }
  bf16* o_ws = (bf16*)d_ws;
  float* slabs = (float*)((char*)d_ws + (size_t)16 * 1024 * 1024);
  (void)hipFuncSetAttribute((const void*)attn_kernel, hipFuncAttributeMaxDynamicSharedMemorySize, NSLOT * SLOT);
  (void)hipFuncSetAttribute((const void*)oproj_kernel, hipFuncAttributeMaxDynamicSharedMemorySize, NSLOT2 * SLOT);
  hipLaunchKernelGGL(attn_kernel, dim3(512), dim3(256), NSLOT * SLOT, stream, xA, kA, vA, wqA, xB, kB, vB, wqB, o_ws);
  hipLaunchKernelGGL(oproj_kernel, dim3(256), dim3(256), NSLOT2 * SLOT, stream, (const bf16*)o_ws, woA, woB, slabs);
  hipLaunchKernelGGL(reduce_kernel, dim3(384), dim3(256), 0, stream, (const float*)slabs, (bf16*)d_out);
}
```
